```python
import torch

def _softmax(x, axis=-1):
    return torch.softmax(x, dim=axis)
def _gelu(x):
    return torch.nn.functional.gelu(x, approximate="tanh")
def _silu(x):
    return torch.nn.functional.silu(x)
def _sigmoid(x):
    return torch.sigmoid(x)
def _layernorm(x, axis=-1, eps=1e-5):
    m = x.mean(dim=axis, keepdim=True)
    v = x.var(dim=axis, keepdim=True, unbiased=False)
    return (x - m) / torch.sqrt(v + eps)
def _scatter_add(tgt, idx, src, axis=0):
    return tgt.clone().index_add_(axis, idx, src)
def _segment_sum(src, seg, n):
    out = torch.zeros((n,) + src.shape[1:], dtype=src.dtype)
    return out.index_add_(0, seg, src)
def _pad(x, pw):
    flat = tuple(v for pair in reversed(pw) for v in pair)
    return torch.nn.functional.pad(x, flat)

kernel_name = 'parallel_sum__linear_kernel_hedgehog__glu_gated_reglu__prompt'

def setup_inputs():

    _w = (lambda a: (lambda t: t.to(torch.bfloat16) if t.is_floating_point() else t)(torch.as_tensor(a)))
    def _A_setup():
        B, S, D, H = 1, 2048, 576, 32
        Dh = D // H
        return {
            "x": _w((lambda *s: torch.randn(*s, dtype=torch.bfloat16) * 0.05)(B, S, D)),
            "wqkv": _w((lambda *s: torch.randn(*s, dtype=torch.bfloat16) * 0.2)(D, 3 * D)),
            "w_phi": _w((lambda *s: torch.randn(*s, dtype=torch.bfloat16) * 0.2)(H, Dh, Dh) * (Dh ** -0.5)),
            "wo": _w((lambda *s: torch.randn(*s, dtype=torch.bfloat16) * 0.05)(D, D)),
        }
    _A_in = _A_setup()

    def _B_setup():
        B, S, D, F = 1, 2048, 576, 4608
        return {
            "x": _w((lambda *s: torch.randn(*s, dtype=torch.bfloat16) * 0.05)(B, S, D)),
            "W_gate": _w((lambda *s: torch.randn(*s, dtype=torch.bfloat16) * 0.4)(D, F) * (2.0 / D) ** 0.5),
            "W_up": _w((lambda *s: torch.randn(*s, dtype=torch.bfloat16) * 0.4)(D, F) * (2.0 / D) ** 0.5),
            "W_down": _w((lambda *s: torch.randn(*s, dtype=torch.bfloat16) * 0.4)(F, D) * (1.0 / F) ** 0.5),
        }
    _B_in = _B_setup()

    return {
        "A_x": _A_in["x"],
        "A_wqkv": _A_in["wqkv"],
        "A_w_phi": _A_in["w_phi"],
        "A_wo": _A_in["wo"],
        "B_x": _B_in["x"],
        "B_W_gate": _B_in["W_gate"],
        "B_W_up": _B_in["W_up"],
        "B_W_down": _B_in["W_down"],
    }

def reference(A_x, A_wqkv, A_w_phi, A_wo, B_x, B_W_gate, B_W_up, B_W_down):
    def _A_ref(x, wqkv, w_phi, wo):
        B, S, D = x.shape
        H = 32; Dh = D // H
        qkv = (lambda x, *d: x.permute(*d))((x @ wqkv).reshape(B, S, 3, H, Dh), 2, 0, 3, 1, 4)
        q, k, v = qkv[0], qkv[1], qkv[2]
        pq = torch.einsum("bhsd,hdf->bhsf", q, w_phi)
        pk = torch.einsum("bhsd,hdf->bhsf", k, w_phi)
        phi_q = torch.concatenate([_softmax(pq, axis=-1), _softmax(-pq, axis=-1)], axis=-1)
        phi_k = torch.concatenate([_softmax(pk, axis=-1), _softmax(-pk, axis=-1)], axis=-1)
        kv = torch.einsum("bhsf,bhsd->bhfd", phi_k, v)
        z  = torch.einsum("bhsf->bhf", phi_k)
        num = torch.einsum("bhqf,bhfd->bhqd", phi_q, kv)
        den = torch.einsum("bhqf,bhf->bhq", phi_q, z)[..., None] + 1e-6
        o = num / den
        return (lambda x, *d: x.permute(*d))(o, 0, 2, 1, 3).reshape(B, S, D) @ wo

    def _B_ref(x, W_gate, W_up, W_down):
        gate = torch.einsum("bsd,df->bsf", x, W_gate)
        up = torch.einsum("bsd,df->bsf", x, W_up)
        gate = gate * (lambda x, dt: x.to(dt))(gate > 0, gate.dtype)
        h = gate * up
        return torch.einsum("bsf,fd->bsd", h, W_down)

    x = A_x
    xn = _layernorm(x, axis=-1)
    return x + _A_ref(xn, A_wqkv, A_w_phi, A_wo) + _B_ref(xn, B_W_gate, B_W_up, B_W_down)
```

```cpp
#include <hip/hip_runtime.h>
#include <hip/hip_bf16.h>
typedef unsigned short u16;
constexpr int S=2048, D=576, H=32, DH=18, F=4608;
__device__ __forceinline__ float bf2f(u16 x){ return __uint_as_float(((unsigned)x)<<16); }
__device__ __forceinline__ u16 f2bf(float x){ unsigned u=__float_as_uint(x); u += 0x7fffu + ((u>>16)&1u); return (u16)(u>>16); }

__global__ void k_ln(const u16* x, u16* xn){
  int row = blockIdx.x; int t = threadIdx.x;
  float v[9]; float s=0;
  for(int i=0;i<9;i++){ v[i]=bf2f(x[row*D+t+64*i]); s+=v[i]; }
  for(int o=32;o>0;o>>=1) s+=__shfl_xor(s,o);
  float m=s/D; float q=0;
  for(int i=0;i<9;i++){ float d=v[i]-m; q+=d*d; }
  for(int o=32;o>0;o>>=1) q+=__shfl_xor(q,o);
  float r=rsqrtf(q/D+1e-5f);
  for(int i=0;i<9;i++) xn[row*D+t+64*i]=f2bf((v[i]-m)*r);
}
__global__ void k_gemm(const u16* A, const u16* B, float* C, int M, int N, int K){
  __shared__ float As[32][33], Bs[32][33];
  int tx=threadIdx.x, ty=threadIdx.y; int r=blockIdx.y*32+ty, c=blockIdx.x*32+tx;
  float acc=0;
  for(int k0=0;k0<K;k0+=32){
    As[ty][tx]=bf2f(A[(size_t)r*K+k0+tx]); Bs[ty][tx]=bf2f(B[(size_t)(k0+ty)*N+c]);
    __syncthreads();
    for(int k=0;k<32;k++) acc+=As[ty][k]*Bs[k][tx];
    __syncthreads();
  }
  C[(size_t)r*N+c]=acc;
}
__global__ void k_h(const float* g, const float* u, u16* h, int n){
  int i=blockIdx.x*256+threadIdx.x; if(i<n){ float a=g[i]; h[i]=f2bf((a>0?a:0.f)*u[i]); }
}
__global__ void k_phi(const float* qkv, const u16* wphi, float* phiq, float* phik){
  int i=blockIdx.x*256+threadIdx.x; if(i>=S*H*2) return;
  int which=i&1; int h=(i>>1)%H; int s=(i>>1)/H;
  const float* q=qkv+(size_t)s*3*D+which*D+h*DH;
  float* o=(which?phik:phiq)+((size_t)s*H+h)*36;
  float mx=-1e30f,mn=1e30f;
  for(int f=0;f<DH;f++){ float a=0; for(int d=0;d<DH;d++) a+=q[d]*bf2f(wphi[(h*DH+d)*DH+f]); o[f]=a; mx=fmaxf(mx,a); mn=fminf(mn,a);}
  float s1=0,s2=0;
  for(int f=0;f<DH;f++){ float a=o[f]; float e1=__expf(a-mx), e2=__expf(mn-a); o[f]=e1; o[18+f]=e2; s1+=e1; s2+=e2; }
  for(int f=0;f<DH;f++){ o[f]/=s1; o[18+f]/=s2; }
}
__global__ void k_kv(const float* qkv, const float* phik, float* kv){
  int h=blockIdx.x; int t=threadIdx.x; if(t>=36*19) return; int f=t/19, d=t%19;
  float a=0;
  for(int s=0;s<S;s++){ float p=phik[((size_t)s*H+h)*36+f]; float v = d<18? qkv[(size_t)s*3*D+2*D+h*DH+d] : 1.f; a+=p*v; }
  kv[(h*36+f)*19+d]=a;
}
__global__ void k_o(const float* phiq, const float* kv, u16* o){
  int i=blockIdx.x*256+threadIdx.x; if(i>=S*D) return; int s=i/D, c=i%D, h=c/DH, d=c%DH;
  const float* p=phiq+((size_t)s*H+h)*36; float n=0,de=0;
  for(int f=0;f<36;f++){ n+=p[f]*kv[(h*36+f)*19+d]; de+=p[f]*kv[(h*36+f)*19+18]; }
  o[i]=f2bf(n/(de+1e-6f));
}
__global__ void k_fin(const u16* x, const float* a, const float* b, u16* out){
  int i=blockIdx.x*256+threadIdx.x; if(i<S*D) out[i]=f2bf(bf2f(x[i])+a[i]+b[i]);
}
extern "C" void kernel_launch(void* const* d_in, const int* in_sizes, int n_in, void* d_out, int out_size, void* d_ws, size_t ws_size, hipStream_t stream){
  const u16* x=(const u16*)d_in[0]; const u16* wqkv=(const u16*)d_in[1]; const u16* wphi=(const u16*)d_in[2]; const u16* wo=(const u16*)d_in[3];
  const u16* wg=(const u16*)d_in[5]; const u16* wu=(const u16*)d_in[6]; const u16* wd=(const u16*)d_in[7];
  char* w=(char*)d_ws; size_t off=0; auto al=[&](size_t n){ void* p=w+off; off+=(n+255)&~(size_t)255; return p; };
  u16* xn=(u16*)al(S*D*2); float* qkv=(float*)al((size_t)S*3*D*4); float* g=(float*)al((size_t)S*F*4); float* u=(float*)al((size_t)S*F*4);
  u16* h=(u16*)al((size_t)S*F*2); float* phiq=(float*)al((size_t)S*H*36*4); float* phik=(float*)al((size_t)S*H*36*4); float* kv=(float*)al(H*36*19*4);
  u16* o=(u16*)al(S*D*2); float* a=(float*)al(S*D*4); float* b=(float*)al(S*D*4);
  k_ln<<<S,64,0,stream>>>(x,xn);
  k_gemm<<<dim3(3*D/32,S/32),dim3(32,32),0,stream>>>(xn,wqkv,qkv,S,3*D,D);
  k_gemm<<<dim3(F/32,S/32),dim3(32,32),0,stream>>>(xn,wg,g,S,F,D);
  k_gemm<<<dim3(F/32,S/32),dim3(32,32),0,stream>>>(xn,wu,u,S,F,D);
  k_h<<<S*F/256,256,0,stream>>>(g,u,h,S*F);
  k_phi<<<S*H*2/256,256,0,stream>>>(qkv,wphi,phiq,phik);
  k_kv<<<H,704,0,stream>>>(qkv,phik,kv);
  k_o<<<S*D/256,256,0,stream>>>(phiq,kv,o);
  k_gemm<<<dim3(D/32,S/32),dim3(32,32),0,stream>>>(o,wo,a,S,D,D);
  k_gemm<<<dim3(D/32,S/32),dim3(32,32),0,stream>>>(h,wd,b,S,D,F);
  k_fin<<<S*D/256,256,0,stream>>>(x,a,b,(u16*)d_out);
}
```

```cpp
#include <hip/hip_runtime.h>
#include <hip/hip_bf16.h>
typedef unsigned short u16;
constexpr int S=2048, D=576, H=32, DH=18, F=4608;
__device__ __forceinline__ float bf2f(u16 x){ return __uint_as_float(((unsigned)x)<<16); }
__device__ __forceinline__ u16 f2bf(float x){ unsigned u=__float_as_uint(x); u += 0x7fffu + ((u>>16)&1u); return (u16)(u>>16); }

__global__ void k_ln(const u16* x, u16* xn){
  int row = blockIdx.x; int t = threadIdx.x;
  float v[9]; float s=0;
  for(int i=0;i<9;i++){ v[i]=bf2f(x[row*D+t+64*i]); s+=v[i]; }
  for(int o=32;o>0;o>>=1) s+=__shfl_xor(s,o);
  float m=s/D; float q=0;
  for(int i=0;i<9;i++){ float d=v[i]-m; q+=d*d; }
  for(int o=32;o>0;o>>=1) q+=__shfl_xor(q,o);
  float r=rsqrtf(q/D+1e-5f);
  for(int i=0;i<9;i++) xn[row*D+t+64*i]=f2bf((v[i]-m)*r);
}
__global__ void k_gemm(const u16* A, const u16* B, float* C, int M, int N, int K){
  __shared__ float As[32][33], Bs[32][33];
  int tx=threadIdx.x, ty=threadIdx.y; int r=blockIdx.y*32+ty, c=blockIdx.x*32+tx;
  float acc=0;
  for(int k0=0;k0<K;k0+=32){
    As[ty][tx]=bf2f(A[(size_t)r*K+k0+tx]); Bs[ty][tx]=bf2f(B[(size_t)(k0+ty)*N+c]);
    __syncthreads();
    for(int k=0;k<32;k++) acc+=As[ty][k]*Bs[k][tx];
    __syncthreads();
  }
  C[(size_t)r*N+c]=acc;
}
__global__ void k_h(const float* g, const float* u, u16* h, int n){
  int i=blockIdx.x*256+threadIdx.x; if(i<n){ float a=g[i]; h[i]=f2bf((a>0?a:0.f)*u[i]); }
}
__global__ void k_phi(const float* qkv, const u16* wphi, float* phiq, float* phik){
  int i=blockIdx.x*256+threadIdx.x; if(i>=S*H*2) return;
  int which=i&1; int h=(i>>1)%H; int s=(i>>1)/H;
  const float* q=qkv+(size_t)s*3*D+which*D+h*DH;
  float* o=(which?phik:phiq)+((size_t)s*H+h)*36;
  float mx=-1e30f,mn=1e30f;
  for(int f=0;f<DH;f++){ float a=0; for(int d=0;d<DH;d++) a+=q[d]*bf2f(wphi[(h*DH+d)*DH+f]); o[f]=a; mx=fmaxf(mx,a); mn=fminf(mn,a);}
  float s1=0,s2=0;
  for(int f=0;f<DH;f++){ float a=o[f]; float e1=__expf(a-mx), e2=__expf(mn-a); o[f]=e1; o[18+f]=e2; s1+=e1; s2+=e2; }
  for(int f=0;f<DH;f++){ o[f]/=s1; o[18+f]/=s2; }
}
__global__ void k_kv(const float* qkv, const float* phik, float* kv){
  int h=blockIdx.x; int t=threadIdx.x; if(t>=36*19) return; int f=t/19, d=t%19;
  float a=0;
  for(int s=0;s<S;s++){ float p=phik[((size_t)s*H+h)*36+f]; float v = d<18? qkv[(size_t)s*3*D+2*D+h*DH+d] : 1.f; a+=p*v; }
  kv[(h*36+f)*19+d]=a;
}
__global__ void k_o(const float* phiq, const float* kv, u16* o){
  int i=blockIdx.x*256+threadIdx.x; if(i>=S*D) return; int s=i/D, c=i%D, h=c/DH, d=c%DH;
  const float* p=phiq+((size_t)s*H+h)*36; float n=0,de=0;
  for(int f=0;f<36;f++){ n+=p[f]*kv[(h*36+f)*19+d]; de+=p[f]*kv[(h*36+f)*19+18]; }
  o[i]=f2bf(n/(de+1e-6f));
}
__global__ void k_fin(const u16* x, const float* a, const float* b, u16* out){
  int i=blockIdx.x*256+threadIdx.x; if(i<S*D) out[i]=f2bf(bf2f(x[i])+a[i]+b[i]);
}
#define AMP 100

typedef unsigned u32x4 __attribute__((ext_vector_type(4)));
template<int MODE>
__global__ __launch_bounds__(256) void k_probe(const char* p0, const char* p1, const char* p2, char* wsp, unsigned* sink, int nb, int np){
  unsigned long long r0=__builtin_amdgcn_s_memrealtime();
  __builtin_amdgcn_s_waitcnt(0);
  u32x4 acc={0,0,0,0};
  if(MODE==0){
    for(int j=0;j<np;j++){ const char* p = j==0?p0:(j==1?p1:p2);
      const u32x4* q=(const u32x4*)(p+(size_t)blockIdx.x*nb);
      #pragma unroll 8
      for(int i=threadIdx.x;i<nb/16;i+=256) acc+=q[i];
    }
  } else if(MODE==1){
    if(threadIdx.x==0) acc[0]=*(const volatile unsigned*)(p0+(size_t)blockIdx.x*nb);
  } else if(MODE==2){
    u32x4* q=(u32x4*)(wsp+(size_t)blockIdx.x*nb);
    u32x4 v={threadIdx.x,1,2,3};
    #pragma unroll 8
    for(int i=threadIdx.x;i<nb/16;i+=256) q[i]=v;
  }
  asm volatile("s_waitcnt vmcnt(0)":"+v"(acc)::"memory");
  __syncthreads();
  unsigned long long r1=__builtin_amdgcn_s_memrealtime();
  unsigned long long dt=r1-r0;
  if(dt>5000) dt=5000;
  while(__builtin_amdgcn_s_memrealtime()-r1 < dt*AMP) __builtin_amdgcn_s_sleep(8);
  if(acc[0]+acc[1]+acc[2]+acc[3]==0x12345677u) sink[threadIdx.x]=1;
}
extern "C" void kernel_launch(void* const* d_in, const int* in_sizes, int n_in, void* d_out, int out_size, void* d_ws, size_t ws_size, hipStream_t stream){
  const u16* x=(const u16*)d_in[0]; const u16* wqkv=(const u16*)d_in[1]; const u16* wphi=(const u16*)d_in[2]; const u16* wo=(const u16*)d_in[3];
  const u16* wg=(const u16*)d_in[5]; const u16* wu=(const u16*)d_in[6]; const u16* wd=(const u16*)d_in[7];
  char* w=(char*)d_ws; size_t off=0; auto al=[&](size_t n){ void* p=w+off; off+=(n+255)&~(size_t)255; return p; };
  u16* xn=(u16*)al(S*D*2); float* qkv=(float*)al((size_t)S*3*D*4); float* g=(float*)al((size_t)S*F*4); float* u=(float*)al((size_t)S*F*4);
  u16* h=(u16*)al((size_t)S*F*2); float* phiq=(float*)al((size_t)S*H*36*4); float* phik=(float*)al((size_t)S*H*36*4); float* kv=(float*)al(H*36*19*4);
  u16* o=(u16*)al(S*D*2); float* a=(float*)al(S*D*4); float* b=(float*)al(S*D*4);
  k_probe<1><<<256,256,0,stream>>>((const char*)wd,(const char*)wu,(const char*)wd,w,(unsigned*)(w+(200<<20)),20736,1);
  k_ln<<<S,64,0,stream>>>(x,xn);
  k_gemm<<<dim3(3*D/32,S/32),dim3(32,32),0,stream>>>(xn,wqkv,qkv,S,3*D,D);
  k_gemm<<<dim3(F/32,S/32),dim3(32,32),0,stream>>>(xn,wg,g,S,F,D);
  k_gemm<<<dim3(F/32,S/32),dim3(32,32),0,stream>>>(xn,wu,u,S,F,D);
  k_h<<<S*F/256,256,0,stream>>>(g,u,h,S*F);
  k_phi<<<S*H*2/256,256,0,stream>>>(qkv,wphi,phiq,phik);
  k_kv<<<H,704,0,stream>>>(qkv,phik,kv);
  k_o<<<S*D/256,256,0,stream>>>(phiq,kv,o);
  k_gemm<<<dim3(D/32,S/32),dim3(32,32),0,stream>>>(o,wo,a,S,D,D);
  k_gemm<<<dim3(D/32,S/32),dim3(32,32),0,stream>>>(h,wd,b,S,D,F);
  k_fin<<<S*D/256,256,0,stream>>>(x,a,b,(u16*)d_out);
}
```

```cpp
#include <hip/hip_runtime.h>
#include <hip/hip_bf16.h>
typedef unsigned short u16;
constexpr int S=2048, D=576, H=32, DH=18, F=4608;
__device__ __forceinline__ float bf2f(u16 x){ return __uint_as_float(((unsigned)x)<<16); }
__device__ __forceinline__ u16 f2bf(float x){ unsigned u=__float_as_uint(x); u += 0x7fffu + ((u>>16)&1u); return (u16)(u>>16); }

__global__ void k_ln(const u16* x, u16* xn){
  int row = blockIdx.x; int t = threadIdx.x;
  float v[9]; float s=0;
  for(int i=0;i<9;i++){ v[i]=bf2f(x[row*D+t+64*i]); s+=v[i]; }
  for(int o=32;o>0;o>>=1) s+=__shfl_xor(s,o);
  float m=s/D; float q=0;
  for(int i=0;i<9;i++){ float d=v[i]-m; q+=d*d; }
  for(int o=32;o>0;o>>=1) q+=__shfl_xor(q,o);
  float r=rsqrtf(q/D+1e-5f);
  for(int i=0;i<9;i++) xn[row*D+t+64*i]=f2bf((v[i]-m)*r);
}
__global__ void k_gemm(const u16* A, const u16* B, float* C, int M, int N, int K){
  __shared__ float As[32][33], Bs[32][33];
  int tx=threadIdx.x, ty=threadIdx.y; int r=blockIdx.y*32+ty, c=blockIdx.x*32+tx;
  float acc=0;
  for(int k0=0;k0<K;k0+=32){
    As[ty][tx]=bf2f(A[(size_t)r*K+k0+tx]); Bs[ty][tx]=bf2f(B[(size_t)(k0+ty)*N+c]);
    __syncthreads();
    for(int k=0;k<32;k++) acc+=As[ty][k]*Bs[k][tx];
    __syncthreads();
  }
  C[(size_t)r*N+c]=acc;
}
__global__ void k_h(const float* g, const float* u, u16* h, int n){
  int i=blockIdx.x*256+threadIdx.x; if(i<n){ float a=g[i]; h[i]=f2bf((a>0?a:0.f)*u[i]); }
}
__global__ void k_phi(const float* qkv, const u16* wphi, float* phiq, float* phik){
  int i=blockIdx.x*256+threadIdx.x; if(i>=S*H*2) return;
  int which=i&1; int h=(i>>1)%H; int s=(i>>1)/H;
  const float* q=qkv+(size_t)s*3*D+which*D+h*DH;
  float* o=(which?phik:phiq)+((size_t)s*H+h)*36;
  float mx=-1e30f,mn=1e30f;
  for(int f=0;f<DH;f++){ float a=0; for(int d=0;d<DH;d++) a+=q[d]*bf2f(wphi[(h*DH+d)*DH+f]); o[f]=a; mx=fmaxf(mx,a); mn=fminf(mn,a);}
  float s1=0,s2=0;
  for(int f=0;f<DH;f++){ float a=o[f]; float e1=__expf(a-mx), e2=__expf(mn-a); o[f]=e1; o[18+f]=e2; s1+=e1; s2+=e2; }
  for(int f=0;f<DH;f++){ o[f]/=s1; o[18+f]/=s2; }
}
__global__ void k_kv(const float* qkv, const float* phik, float* kv){
  int h=blockIdx.x; int t=threadIdx.x; if(t>=36*19) return; int f=t/19, d=t%19;
  float a=0;
  for(int s=0;s<S;s++){ float p=phik[((size_t)s*H+h)*36+f]; float v = d<18? qkv[(size_t)s*3*D+2*D+h*DH+d] : 1.f; a+=p*v; }
  kv[(h*36+f)*19+d]=a;
}
__global__ void k_o(const float* phiq, const float* kv, u16* o){
  int i=blockIdx.x*256+threadIdx.x; if(i>=S*D) return; int s=i/D, c=i%D, h=c/DH, d=c%DH;
  const float* p=phiq+((size_t)s*H+h)*36; float n=0,de=0;
  for(int f=0;f<36;f++){ n+=p[f]*kv[(h*36+f)*19+d]; de+=p[f]*kv[(h*36+f)*19+18]; }
  o[i]=f2bf(n/(de+1e-6f));
}
__global__ void k_fin(const u16* x, const float* a, const float* b, u16* out){
  int i=blockIdx.x*256+threadIdx.x; if(i<S*D) out[i]=f2bf(bf2f(x[i])+a[i]+b[i]);
}

typedef unsigned u32x4 __attribute__((ext_vector_type(4)));
__global__ __launch_bounds__(256) void k_empty(unsigned* p){ if(p==nullptr) p[threadIdx.x]=1; }
__global__ __launch_bounds__(256,1) void k_fat(unsigned* p){ extern __shared__ char sm[];
  asm volatile("v_mov_b32 v255, 0\n v_accvgpr_write_b32 a255, v255" ::: "v255","a255");
  if(p==nullptr) p[threadIdx.x]=sm[threadIdx.x]; }
template<int POL>
__global__ __launch_bounds__(256) void k_wr(char* wsp, int nb){
  auto rsrc=__builtin_amdgcn_make_buffer_rsrc((void*)(wsp+(size_t)blockIdx.x*nb),0,nb,0x00020000);
  u32x4 v={threadIdx.x,1,2,3};
  #pragma unroll 6
  for(int i=threadIdx.x*16;i<nb;i+=4096) __builtin_amdgcn_raw_buffer_store_b128(v,rsrc,i,0,POL);
}
extern "C" void kernel_launch(void* const* d_in, const int* in_sizes, int n_in, void* d_out, int out_size, void* d_ws, size_t ws_size, hipStream_t stream){
  const u16* x=(const u16*)d_in[0]; const u16* wqkv=(const u16*)d_in[1]; const u16* wphi=(const u16*)d_in[2]; const u16* wo=(const u16*)d_in[3];
  const u16* wg=(const u16*)d_in[5]; const u16* wu=(const u16*)d_in[6]; const u16* wd=(const u16*)d_in[7];
  char* w=(char*)d_ws; size_t off=0; auto al=[&](size_t n){ void* p=w+off; off+=(n+255)&~(size_t)255; return p; };
  u16* xn=(u16*)al(S*D*2); float* qkv=(float*)al((size_t)S*3*D*4); float* g=(float*)al((size_t)S*F*4); float* u=(float*)al((size_t)S*F*4);
  u16* h=(u16*)al((size_t)S*F*2); float* phiq=(float*)al((size_t)S*H*36*4); float* phik=(float*)al((size_t)S*H*36*4); float* kv=(float*)al(H*36*19*4);
  u16* o=(u16*)al(S*D*2); float* a=(float*)al(S*D*4); float* b=(float*)al(S*D*4);
  for(int i=0;i<200;i++) k_empty<<<256,256,0,stream>>>((unsigned*)w);
  k_ln<<<S,64,0,stream>>>(x,xn);
  k_gemm<<<dim3(3*D/32,S/32),dim3(32,32),0,stream>>>(xn,wqkv,qkv,S,3*D,D);
  k_gemm<<<dim3(F/32,S/32),dim3(32,32),0,stream>>>(xn,wg,g,S,F,D);
  k_gemm<<<dim3(F/32,S/32),dim3(32,32),0,stream>>>(xn,wu,u,S,F,D);
  k_h<<<S*F/256,256,0,stream>>>(g,u,h,S*F);
  k_phi<<<S*H*2/256,256,0,stream>>>(qkv,wphi,phiq,phik);
  k_kv<<<H,704,0,stream>>>(qkv,phik,kv);
  k_o<<<S*D/256,256,0,stream>>>(phiq,kv,o);
  k_gemm<<<dim3(D/32,S/32),dim3(32,32),0,stream>>>(o,wo,a,S,D,D);
  k_gemm<<<dim3(D/32,S/32),dim3(32,32),0,stream>>>(h,wd,b,S,D,F);
  k_fin<<<S*D/256,256,0,stream>>>(x,a,b,(u16*)d_out);
}
```

```cpp
#include <hip/hip_runtime.h>
#include <hip/hip_bf16.h>
typedef unsigned short u16;
constexpr int S=2048, D=576, H=32, DH=18, F=4608;
__device__ __forceinline__ float bf2f(u16 x){ return __uint_as_float(((unsigned)x)<<16); }
__device__ __forceinline__ u16 f2bf(float x){ unsigned u=__float_as_uint(x); u += 0x7fffu + ((u>>16)&1u); return (u16)(u>>16); }

__global__ void k_ln(const u16* x, u16* xn){
  int row = blockIdx.x; int t = threadIdx.x;
  float v[9]; float s=0;
  for(int i=0;i<9;i++){ v[i]=bf2f(x[row*D+t+64*i]); s+=v[i]; }
  for(int o=32;o>0;o>>=1) s+=__shfl_xor(s,o);
  float m=s/D; float q=0;
  for(int i=0;i<9;i++){ float d=v[i]-m; q+=d*d; }
  for(int o=32;o>0;o>>=1) q+=__shfl_xor(q,o);
  float r=rsqrtf(q/D+1e-5f);
  for(int i=0;i<9;i++) xn[row*D+t+64*i]=f2bf((v[i]-m)*r);
}
__global__ void k_gemm(const u16* A, const u16* B, float* C, int M, int N, int K){
  __shared__ float As[32][33], Bs[32][33];
  int tx=threadIdx.x, ty=threadIdx.y; int r=blockIdx.y*32+ty, c=blockIdx.x*32+tx;
  float acc=0;
  for(int k0=0;k0<K;k0+=32){
    As[ty][tx]=bf2f(A[(size_t)r*K+k0+tx]); Bs[ty][tx]=bf2f(B[(size_t)(k0+ty)*N+c]);
    __syncthreads();
    for(int k=0;k<32;k++) acc+=As[ty][k]*Bs[k][tx];
    __syncthreads();
  }
  C[(size_t)r*N+c]=acc;
}
__global__ void k_h(const float* g, const float* u, u16* h, int n){
  int i=blockIdx.x*256+threadIdx.x; if(i<n){ float a=g[i]; h[i]=f2bf((a>0?a:0.f)*u[i]); }
}
__global__ void k_phi(const float* qkv, const u16* wphi, float* phiq, float* phik){
  int i=blockIdx.x*256+threadIdx.x; if(i>=S*H*2) return;
  int which=i&1; int h=(i>>1)%H; int s=(i>>1)/H;
  const float* q=qkv+(size_t)s*3*D+which*D+h*DH;
  float* o=(which?phik:phiq)+((size_t)s*H+h)*36;
  float mx=-1e30f,mn=1e30f;
  for(int f=0;f<DH;f++){ float a=0; for(int d=0;d<DH;d++) a+=q[d]*bf2f(wphi[(h*DH+d)*DH+f]); o[f]=a; mx=fmaxf(mx,a); mn=fminf(mn,a);}
  float s1=0,s2=0;
  for(int f=0;f<DH;f++){ float a=o[f]; float e1=__expf(a-mx), e2=__expf(mn-a); o[f]=e1; o[18+f]=e2; s1+=e1; s2+=e2; }
  for(int f=0;f<DH;f++){ o[f]/=s1; o[18+f]/=s2; }
}
__global__ void k_kv(const float* qkv, const float* phik, float* kv){
  int h=blockIdx.x; int t=threadIdx.x; if(t>=36*19) return; int f=t/19, d=t%19;
  float a=0;
  for(int s=0;s<S;s++){ float p=phik[((size_t)s*H+h)*36+f]; float v = d<18? qkv[(size_t)s*3*D+2*D+h*DH+d] : 1.f; a+=p*v; }
  kv[(h*36+f)*19+d]=a;
}
__global__ void k_o(const float* phiq, const float* kv, u16* o){
  int i=blockIdx.x*256+threadIdx.x; if(i>=S*D) return; int s=i/D, c=i%D, h=c/DH, d=c%DH;
  const float* p=phiq+((size_t)s*H+h)*36; float n=0,de=0;
  for(int f=0;f<36;f++){ n+=p[f]*kv[(h*36+f)*19+d]; de+=p[f]*kv[(h*36+f)*19+18]; }
  o[i]=f2bf(n/(de+1e-6f));
}
__global__ void k_fin(const u16* x, const float* a, const float* b, u16* out){
  int i=blockIdx.x*256+threadIdx.x; if(i<S*D) out[i]=f2bf(bf2f(x[i])+a[i]+b[i]);
}
#define AMP 50

typedef float f32x4 __attribute__((ext_vector_type(4)));
typedef unsigned u32x4 __attribute__((ext_vector_type(4)));
__global__ __launch_bounds__(256) void k_code(float* sink, int loops){
  f32x4 c0={0,0,0,0},c1=c0,c2=c0,c3=c0; u32x4 a={0,0,0,0};
  unsigned long long r0=__builtin_amdgcn_s_memrealtime();
  __builtin_amdgcn_s_waitcnt(0);
  for(int l=0;l<loops;l++){
  asm volatile(".rept 32\n v_mfma_f32_16x16x32_bf16 %0, %4, %4, %0\n v_mfma_f32_16x16x32_bf16 %1, %4, %4, %1\n v_mfma_f32_16x16x32_bf16 %2, %4, %4, %2\n v_mfma_f32_16x16x32_bf16 %3, %4, %4, %3\n .endr\n s_nop 15" : "+v"(c0),"+v"(c1),"+v"(c2),"+v"(c3) : "v"(a));
  }
  __syncthreads();
  unsigned long long r1=__builtin_amdgcn_s_memrealtime();
  unsigned long long dt=r1-r0;
  if(dt>20000) dt=20000;
  while(__builtin_amdgcn_s_memrealtime()-r1 < dt*AMP) __builtin_amdgcn_s_sleep(8);
  if(c0[0]+c1[1]+c2[2]+c3[3]==1.2345f) sink[threadIdx.x]=1;
}
extern "C" void kernel_launch(void* const* d_in, const int* in_sizes, int n_in, void* d_out, int out_size, void* d_ws, size_t ws_size, hipStream_t stream){
  const u16* x=(const u16*)d_in[0]; const u16* wqkv=(const u16*)d_in[1]; const u16* wphi=(const u16*)d_in[2]; const u16* wo=(const u16*)d_in[3];
  const u16* wg=(const u16*)d_in[5]; const u16* wu=(const u16*)d_in[6]; const u16* wd=(const u16*)d_in[7];
  char* w=(char*)d_ws; size_t off=0; auto al=[&](size_t n){ void* p=w+off; off+=(n+255)&~(size_t)255; return p; };
  u16* xn=(u16*)al(S*D*2); float* qkv=(float*)al((size_t)S*3*D*4); float* g=(float*)al((size_t)S*F*4); float* u=(float*)al((size_t)S*F*4);
  u16* h=(u16*)al((size_t)S*F*2); float* phiq=(float*)al((size_t)S*H*36*4); float* phik=(float*)al((size_t)S*H*36*4); float* kv=(float*)al(H*36*19*4);
  u16* o=(u16*)al(S*D*2); float* a=(float*)al(S*D*4); float* b=(float*)al(S*D*4);
  k_code<<<256,256,0,stream>>>((float*)(w+(200<<20)),1);
  k_ln<<<S,64,0,stream>>>(x,xn);
  k_gemm<<<dim3(3*D/32,S/32),dim3(32,32),0,stream>>>(xn,wqkv,qkv,S,3*D,D);
  k_gemm<<<dim3(F/32,S/32),dim3(32,32),0,stream>>>(xn,wg,g,S,F,D);
  k_gemm<<<dim3(F/32,S/32),dim3(32,32),0,stream>>>(xn,wu,u,S,F,D);
  k_h<<<S*F/256,256,0,stream>>>(g,u,h,S*F);
  k_phi<<<S*H*2/256,256,0,stream>>>(qkv,wphi,phiq,phik);
  k_kv<<<H,704,0,stream>>>(qkv,phik,kv);
  k_o<<<S*D/256,256,0,stream>>>(phiq,kv,o);
  k_gemm<<<dim3(D/32,S/32),dim3(32,32),0,stream>>>(o,wo,a,S,D,D);
  k_gemm<<<dim3(D/32,S/32),dim3(32,32),0,stream>>>(h,wd,b,S,D,F);
  k_fin<<<S*D/256,256,0,stream>>>(x,a,b,(u16*)d_out);
}
```

```cpp
#include <hip/hip_runtime.h>
#include <hip/hip_bf16.h>
typedef unsigned short u16;
constexpr int S=2048, D=576, H=32, DH=18, F=4608;
__device__ __forceinline__ float bf2f(u16 x){ return __uint_as_float(((unsigned)x)<<16); }
__device__ __forceinline__ u16 f2bf(float x){ unsigned u=__float_as_uint(x); u += 0x7fffu + ((u>>16)&1u); return (u16)(u>>16); }

__global__ void k_ln(const u16* x, u16* xn){
  int row = blockIdx.x; int t = threadIdx.x;
  float v[9]; float s=0;
  for(int i=0;i<9;i++){ v[i]=bf2f(x[row*D+t+64*i]); s+=v[i]; }
  for(int o=32;o>0;o>>=1) s+=__shfl_xor(s,o);
  float m=s/D; float q=0;
  for(int i=0;i<9;i++){ float d=v[i]-m; q+=d*d; }
  for(int o=32;o>0;o>>=1) q+=__shfl_xor(q,o);
  float r=rsqrtf(q/D+1e-5f);
  for(int i=0;i<9;i++) xn[row*D+t+64*i]=f2bf((v[i]-m)*r);
}
__global__ void k_gemm(const u16* A, const u16* B, float* C, int M, int N, int K){
  __shared__ float As[32][33], Bs[32][33];
  int tx=threadIdx.x, ty=threadIdx.y; int r=blockIdx.y*32+ty, c=blockIdx.x*32+tx;
  float acc=0;
  for(int k0=0;k0<K;k0+=32){
    As[ty][tx]=bf2f(A[(size_t)r*K+k0+tx]); Bs[ty][tx]=bf2f(B[(size_t)(k0+ty)*N+c]);
    __syncthreads();
    for(int k=0;k<32;k++) acc+=As[ty][k]*Bs[k][tx];
    __syncthreads();
  }
  C[(size_t)r*N+c]=acc;
}
__global__ void k_h(const float* g, const float* u, u16* h, int n){
  int i=blockIdx.x*256+threadIdx.x; if(i<n){ float a=g[i]; h[i]=f2bf((a>0?a:0.f)*u[i]); }
}
__global__ void k_phi(const float* qkv, const u16* wphi, float* phiq, float* phik){
  int i=blockIdx.x*256+threadIdx.x; if(i>=S*H*2) return;
  int which=i&1; int h=(i>>1)%H; int s=(i>>1)/H;
  const float* q=qkv+(size_t)s*3*D+which*D+h*DH;
  float* o=(which?phik:phiq)+((size_t)s*H+h)*36;
  float mx=-1e30f,mn=1e30f;
  for(int f=0;f<DH;f++){ float a=0; for(int d=0;d<DH;d++) a+=q[d]*bf2f(wphi[(h*DH+d)*DH+f]); o[f]=a; mx=fmaxf(mx,a); mn=fminf(mn,a);}
  float s1=0,s2=0;
  for(int f=0;f<DH;f++){ float a=o[f]; float e1=__expf(a-mx), e2=__expf(mn-a); o[f]=e1; o[18+f]=e2; s1+=e1; s2+=e2; }
  for(int f=0;f<DH;f++){ o[f]/=s1; o[18+f]/=s2; }
}
__global__ void k_kv(const float* qkv, const float* phik, float* kv){
  int h=blockIdx.x; int t=threadIdx.x; if(t>=36*19) return; int f=t/19, d=t%19;
  float a=0;
  for(int s=0;s<S;s++){ float p=phik[((size_t)s*H+h)*36+f]; float v = d<18? qkv[(size_t)s*3*D+2*D+h*DH+d] : 1.f; a+=p*v; }
  kv[(h*36+f)*19+d]=a;
}
__global__ void k_o(const float* phiq, const float* kv, u16* o){
  int i=blockIdx.x*256+threadIdx.x; if(i>=S*D) return; int s=i/D, c=i%D, h=c/DH, d=c%DH;
  const float* p=phiq+((size_t)s*H+h)*36; float n=0,de=0;
  for(int f=0;f<36;f++){ n+=p[f]*kv[(h*36+f)*19+d]; de+=p[f]*kv[(h*36+f)*19+18]; }
  o[i]=f2bf(n/(de+1e-6f));
}
__global__ void k_fin(const u16* x, const float* a, const float* b, u16* out){
  int i=blockIdx.x*256+threadIdx.x; if(i<S*D) out[i]=f2bf(bf2f(x[i])+a[i]+b[i]);
}
#define AMP 100

typedef unsigned u32x4 __attribute__((ext_vector_type(4)));
template<int MODE,int POL>
__global__ __launch_bounds__(256) void k_probe(const char* p0, const char* p1, const char* p2, char* wsp, unsigned* sink, int nb, int np){
  __shared__ __attribute__((aligned(16))) char smem[65536];
  int w=__builtin_amdgcn_readfirstlane(threadIdx.x>>6);
  unsigned long long r0=__builtin_amdgcn_s_memrealtime();
  __builtin_amdgcn_s_waitcnt(0);
  if(MODE==0){
    unsigned ldsb=(unsigned)(size_t)smem + w*16384;
    int d=0;
    for(int j=0;j<np;j++){ const char* p = j==0?p0:(j==1?p1:p2);
      auto rsrc=__builtin_amdgcn_make_buffer_rsrc((void*)(p+(size_t)blockIdx.x*nb),0,nb,0x00020000);
      unsigned voff=threadIdx.x*16;
      for(int i=0;i<nb;i+=4096){
        unsigned m0v=ldsb+(d&15)*1024; d++;
        if(POL==0) asm volatile("s_mov_b32 m0, %0\n\ts_nop 0\n\tbuffer_load_dwordx4 %1, %2, %3 offen lds\n\ts_waitcnt vmcnt(15)"::"s"(m0v),"v"(voff),"s"(rsrc),"s"(i):"memory");
        else asm volatile("s_mov_b32 m0, %0\n\ts_nop 0\n\tbuffer_load_dwordx4 %1, %2, %3 offen nt lds\n\ts_waitcnt vmcnt(15)"::"s"(m0v),"v"(voff),"s"(rsrc),"s"(i):"memory");
      }
    }
  } else if(MODE==2){
    auto rsrc=__builtin_amdgcn_make_buffer_rsrc((void*)(wsp+(size_t)blockIdx.x*nb),0,nb,0x00020000);
    u32x4 v={threadIdx.x,1,2,3};
    #pragma unroll 6
    for(int i=threadIdx.x*16;i<nb;i+=4096) __builtin_amdgcn_raw_buffer_store_b128(v,rsrc,i,0,POL);
  }
  asm volatile("s_waitcnt vmcnt(0)":::"memory");
  __syncthreads();
  unsigned long long r1=__builtin_amdgcn_s_memrealtime();
  unsigned long long dt=r1-r0;
  if(dt>5000) dt=5000;
  while(__builtin_amdgcn_s_memrealtime()-r1 < dt*AMP) __builtin_amdgcn_s_sleep(8);
  if(smem[threadIdx.x]==77 && np==1234) sink[threadIdx.x]=1;
}
extern "C" void kernel_launch(void* const* d_in, const int* in_sizes, int n_in, void* d_out, int out_size, void* d_ws, size_t ws_size, hipStream_t stream){
  const u16* x=(const u16*)d_in[0]; const u16* wqkv=(const u16*)d_in[1]; const u16* wphi=(const u16*)d_in[2]; const u16* wo=(const u16*)d_in[3];
  const u16* wg=(const u16*)d_in[5]; const u16* wu=(const u16*)d_in[6]; const u16* wd=(const u16*)d_in[7];
  char* w=(char*)d_ws; size_t off=0; auto al=[&](size_t n){ void* p=w+off; off+=(n+255)&~(size_t)255; return p; };
  u16* xn=(u16*)al(S*D*2); float* qkv=(float*)al((size_t)S*3*D*4); float* g=(float*)al((size_t)S*F*4); float* u=(float*)al((size_t)S*F*4);
  u16* h=(u16*)al((size_t)S*F*2); float* phiq=(float*)al((size_t)S*H*36*4); float* phik=(float*)al((size_t)S*H*36*4); float* kv=(float*)al(H*36*19*4);
  u16* o=(u16*)al(S*D*2); float* a=(float*)al(S*D*4); float* b=(float*)al(S*D*4);
  k_probe<0,0><<<256,256,0,stream>>>((const char*)wg,(const char*)wu,(const char*)wd,w,(unsigned*)(w+(200<<20)),0,0);
  k_ln<<<S,64,0,stream>>>(x,xn);
  k_gemm<<<dim3(3*D/32,S/32),dim3(32,32),0,stream>>>(xn,wqkv,qkv,S,3*D,D);
  k_gemm<<<dim3(F/32,S/32),dim3(32,32),0,stream>>>(xn,wg,g,S,F,D);
  k_gemm<<<dim3(F/32,S/32),dim3(32,32),0,stream>>>(xn,wu,u,S,F,D);
  k_h<<<S*F/256,256,0,stream>>>(g,u,h,S*F);
  k_phi<<<S*H*2/256,256,0,stream>>>(qkv,wphi,phiq,phik);
  k_kv<<<H,704,0,stream>>>(qkv,phik,kv);
  k_o<<<S*D/256,256,0,stream>>>(phiq,kv,o);
  k_gemm<<<dim3(D/32,S/32),dim3(32,32),0,stream>>>(o,wo,a,S,D,D);
  k_gemm<<<dim3(D/32,S/32),dim3(32,32),0,stream>>>(h,wd,b,S,D,F);
  k_fin<<<S*D/256,256,0,stream>>>(x,a,b,(u16*)d_out);
}
```

```cpp
#include <hip/hip_runtime.h>
#include <hip/hip_bf16.h>
__global__ __launch_bounds__(256,1) __attribute__((aligned(4096))) void k_main(const void* x,const void* wqkv,const void* wphi,const void* wo,const void* wg,const void* wu,const void* wd,void* ws,void* out){
  __shared__ __attribute__((aligned(1024))) char smem[163840];
  unsigned wid=__builtin_amdgcn_readfirstlane(threadIdx.x>>6);
  asm volatile(
"Lstart_%=:\n"
"s_load_dwordx16 s[8:23], %[ka], 0\n"
"s_load_dwordx2 s[24:25], %[ka], 64\n"
"s_mov_b32 s60, %[bid]\n"
"s_mov_b32 s61, %[wid]\n"
"v_mbcnt_lo_u32_b32 v221, -1, 0\n"
"v_mbcnt_hi_u32_b32 v221, -1, v221\n"
"s_getpc_b64 s[52:53]\n"
"s_and_b32 s52, s52, 0xfffff000\n"
"s_mov_b32 s54, Lend_%=-Lstart_%=\n"
"s_mov_b32 s55, 0x20000\n"
"v_lshrrev_b32 v80, 4, v221\n"
"v_lshlrev_b32 v80, 7, v80\n"
"s_lshr_b32 s92, s60, 3\n"
"s_lshl2_add_u32 s92, s92, s61\n"
"s_lshl_b32 s92, s92, 9\n"
"buffer_load_dword v80, v80, s[52:55], s92 offen offset:0\n"
"s_and_b32 s62, s60, 3\n"
"s_lshr_b32 s63, s60, 5\n"
"s_and_b32 s67, s60, 4\n"
"s_bfe_u32 s64, s60, 0x20003\n"
"s_or_b32 s64, s64, s67\n"
"s_lshl3_add_u32 s65, s62, s64\n"
"s_lshr_b32 s66, s64, 2\n"
"s_lshl1_add_u32 s66, s63, s66\n"
"s_lshl2_add_u32 s66, s66, s62\n"
"s_and_b32 s67, s64, 3\n"
"s_lshl2_add_u32 s66, s66, s67\n"
"s_mul_i32 s66, s66, 9216\n"
"s_mul_i32 s67, s61, 384\n"
"s_add_u32 s67, s67, s66\n"
"s_cmp_eq_u32 s61, 3\n"
"s_cselect_b32 s93, 0x40000000, 0\n"
"v_and_b32 v70, 7, v221\n"
"v_lshrrev_b32 v71, 3, v221\n"
"v_mul_u32_u24 v72, 1152, v70\n"
"v_add_u32 v72, s93, v72\n"
"v_xor_b32 v74, v70, v71\n"
"v_lshlrev_b32 v74, 4, v74\n"
"v_mul_u32_u24 v73, 1152, v71\n"
"v_add3_u32 v73, v73, v74, s93\n"
"s_mul_i32 s68, s61, 3072\n"
"s_add_u32 s68, s68, 92160\n"
"s_mov_b32 m0, s68\n"
"s_waitcnt lgkmcnt(0)\n"
"s_mov_b64 s[48:49], s[8:9]\n"
"s_mov_b32 s50, 2359296\n"
"s_mov_b32 s51, 0x20000\n"
"buffer_load_dwordx4 v73, s[48:51], s67 offen offset:0 lds\n"
"s_add_u32 m0, m0, 896\n"
"s_nop 0\n"
"buffer_load_dwordx4 v73, s[48:51], s67 offen offset:128 lds\n"
"s_add_u32 m0, m0, 896\n"
"s_nop 0\n"
"buffer_load_dwordx4 v73, s[48:51], s67 offen offset:256 lds\n"
"buffer_load_ushort v20, v72, s[48:51], s66 offen\n"
"v_lshlrev_b32 v212, 4, v221\n"
"v_lshrrev_b32 v77, 4, v221\n"
"v_lshl_add_u32 v74, v70, 7, v74\n"
"v_add_u32 v74, s68, v74\n"
"s_mov_b64 s[36:37], s[22:23]\n"
"s_mov_b32 s38, 0x2a81000\n"
"s_mov_b32 s39, 0x20000\n"
"s_mov_b32 s66, 44564480\n"
"buffer_load_dword v78, off, s[36:39], s66 sc1\n"
"s_mov_b64 s[40:41], s[16:17]\n"
"s_mov_b32 s42, 5308416\n"
"s_mov_b32 s43, 0x20000\n"
"s_sub_u32 s44, s18, 1024\n"
"s_subb_u32 s45, s19, 0\n"
"s_mov_b32 s46, 5309440\n"
"s_mov_b32 s47, 0x20000\n"
"s_lshr_b32 s73, s60, 3\n"
"s_lshr_b32 s74, s65, 2\n"
"s_bitcmp1_b32 s61, 0\n"
"s_cselect_b64 s[56:57], s[18:19], s[16:17]\n"
"s_mov_b64 s[58:59], s[42:43]\n"
"v_min_u32 v215, 35, v221\n"
"v_lshrrev_b32 v216, 2, v215\n"
"v_mul_u32_u24 v216, 9216, v216\n"
"v_and_b32 v215, 3, v215\n"
"v_lshl_add_u32 v79, v215, 8, v216\n"
"s_lshr_b32 s66, s61, 1\n"
"s_lshl1_add_u32 s66, s73, s66\n"
"s_lshl_b32 s69, s74, 10\n"
"s_mul_i32 s66, s66, 82944\n"
"s_add_u32 s91, s66, s69\n"
"s_mul_i32 s68, s74, 896\n"
"s_sub_u32 s68, 7808, s68\n"
"v_cmp_eq_u32 vcc, 1, v215\n"
"v_add_u32 v216, s68, v79\n"
"s_nop 0\n"
"v_cndmask_b32 v79, v79, v216, vcc\n"
"s_mov_b32 s31, 0x20000\n"
"s_and_b32 s67, s65, 3\n"
"s_cmp_eq_u32 s67, 1\n"
"s_cselect_b32 s68, s68, 0\n"
"s_lshl_b32 s67, s67, 8\n"
"s_add_u32 s72, s69, s67\n"
"s_add_u32 s72, s72, s68\n"
"s_lshr_b32 s66, s64, 2\n"
"s_and_b32 s67, s62, 1\n"
"s_lshl1_add_u32 s66, s66, s67\n"
"s_lshl3_add_u32 s97, s66, 0x80000\n"
"s_bfe_u32 s66, 0x40241c00, s97\n"
"s_bfe_u32 s67, 0x81c081c, s97\n"
"s_add_u32 s66, s66, s73\n"
"s_cmp_lt_u32 s73, s67\n"
"s_cselect_b32 s68, 0, 0x40000000\n"
"s_lshr_b32 s75, s66, 2\n"
"s_and_b32 s66, s66, 3\n"
"s_and_b32 s67, s62, 2\n"
"s_lshl1_add_u32 s74, s67, s66\n"
"s_lshl_b32 s69, s74, 10\n"
"s_lshl2_add_u32 s80, s74, s61\n"
"s_mul_i32 s66, s80, 648\n"
"s_add_u32 s28, s12, s66\n"
"s_addc_u32 s29, s13, 0\n"
"s_mov_b32 s30, 648\n"
"s_lshl_b32 s70, s61, 11\n"
"s_add_u32 m0, s70, 149504\n"
"s_nop 0\n"
"buffer_load_dwordx4 v212, s[28:31], 0 offen offset:0 lds\n"
"buffer_load_dwordx4 v212, s[28:31], 0 offen offset:1024 lds\n"
"v_mul_u32_u24 v189, 9216, v71\n"
"v_and_b32 v216, 112, v212\n"
"v_add3_u32 v189, v189, v216, s68\n"
"s_lshl2_add_u32 s66, s75, s61\n"
"s_mul_i32 s67, s66, 73728\n"
"s_add_u32 s87, s69, s67\n"
"s_mov_b64 s[28:29], s[10:11]\n"
"s_mov_b32 s30, 1990656\n"
"s_mov_b32 s31, 0x20000\n"
"v_mul_u32_u24 v188, 3456, v77\n"
"v_and_b32 v216, 240, v212\n"
"v_add3_u32 v188, v188, v216, s68\n"
"s_mul_i32 s66, s66, 27648\n"
"s_lshl_b32 s67, s74, 7\n"
"s_add_u32 s88, s66, s67\n"
"s_add_u32 s89, s88, 13824\n"
"s_mul_i32 s67, s61, 36\n"
"s_lshl4_add_u32 s86, s74, s67\n"
"s_add_u32 s86, s86, 108544\n"
"v_add_u32 v176, s68, v212\n"
"s_mul_i32 s68, s80, 114688\n"
"s_add_u32 s68, s68, 2359296\n"
"s_lshl_b32 s66, s75, 12\n"
"s_add_u32 s78, s68, s66\n"
"s_lshr_b32 s66, s66, 1\n"
"s_add_u32 s79, s68, s66\n"
"s_add_u32 s79, s79, 73728\n"
"s_lshl_b32 s66, s80, 5\n"
"s_add_u32 s66, s75, s66\n"
"s_lshl_b32 s66, s66, 7\n"
"s_add_u32 s80, s66, 44171264\n"
"buffer_load_dword v81, v79, s[56:59], s91 offen offset:128\n"
"v_bfe_u32 v215, v221, 4, 1\n"
"s_and_b32 s66, s61, 1\n"
"s_lshl_b32 s66, s66, 2\n"
"v_lshl_add_u32 v215, v215, 1, s66\n"
"v_xor_b32 v215, v215, v70\n"
"v_lshlrev_b32 v220, 2, v70\n"
"s_lshl_b32 s66, s61, 3\n"
"v_add_u32 v222, s66, v71\n"
"v_mul_u32_u24 v222, 9216, v222\n"
"v_lshl_add_u32 v213, v215, 4, v222\n"
"s_lshl2_add_u32 s68, s63, s61\n"
"s_mul_i32 s66, s68, 73728\n"
"s_lshr_b32 s67, s62, 1\n"
"s_lshl_b32 s67, s67, 10\n"
"s_add_u32 s73, s66, s67\n"
"s_sub_u32 s74, s66, s67\n"
"s_add_u32 s74, s74, 1024\n"
"s_lshl_b32 s68, s68, 12\n"
"s_add_u32 s96, s68, 44433408\n"
"s_mov_b32 s76, 2000000\n"
"s_mov_b32 s77, 100000000\n"
"v_lshlrev_b32 v215, 11, v77\n"
"v_bfe_u32 v216, v221, 2, 2\n"
"v_lshl_add_u32 v215, v216, 7, v215\n"
"v_and_b32 v216, 3, v221\n"
"v_lshl_add_u32 v215, v216, 3, v215\n"
"v_bfe_u32 v216, v221, 3, 2\n"
"v_xor_b32 v217, 0, v216\n"
"v_lshl_add_u32 v208, v217, 5, v215\n"
"v_xor_b32 v217, 1, v216\n"
"v_lshl_add_u32 v209, v217, 5, v215\n"
"v_xor_b32 v217, 2, v216\n"
"v_lshl_add_u32 v210, v217, 5, v215\n"
"v_xor_b32 v217, 3, v216\n"
"v_lshl_add_u32 v211, v217, 5, v215\n"
"v_bfe_u32 v60, v221, 1, 2\n"
"v_min_u32 v60, 2, v60\n"
"v_lshl_add_u32 v60, v71, 2, v60\n"
"v_lshlrev_b32 v60, 7, v60\n"
"v_mov_b32 v222, v212\n"
"v_lshrrev_b32 v216, 2, v71\n"
"v_lshl_add_u32 v126, v216, 12, s93\n"
"v_and_b32 v216, 3, v71\n"
"v_lshl_add_u32 v126, v216, 8, v126\n"
"v_lshl_add_u32 v126, v70, 4, v126\n"
"v_lshlrev_b32 v123, 5, v70\n"
"v_add_u32 v123, 157696, v123\n"
"v_and_b32 v216, 16, v221\n"
"s_lshl_b32 s68, s61, 2\n"
"v_add3_u32 v127, v123, v216, s68\n"
"v_mov_b32 v196, 0\n"
"v_mov_b32 v197, 0\n"
"v_lshlrev_b32 v214, 3, v221\n"
"s_lshl_b32 s66, s61, 9\n"
"v_add_u32 v214, s66, v214\n"
"ds_write_b64 v214, v[196:197] offset:0\n"
"v_and_b32 v214, 1, v221\n"
"v_lshl_add_u32 v214, v214, 4, s68\n"
"s_lshl2_add_u32 s66, s63, s62\n"
"s_mul_i32 s67, s66, 73728\n"
"s_mul_i32 s68, s61, 24576\n"
"s_add_u32 s67, s67, s68\n"
"s_lshr_b32 s68, s64, 1\n"
"s_lshl_b32 s68, s68, 10\n"
"s_add_u32 s67, s67, s68\n"
"s_and_b32 s68, s64, 1\n"
"s_lshl_b32 s68, s68, 7\n"
"s_add_u32 s67, s67, s68\n"
"s_lshl3_add_u32 s66, s66, s64\n"
"s_lshl2_add_u32 s66, s66, s61\n"
"s_lshl_b32 s66, s66, 7\n"
"s_add_u32 s90, s66, 44433408\n"
"v_bfe_u32 v121, v212, 2, 7\n"
"v_add_u32 v121, s93, v121\n"
"s_mul_i32 s85, s61, 8224\n"
"s_add_u32 s85, s85, 108544\n"
"s_lshl_b32 s68, s61, 5\n"
"s_add_u32 s68, s68, 108544\n"
"v_bfe_u32 v187, v221, 2, 2\n"
"v_mul_u32_u24 v187, 8224, v187\n"
"v_and_b32 v216, 3, v221\n"
"v_lshl_add_u32 v186, v216, 8, v187\n"
"v_mul_u32_u24 v187, 8224, v77\n"
"v_lshl_add_u32 v187, v216, 3, v187\n"
"v_bfe_u32 v216, v221, 2, 2\n"
"v_lshlrev_b32 v216, 7, v216\n"
"v_add3_u32 v187, v187, v216, s68\n"
"v_min_u32 v216, 2, v77\n"
"v_lshlrev_b32 v216, 4, v216\n"
"v_add3_u32 v186, v186, v216, s86\n"
"v_add_u32 v216, 16, v221\n"
"v_lshrrev_b32 v216, 6, v216\n"
"v_lshl_add_u32 v186, v216, 11, v186\n"
"s_mov_b64 exec, 0\n"
"s_cmp_eq_u32 s61, 0\n"
"s_cbranch_scc1 Lpad0_%=\n"
"s_cmp_eq_u32 s61, 1\n"
"s_cbranch_scc1 Lpad1_%=\n"
"s_cmp_eq_u32 s61, 2\n"
"s_cbranch_scc1 Lpad2_%=\n"
"s_cmp_eq_u32 s61, 3\n"
"s_cbranch_scc1 Lpad3_%=\n"
"Lret_%=:\n"
"s_mov_b64 exec, -1\n"
"s_waitcnt vmcnt(7)\n"
"ds_read_b128 v[0:3], v74 offset:0\n"
"s_waitcnt vmcnt(6)\n"
"ds_read_b128 v[4:7], v74 offset:1024\n"
"s_waitcnt vmcnt(5)\n"
"ds_read_b128 v[8:11], v74 offset:2048\n"
"s_waitcnt vmcnt(4)\n"
"v_lshlrev_b32 v20, 16, v20\n"
"v_mov_b32 v21, v20\n"
"s_waitcnt lgkmcnt(2)\n"
"v_lshlrev_b32 v24, 16, v0\n"
"v_and_b32 v25, 0xffff0000, v0\n"
"v_pk_add_f32 v[24:25], v[24:25], v[20:21] neg_lo:[0,1] neg_hi:[0,1]\n"
"v_pk_mul_f32 v[66:67], v[24:25], v[24:25]\n"
"v_lshlrev_b32 v26, 16, v1\n"
"v_and_b32 v27, 0xffff0000, v1\n"
"v_pk_add_f32 v[26:27], v[26:27], v[20:21] neg_lo:[0,1] neg_hi:[0,1]\n"
"v_pk_add_f32 v[64:65], v[24:25], v[26:27]\n"
"v_pk_fma_f32 v[66:67], v[26:27], v[26:27], v[66:67]\n"
"v_lshlrev_b32 v28, 16, v2\n"
"v_and_b32 v29, 0xffff0000, v2\n"
"v_pk_add_f32 v[28:29], v[28:29], v[20:21] neg_lo:[0,1] neg_hi:[0,1]\n"
"v_pk_add_f32 v[64:65], v[64:65], v[28:29]\n"
"v_pk_fma_f32 v[66:67], v[28:29], v[28:29], v[66:67]\n"
"v_lshlrev_b32 v30, 16, v3\n"
"v_and_b32 v31, 0xffff0000, v3\n"
"v_pk_add_f32 v[30:31], v[30:31], v[20:21] neg_lo:[0,1] neg_hi:[0,1]\n"
"v_pk_add_f32 v[64:65], v[64:65], v[30:31]\n"
"v_pk_fma_f32 v[66:67], v[30:31], v[30:31], v[66:67]\n"
"s_waitcnt lgkmcnt(1)\n"
"v_lshlrev_b32 v32, 16, v4\n"
"v_and_b32 v33, 0xffff0000, v4\n"
"v_pk_add_f32 v[32:33], v[32:33], v[20:21] neg_lo:[0,1] neg_hi:[0,1]\n"
"v_pk_add_f32 v[64:65], v[64:65], v[32:33]\n"
"v_pk_fma_f32 v[66:67], v[32:33], v[32:33], v[66:67]\n"
"v_lshlrev_b32 v34, 16, v5\n"
"v_and_b32 v35, 0xffff0000, v5\n"
"v_pk_add_f32 v[34:35], v[34:35], v[20:21] neg_lo:[0,1] neg_hi:[0,1]\n"
"v_pk_add_f32 v[64:65], v[64:65], v[34:35]\n"
"v_pk_fma_f32 v[66:67], v[34:35], v[34:35], v[66:67]\n"
"v_lshlrev_b32 v36, 16, v6\n"
"v_and_b32 v37, 0xffff0000, v6\n"
"v_pk_add_f32 v[36:37], v[36:37], v[20:21] neg_lo:[0,1] neg_hi:[0,1]\n"
"v_pk_add_f32 v[64:65], v[64:65], v[36:37]\n"
"v_pk_fma_f32 v[66:67], v[36:37], v[36:37], v[66:67]\n"
"v_lshlrev_b32 v38, 16, v7\n"
"v_and_b32 v39, 0xffff0000, v7\n"
"v_pk_add_f32 v[38:39], v[38:39], v[20:21] neg_lo:[0,1] neg_hi:[0,1]\n"
"v_pk_add_f32 v[64:65], v[64:65], v[38:39]\n"
"v_pk_fma_f32 v[66:67], v[38:39], v[38:39], v[66:67]\n"
"s_waitcnt lgkmcnt(0)\n"
"v_lshlrev_b32 v40, 16, v8\n"
"v_and_b32 v41, 0xffff0000, v8\n"
"v_pk_add_f32 v[40:41], v[40:41], v[20:21] neg_lo:[0,1] neg_hi:[0,1]\n"
"v_pk_add_f32 v[64:65], v[64:65], v[40:41]\n"
"v_pk_fma_f32 v[66:67], v[40:41], v[40:41], v[66:67]\n"
"v_lshlrev_b32 v42, 16, v9\n"
"v_and_b32 v43, 0xffff0000, v9\n"
"v_pk_add_f32 v[42:43], v[42:43], v[20:21] neg_lo:[0,1] neg_hi:[0,1]\n"
"v_pk_add_f32 v[64:65], v[64:65], v[42:43]\n"
"v_pk_fma_f32 v[66:67], v[42:43], v[42:43], v[66:67]\n"
"v_lshlrev_b32 v44, 16, v10\n"
"v_and_b32 v45, 0xffff0000, v10\n"
"v_pk_add_f32 v[44:45], v[44:45], v[20:21] neg_lo:[0,1] neg_hi:[0,1]\n"
"v_pk_add_f32 v[64:65], v[64:65], v[44:45]\n"
"v_pk_fma_f32 v[66:67], v[44:45], v[44:45], v[66:67]\n"
"v_lshlrev_b32 v46, 16, v11\n"
"v_and_b32 v47, 0xffff0000, v11\n"
"v_pk_add_f32 v[46:47], v[46:47], v[20:21] neg_lo:[0,1] neg_hi:[0,1]\n"
"v_pk_add_f32 v[64:65], v[64:65], v[46:47]\n"
"v_pk_fma_f32 v[66:67], v[46:47], v[46:47], v[66:67]\n"
"v_add_f32 v64, v64, v65\n"
"v_add_f32 v66, v66, v67\n"
"s_nop 0\n"
"v_add_f32_dpp v64, v64, v64 row_ror:8 row_mask:0xf bank_mask:0xf\n"
"v_add_f32_dpp v66, v66, v66 row_ror:8 row_mask:0xf bank_mask:0xf\n"
"s_nop 1\n"
"v_permlane16_swap_b32 v64, v66\n"
"v_add_f32 v64, v64, v66\n"
"v_mov_b32 v66, v64\n"
"s_nop 1\n"
"v_permlane32_swap_b32 v64, v66\n"
"v_add_f32 v64, v64, v66\n"
"ds_write_b32 v127, v64 offset:0\n"
"s_waitcnt lgkmcnt(0)\n"
"s_barrier\n"
"ds_read_b128 v[12:15], v123 offset:0\n"
"ds_read_b128 v[16:19], v123 offset:16\n"
"s_waitcnt lgkmcnt(1)\n"
"v_add_f32 v64, v12, v13\n"
"s_waitcnt lgkmcnt(0)\n"
"v_add_f32 v66, v16, v17\n"
"v_add_f32 v64, v64, v14\n"
"v_add_f32 v66, v66, v18\n"
"v_mul_f32 v64, 0x3ae38e39, v64\n"
"v_mul_f32 v66, 0x3ae38e39, v66\n"
"v_fma_f32 v66, -v64, v64, v66\n"
"v_max_f32 v66, 0, v66\n"
"v_add_f32 v66, 0x3727c5ac, v66\n"
"v_rsq_f32 v66, v66\n"
"s_nop 0\n"
"v_mul_f32 v64, v64, v66\n"
"v_mov_b32 v67, v66\n"
"v_mov_b32 v65, v64\n"
"v_pk_fma_f32 v[24:25], v[24:25], v[66:67], v[64:65] neg_lo:[0,0,1] neg_hi:[0,0,1]\n"
"v_pk_fma_f32 v[26:27], v[26:27], v[66:67], v[64:65] neg_lo:[0,0,1] neg_hi:[0,0,1]\n"
"v_pk_fma_f32 v[28:29], v[28:29], v[66:67], v[64:65] neg_lo:[0,0,1] neg_hi:[0,0,1]\n"
"v_pk_fma_f32 v[30:31], v[30:31], v[66:67], v[64:65] neg_lo:[0,0,1] neg_hi:[0,0,1]\n"
"v_cvt_pk_bf16_f32 v0, v24, v25\n"
"v_cvt_pk_bf16_f32 v1, v26, v27\n"
"v_cvt_pk_bf16_f32 v2, v28, v29\n"
"v_cvt_pk_bf16_f32 v3, v30, v31\n"
"buffer_store_dwordx4 v[0:3], v126, s[36:39], s67 offen offset:0 sc1\n"
"s_add_u32 s67, s67, 8192\n"
"v_pk_fma_f32 v[32:33], v[32:33], v[66:67], v[64:65] neg_lo:[0,0,1] neg_hi:[0,0,1]\n"
"v_pk_fma_f32 v[34:35], v[34:35], v[66:67], v[64:65] neg_lo:[0,0,1] neg_hi:[0,0,1]\n"
"v_pk_fma_f32 v[36:37], v[36:37], v[66:67], v[64:65] neg_lo:[0,0,1] neg_hi:[0,0,1]\n"
"v_pk_fma_f32 v[38:39], v[38:39], v[66:67], v[64:65] neg_lo:[0,0,1] neg_hi:[0,0,1]\n"
"v_cvt_pk_bf16_f32 v4, v32, v33\n"
"v_cvt_pk_bf16_f32 v5, v34, v35\n"
"v_cvt_pk_bf16_f32 v6, v36, v37\n"
"v_cvt_pk_bf16_f32 v7, v38, v39\n"
"buffer_store_dwordx4 v[4:7], v126, s[36:39], s67 offen offset:0 sc1\n"
"s_add_u32 s67, s67, 8192\n"
"v_pk_fma_f32 v[40:41], v[40:41], v[66:67], v[64:65] neg_lo:[0,0,1] neg_hi:[0,0,1]\n"
"v_pk_fma_f32 v[42:43], v[42:43], v[66:67], v[64:65] neg_lo:[0,0,1] neg_hi:[0,0,1]\n"
"v_pk_fma_f32 v[44:45], v[44:45], v[66:67], v[64:65] neg_lo:[0,0,1] neg_hi:[0,0,1]\n"
"v_pk_fma_f32 v[46:47], v[46:47], v[66:67], v[64:65] neg_lo:[0,0,1] neg_hi:[0,0,1]\n"
"v_cvt_pk_bf16_f32 v8, v40, v41\n"
"v_cvt_pk_bf16_f32 v9, v42, v43\n"
"v_cvt_pk_bf16_f32 v10, v44, v45\n"
"v_cvt_pk_bf16_f32 v11, v46, v47\n"
"buffer_store_dwordx4 v[8:11], v126, s[36:39], s67 offen offset:0 sc1\n"
"s_waitcnt vmcnt(6)\n"
"v_readfirstlane_b32 s75, v78\n"
"s_nop 1\n"
"s_mul_i32 s75, s75, 0x9e3779b1\n"
"s_add_u32 s75, s75, 0x7f4a7c15\n"
"s_cmp_eq_u32 s75, 0\n"
"s_cselect_b32 s75, 0x7f4a7c15, s75\n"
"v_mov_b32 v219, s75\n"
"v_bfe_u32 v217, v221, 1, 3\n"
"v_lshlrev_b32 v218, 2, v77\n"
"v_sub_u32 v217, v217, v218\n"
"v_and_b32 v218, 1, v221\n"
"v_lshlrev_b32 v218, 4, v218\n"
"v_mov_b32 v205, 0x3f80\n"
"v_lshlrev_b32 v218, v218, v205\n"
"v_cmp_eq_u32 vcc, 0, v217\n"
"s_nop 1\n"
"v_cndmask_b32 v248, 0, v218, vcc\n"
"v_cmp_eq_u32 vcc, 1, v217\n"
"s_nop 1\n"
"v_cndmask_b32 v249, 0, v218, vcc\n"
"v_cmp_eq_u32 vcc, 2, v217\n"
"s_nop 1\n"
"v_cndmask_b32 v250, 0, v218, vcc\n"
"v_cmp_eq_u32 vcc, 3, v217\n"
"s_nop 1\n"
"v_cndmask_b32 v251, 0, v218, vcc\n"
"v_mul_u32_u24 v215, 288, v77\n"
"v_bfe_u32 v216, v221, 2, 1\n"
"v_mul_u32_u24 v216, 18, v216\n"
"v_and_b32 v217, 3, v221\n"
"v_lshl_add_u32 v216, v217, 1, v216\n"
"s_add_u32 s67, s70, 149504\n"
"s_add_u32 s66, s67, 1280\n"
"v_add3_u32 v215, v215, v216, s67\n"
"v_cmp_eq_u32 vcc, 0, v217\n"
"v_mov_b32 v216, s66\n"
"v_mov_b32 v177, 0x3fb8aa3b\n"
"v_cndmask_b32 v216, v216, v215, vcc\n"
"v_cndmask_b32 v177, 1.0, v177, vcc\n"
"s_waitcnt vmcnt(0)\n"
"buffer_store_dword v219, v121, s[36:39], s90 offen offset:0 sc1\n"
"s_add_u32 m0, s70, 2048\n"
"s_add_u32 s71, s72, 128\n"
"buffer_load_dwordx4 v213, s[40:43], s71 offen offset:0 lds\n"
"buffer_load_dwordx4 v213, s[44:47], s71 offen offset:1024 lds\n"
"s_add_u32 m0, s70, 10240\n"
"s_add_u32 s71, s72, 295040\n"
"buffer_load_dwordx4 v213, s[40:43], s71 offen offset:0 lds\n"
"buffer_load_dwordx4 v213, s[44:47], s71 offen offset:1024 lds\n"
"s_add_u32 m0, s70, 18432\n"
"s_add_u32 s71, s72, 589952\n"
"buffer_load_dwordx4 v213, s[40:43], s71 offen offset:0 lds\n"
"buffer_load_dwordx4 v213, s[44:47], s71 offen offset:1024 lds\n"
"s_add_u32 m0, s70, 26624\n"
"s_add_u32 s71, s72, 884864\n"
"buffer_load_dwordx4 v213, s[40:43], s71 offen offset:0 lds\n"
"buffer_load_dwordx4 v213, s[44:47], s71 offen offset:1024 lds\n"
"s_add_u32 m0, s70, 34816\n"
"s_add_u32 s71, s72, 1179776\n"
"buffer_load_dwordx4 v213, s[40:43], s71 offen offset:0 lds\n"
"buffer_load_dwordx4 v213, s[44:47], s71 offen offset:1024 lds\n"
"s_add_u32 m0, s70, 43008\n"
"s_add_u32 s71, s72, 1474688\n"
"buffer_load_dwordx4 v213, s[40:43], s71 offen offset:0 lds\n"
"buffer_load_dwordx4 v213, s[44:47], s71 offen offset:1024 lds\n"
"s_add_u32 m0, s70, 51200\n"
"s_add_u32 s71, s72, 1769600\n"
"buffer_load_dwordx4 v213, s[40:43], s71 offen offset:0 lds\n"
"buffer_load_dwordx4 v213, s[44:47], s71 offen offset:1024 lds\n"
"s_add_u32 m0, s70, 59392\n"
"s_add_u32 s71, s72, 2064512\n"
"buffer_load_dwordx4 v213, s[40:43], s71 offen offset:0 lds\n"
"buffer_load_dwordx4 v213, s[44:47], s71 offen offset:1024 lds\n"
"s_add_u32 m0, s70, 67584\n"
"s_add_u32 s71, s72, 2359424\n"
"buffer_load_dwordx4 v213, s[40:43], s71 offen offset:0 lds\n"
"buffer_load_dwordx4 v213, s[44:47], s71 offen offset:1024 lds\n"
"s_add_u32 m0, s70, 75776\n"
"s_add_u32 s71, s72, 2654336\n"
"buffer_load_dwordx4 v213, s[40:43], s71 offen offset:0 lds\n"
"buffer_load_dwordx4 v213, s[44:47], s71 offen offset:1024 lds\n"
"s_sleep 2\n"
"Lxw_%=:\n"
"buffer_load_dword v61, v60, s[36:39], s96 offen offset:0 sc1\n"
"s_waitcnt vmcnt(0)\n"
"v_cmp_ne_u32 vcc, s75, v61\n"
"s_nop 1\n"
"s_cbranch_vccz Lxr_%=\n"
"s_sleep 2\n"
"s_sub_u32 s76, s76, 1\n"
"s_cbranch_scc0 Lxw_%=\n"
"s_mov_b32 s76, 0\n"
"Lxr_%=:\n"
"buffer_load_dwordx4 a[0:3], v212, s[36:39], s73 offen offset:0 sc1\n"
"buffer_load_dwordx4 a[4:7], v212, s[36:39], s74 offen offset:0 sc1\n"
"buffer_load_dwordx4 a[8:11], v212, s[36:39], s73 offen offset:2048 sc1\n"
"buffer_load_dwordx4 a[12:15], v212, s[36:39], s74 offen offset:2048 sc1\n"
"s_add_u32 s73, s73, 4096\n"
"s_add_u32 s74, s74, 4096\n"
"buffer_load_dwordx4 a[16:19], v212, s[36:39], s73 offen offset:0 sc1\n"
"buffer_load_dwordx4 a[20:23], v212, s[36:39], s74 offen offset:0 sc1\n"
"buffer_load_dwordx4 a[24:27], v212, s[36:39], s73 offen offset:2048 sc1\n"
"buffer_load_dwordx4 a[28:31], v212, s[36:39], s74 offen offset:2048 sc1\n"
"s_add_u32 s73, s73, 4096\n"
"s_add_u32 s74, s74, 4096\n"
"buffer_load_dwordx4 a[32:35], v212, s[36:39], s73 offen offset:0 sc1\n"
"buffer_load_dwordx4 a[36:39], v212, s[36:39], s74 offen offset:0 sc1\n"
"buffer_load_dwordx4 a[40:43], v212, s[36:39], s73 offen offset:2048 sc1\n"
"buffer_load_dwordx4 a[44:47], v212, s[36:39], s74 offen offset:2048 sc1\n"
"s_add_u32 s73, s73, 4096\n"
"s_add_u32 s74, s74, 4096\n"
"buffer_load_dwordx4 a[48:51], v212, s[36:39], s73 offen offset:0 sc1\n"
"buffer_load_dwordx4 a[52:55], v212, s[36:39], s74 offen offset:0 sc1\n"
"buffer_load_dwordx4 a[56:59], v212, s[36:39], s73 offen offset:2048 sc1\n"
"buffer_load_dwordx4 a[60:63], v212, s[36:39], s74 offen offset:2048 sc1\n"
"s_add_u32 s73, s73, 4096\n"
"s_add_u32 s74, s74, 4096\n"
"s_waitcnt lgkmcnt(0)\n"
"s_branch Lpad0_%=\n"
".p2align 12\n"
"Lpad0_%=:\n"
"s_cbranch_execz Lpad4_%=\n"
"s_barrier\n"
"ds_read_b64_tr_b16 v[192:193], v208 offset:2048\n"
"ds_read_b64_tr_b16 v[194:195], v208 offset:2560\n"
"ds_read_b64_tr_b16 v[196:197], v208 offset:10240\n"
"ds_read_b64_tr_b16 v[198:199], v208 offset:10752\n"
"ds_read_b64_tr_b16 v[200:201], v208 offset:3072\n"
"ds_read_b64_tr_b16 v[202:203], v208 offset:3584\n"
"ds_read_b64_tr_b16 v[204:205], v208 offset:11264\n"
"ds_read_b64_tr_b16 v[206:207], v208 offset:11776\n"
"s_waitcnt vmcnt(15) lgkmcnt(6)\n"
"v_mfma_f32_16x16x32_bf16 v[0:3], v[192:195], a[0:3], 0\n"
"s_waitcnt vmcnt(11) lgkmcnt(4)\n"
"s_add_u32 m0, s85, 0x0\n"
"s_mov_b32 s92, 0x00ff00ff\n"
"v_mfma_f32_16x16x32_bf16 v[0:3], v[196:199], a[16:19], v[0:3]\n"
"s_waitcnt vmcnt(10)\n"
"buffer_load_dwordx4 v188, s[28:31], s88 offen offset:0 lds\n"
"v_mfma_f32_16x16x32_bf16 v[4:7], v[196:199], a[20:23], 0\n"
"s_add_u32 m0, s85, 0xffffff80\n"
"s_mov_b32 s93, 0x00ff00ff\n"
"v_mfma_f32_16x16x32_bf16 v[4:7], v[192:195], a[4:7], v[4:7]\n"
"ds_read_u16 v128, v215 offset:0\n"
"ds_read_u16 v129, v215 offset:36\n"
"v_mfma_f32_16x16x32_bf16 v[8:11], v[192:195], a[8:11], 0\n"
"s_waitcnt vmcnt(10)\n"
"v_mfma_f32_16x16x32_bf16 v[8:11], v[196:199], a[24:27], v[8:11]\n"
"s_waitcnt vmcnt(9)\n"
"buffer_load_dwordx4 v188, s[28:31], s88 offen offset:1152 lds\n"
"v_mfma_f32_16x16x32_bf16 v[12:15], v[196:199], a[28:31], 0\n"
"s_add_u32 m0, s85, 0xffffff00\n"
"ds_read_b64_tr_b16 v[196:197], v209 offset:2048\n"
"ds_read_b64_tr_b16 v[198:199], v209 offset:2560\n"
"v_mfma_f32_16x16x32_bf16 v[12:15], v[192:195], a[12:15], v[12:15]\n"
"s_waitcnt lgkmcnt(4)\n"
"ds_read_b64_tr_b16 v[192:193], v209 offset:10240\n"
"ds_read_b64_tr_b16 v[194:195], v209 offset:10752\n"
"v_mfma_f32_16x16x32_bf16 v[28:31], v[200:203], a[12:15], 0\n"
"ds_read_u16 v130, v215 offset:72\n"
"ds_read_u16 v131, v215 offset:108\n"
"v_mfma_f32_16x16x32_bf16 v[28:31], v[204:207], a[28:31], v[28:31]\n"
"buffer_load_dwordx4 v188, s[28:31], s88 offen offset:2304 lds\n"
"v_mfma_f32_16x16x32_bf16 v[24:27], v[204:207], a[24:27], 0\n"
"s_add_u32 m0, s85, 0xc00\n"
"ds_read_u16 v132, v215 offset:144\n"
"ds_read_u16 v133, v215 offset:180\n"
"v_mfma_f32_16x16x32_bf16 v[24:27], v[200:203], a[8:11], v[24:27]\n"
"v_mfma_f32_16x16x32_bf16 v[20:23], v[200:203], a[4:7], 0\n"
"ds_read_u16 v134, v215 offset:216\n"
"ds_read_u16 v135, v215 offset:252\n"
"v_mfma_f32_16x16x32_bf16 v[20:23], v[204:207], a[20:23], v[20:23]\n"
"v_mfma_f32_16x16x32_bf16 v[16:19], v[204:207], a[16:19], 0\n"
"ds_read_b64_tr_b16 v[204:205], v209 offset:3072\n"
"ds_read_b64_tr_b16 v[206:207], v209 offset:3584\n"
"v_mfma_f32_16x16x32_bf16 v[16:19], v[200:203], a[0:3], v[16:19]\n"
"s_waitcnt lgkmcnt(8)\n"
"ds_read_b64_tr_b16 v[200:201], v209 offset:11264\n"
"ds_read_b64_tr_b16 v[202:203], v209 offset:11776\n"
"v_mfma_f32_16x16x32_bf16 v[32:35], v[196:199], a[0:3], 0\n"
"buffer_load_dwordx4 v188, s[28:31], s89 offen offset:0 lds\n"
"v_mfma_f32_16x16x32_bf16 v[32:35], v[192:195], a[16:19], v[32:35]\n"
"s_add_u32 m0, s85, 0xb80\n"
"ds_read_u16 v136, v215 offset:8\n"
"ds_read_u16 v137, v215 offset:44\n"
"v_mfma_f32_16x16x32_bf16 v[36:39], v[192:195], a[20:23], 0\n"
"v_mfma_f32_16x16x32_bf16 v[36:39], v[196:199], a[4:7], v[36:39]\n"
"ds_read_u16 v138, v215 offset:80\n"
"ds_read_u16 v139, v215 offset:116\n"
"v_mfma_f32_16x16x32_bf16 v[40:43], v[196:199], a[8:11], 0\n"
"buffer_load_dwordx4 v188, s[28:31], s89 offen offset:1152 lds\n"
"v_mfma_f32_16x16x32_bf16 v[40:43], v[192:195], a[24:27], v[40:43]\n"
"ds_read_u16 v140, v215 offset:152\n"
"s_add_u32 m0, s85, 0xb00\n"
"v_mfma_f32_16x16x32_bf16 v[44:47], v[192:195], a[28:31], 0\n"
"s_waitcnt lgkmcnt(7)\n"
"ds_read_b64_tr_b16 v[192:193], v210 offset:2048\n"
"ds_read_b64_tr_b16 v[194:195], v210 offset:2560\n"
"v_mfma_f32_16x16x32_bf16 v[44:47], v[196:199], a[12:15], v[44:47]\n"
"ds_read_b64_tr_b16 v[196:197], v210 offset:10240\n"
"ds_read_b64_tr_b16 v[198:199], v210 offset:10752\n"
"v_mfma_f32_16x16x32_bf16 v[60:63], v[204:207], a[12:15], 0\n"
"s_waitcnt lgkmcnt(9)\n"
"buffer_load_dwordx4 v188, s[28:31], s89 offen offset:2304 lds\n"
"v_mfma_f32_16x16x32_bf16 v[60:63], v[200:203], a[28:31], v[60:63]\n"
"s_add_u32 m0, s85, 5760\n"
"ds_read_u16 v141, v215 offset:188\n"
"v_mfma_f32_16x16x32_bf16 v[56:59], v[200:203], a[24:27], 0\n"
"v_mfma_f32_16x16x32_bf16 v[56:59], v[204:207], a[8:11], v[56:59]\n"
"ds_read_u16 v142, v215 offset:224\n"
"ds_read_u16 v143, v215 offset:260\n"
"v_mfma_f32_16x16x32_bf16 v[52:55], v[204:207], a[4:7], 0\n"
"v_mfma_f32_16x16x32_bf16 v[52:55], v[200:203], a[20:23], v[52:55]\n"
"buffer_load_dwordx4 v189, s[40:43], s87 offen offset:384 lds\n"
"v_mfma_f32_16x16x32_bf16 v[48:51], v[200:203], a[16:19], 0\n"
"ds_read_b64_tr_b16 v[200:201], v210 offset:3072\n"
"ds_read_b64_tr_b16 v[202:203], v210 offset:3584\n"
"v_mfma_f32_16x16x32_bf16 v[48:51], v[204:207], a[0:3], v[48:51]\n"
"s_waitcnt lgkmcnt(5)\n"
"ds_read_b64_tr_b16 v[204:205], v210 offset:11264\n"
"ds_read_b64_tr_b16 v[206:207], v210 offset:11776\n"
"v_mfma_f32_16x16x32_bf16 v[64:67], v[192:195], a[0:3], 0\n"
"ds_read_u16 v144, v216 offset:16\n"
"ds_read_u16 v145, v216 offset:52\n"
"v_mfma_f32_16x16x32_bf16 v[64:67], v[196:199], a[16:19], v[64:67]\n"
"buffer_load_dwordx4 v189, s[44:47], s87 offen offset:1408 lds\n"
"v_mfma_f32_16x16x32_bf16 v[68:71], v[196:199], a[20:23], 0\n"
"s_add_u32 m0, s70, 83968\n"
"ds_read_u16 v146, v216 offset:88\n"
"s_add_u32 s71, s72, 2949248\n"
"v_mfma_f32_16x16x32_bf16 v[68:71], v[192:195], a[4:7], v[68:71]\n"
"s_lshr_b32 s68, s60, 3\n"
"ds_read_u16 v147, v216 offset:124\n"
"s_sub_u32 s68, 31, s68\n"
"v_mfma_f32_16x16x32_bf16 v[72:75], v[192:195], a[8:11], 0\n"
"s_bfe_u32 s66, 0x41291800, s97\n"
"s_bfe_u32 s67, 0x11181118, s97\n"
"s_add_u32 s66, s66, s68\n"
"v_mfma_f32_16x16x32_bf16 v[72:75], v[196:199], a[24:27], v[72:75]\n"
"ds_read_u16 v148, v216 offset:160\n"
"ds_read_u16 v149, v216 offset:196\n"
"v_mfma_f32_16x16x32_bf16 v[76:79], v[196:199], a[28:31], 0\n"
"ds_read_b64_tr_b16 v[196:197], v211 offset:2048\n"
"ds_read_b64_tr_b16 v[198:199], v211 offset:2560\n"
"v_mfma_f32_16x16x32_bf16 v[76:79], v[192:195], a[12:15], v[76:79]\n"
"s_waitcnt lgkmcnt(8)\n"
"ds_read_b64_tr_b16 v[192:193], v211 offset:10240\n"
"ds_read_b64_tr_b16 v[194:195], v211 offset:10752\n"
"v_mfma_f32_16x16x32_bf16 v[92:95], v[200:203], a[12:15], 0\n"
"ds_read_u16 v150, v216 offset:232\n"
"ds_read_u16 v151, v216 offset:268\n"
"v_mfma_f32_16x16x32_bf16 v[92:95], v[204:207], a[28:31], v[92:95]\n"
"s_cmp_lt_u32 s68, s67\n"
"s_cselect_b32 s66, s66, 126\n"
"v_mfma_f32_16x16x32_bf16 v[88:91], v[204:207], a[24:27], 0\n"
"s_lshr_b32 s69, s66, 1\n"
"v_lshl_or_b32 v152, v129, 16, v128\n"
"v_lshl_or_b32 v153, v131, 16, v130\n"
"v_mfma_f32_16x16x32_bf16 v[88:91], v[200:203], a[8:11], v[88:91]\n"
"s_and_b32 s66, s66, 1\n"
"v_lshl_or_b32 v154, v133, 16, v132\n"
"v_lshl_or_b32 v155, v135, 16, v134\n"
"v_mfma_f32_16x16x32_bf16 v[84:87], v[200:203], a[4:7], 0\n"
"s_and_b32 s99, s62, 2\n"
"v_cndmask_b32 v224, 0, v152, s[92:93]\n"
"v_cndmask_b32 v236, v152, 0, s[92:93]\n"
"v_mfma_f32_16x16x32_bf16 v[84:87], v[204:207], a[20:23], v[84:87]\n"
"s_or_b32 s99, s99, s66\n"
"v_cndmask_b32 v225, 0, v153, s[92:93]\n"
"v_cndmask_b32 v237, v153, 0, s[92:93]\n"
"v_mfma_f32_16x16x32_bf16 v[80:83], v[204:207], a[16:19], 0\n"
"s_mov_b64 s[52:53], s[24:25]\n"
"ds_read_b64_tr_b16 v[204:205], v211 offset:3072\n"
"ds_read_b64_tr_b16 v[206:207], v211 offset:3584\n"
"v_mfma_f32_16x16x32_bf16 v[80:83], v[200:203], a[0:3], v[80:83]\n"
"s_waitcnt lgkmcnt(4)\n"
"ds_read_b64_tr_b16 v[200:201], v211 offset:11264\n"
"ds_read_b64_tr_b16 v[202:203], v211 offset:11776\n"
"v_mfma_f32_16x16x32_bf16 v[96:99], v[196:199], a[0:3], 0\n"
"s_mov_b32 s54, 2359296\n"
"v_cndmask_b32 v226, 0, v154, s[92:93]\n"
"v_cndmask_b32 v238, v154, 0, s[92:93]\n"
"v_mfma_f32_16x16x32_bf16 v[96:99], v[192:195], a[16:19], v[96:99]\n"
"s_mov_b32 s55, 0x20000\n"
"v_cndmask_b32 v227, 0, v155, s[92:93]\n"
"v_cndmask_b32 v239, v155, 0, s[92:93]\n"
"v_mfma_f32_16x16x32_bf16 v[100:103], v[192:195], a[20:23], 0\n"
"s_mov_b32 s67, 663552\n"
"v_lshl_or_b32 v156, v137, 16, v136\n"
"v_lshl_or_b32 v157, v139, 16, v138\n"
"v_mfma_f32_16x16x32_bf16 v[100:103], v[196:199], a[4:7], v[100:103]\n"
"v_lshl_or_b32 v158, v141, 16, v140\n"
"v_lshl_or_b32 v159, v143, 16, v142\n"
"v_mfma_f32_16x16x32_bf16 v[104:107], v[196:199], a[8:11], 0\n"
"v_cndmask_b32 v228, 0, v156, s[92:93]\n"
"v_cndmask_b32 v240, v156, 0, s[92:93]\n"
"v_mfma_f32_16x16x32_bf16 v[104:107], v[192:195], a[24:27], v[104:107]\n"
"v_cndmask_b32 v229, 0, v157, s[92:93]\n"
"v_cndmask_b32 v241, v157, 0, s[92:93]\n"
"v_mfma_f32_16x16x32_bf16 v[108:111], v[192:195], a[28:31], 0\n"
"ds_read_b64_tr_b16 v[192:193], v208 offset:18432\n"
"ds_read_b64_tr_b16 v[194:195], v208 offset:18944\n"
"v_mfma_f32_16x16x32_bf16 v[108:111], v[196:199], a[12:15], v[108:111]\n"
"s_waitcnt lgkmcnt(2)\n"
"ds_read_b64_tr_b16 v[196:197], v208 offset:26624\n"
"ds_read_b64_tr_b16 v[198:199], v208 offset:27136\n"
"v_mfma_f32_16x16x32_bf16 v[124:127], v[204:207], a[12:15], 0\n"
"buffer_load_dwordx4 a[64:67], v212, s[36:39], s73 offen offset:0 sc1\n"
"v_mfma_f32_16x16x32_bf16 v[124:127], v[200:203], a[28:31], v[124:127]\n"
"v_cndmask_b32 v230, 0, v158, s[92:93]\n"
"v_cndmask_b32 v242, v158, 0, s[92:93]\n"
"v_mfma_f32_16x16x32_bf16 v[120:123], v[200:203], a[24:27], 0\n"
"v_cndmask_b32 v231, 0, v159, s[92:93]\n"
"v_cndmask_b32 v243, v159, 0, s[92:93]\n"
"v_mfma_f32_16x16x32_bf16 v[120:123], v[204:207], a[8:11], v[120:123]\n"
"v_lshl_or_b32 v160, v145, 16, v144\n"
"v_lshl_or_b32 v161, v147, 16, v146\n"
"v_mfma_f32_16x16x32_bf16 v[116:119], v[204:207], a[4:7], 0\n"
"buffer_load_dwordx4 a[68:71], v212, s[36:39], s74 offen offset:0 sc1\n"
"v_mfma_f32_16x16x32_bf16 v[116:119], v[200:203], a[20:23], v[116:119]\n"
"v_lshl_or_b32 v162, v149, 16, v148\n"
"v_lshl_or_b32 v163, v151, 16, v150\n"
"v_mfma_f32_16x16x32_bf16 v[112:115], v[200:203], a[16:19], 0\n"
"ds_read_b64_tr_b16 v[200:201], v208 offset:19456\n"
"ds_read_b64_tr_b16 v[202:203], v208 offset:19968\n"
"v_mfma_f32_16x16x32_bf16 v[112:115], v[204:207], a[0:3], v[112:115]\n"
"s_waitcnt vmcnt(17) lgkmcnt(2)\n"
"ds_read_b64_tr_b16 v[204:205], v208 offset:27648\n"
"ds_read_b64_tr_b16 v[206:207], v208 offset:28160\n"
"v_mfma_f32_16x16x32_bf16 v[0:3], v[192:195], a[32:35], v[0:3]\n"
"s_waitcnt vmcnt(13)\n"
"buffer_load_dwordx4 a[72:75], v212, s[36:39], s73 offen offset:2048 sc1\n"
"v_mfma_f32_16x16x32_bf16 v[0:3], v[196:199], a[48:51], v[0:3]\n"
"s_waitcnt vmcnt(13)\n"
"v_cndmask_b32 v232, 0, v160, s[92:93]\n"
"v_cndmask_b32 v244, v160, 0, s[92:93]\n"
"v_mfma_f32_16x16x32_bf16 v[4:7], v[196:199], a[52:55], v[4:7]\n"
"v_cndmask_b32 v233, 0, v161, s[92:93]\n"
"v_cndmask_b32 v245, v161, 0, s[92:93]\n"
"v_mfma_f32_16x16x32_bf16 v[4:7], v[192:195], a[36:39], v[4:7]\n"
"v_cndmask_b32 v234, 0, v162, s[92:93]\n"
"v_cndmask_b32 v246, v162, 0, s[92:93]\n"
"v_mfma_f32_16x16x32_bf16 v[8:11], v[192:195], a[40:43], v[8:11]\n"
"s_waitcnt vmcnt(12)\n"
"v_cndmask_b32 v235, 0, v163, s[92:93]\n"
"v_cndmask_b32 v247, v163, 0, s[92:93]\n"
"v_mfma_f32_16x16x32_bf16 v[8:11], v[196:199], a[56:59], v[8:11]\n"
"s_waitcnt vmcnt(11)\n"
"buffer_load_dwordx4 a[76:79], v212, s[36:39], s74 offen offset:2048 sc1\n"
"v_mfma_f32_16x16x32_bf16 v[12:15], v[196:199], a[60:63], v[12:15]\n"
"s_add_u32 s73, s73, 4096\n"
"ds_read_b64_tr_b16 v[196:197], v209 offset:18432\n"
"ds_read_b64_tr_b16 v[198:199], v209 offset:18944\n"
"v_mfma_f32_16x16x32_bf16 v[12:15], v[192:195], a[44:47], v[12:15]\n"
"s_waitcnt lgkmcnt(2)\n"
"ds_read_b64_tr_b16 v[192:193], v209 offset:26624\n"
"ds_read_b64_tr_b16 v[194:195], v209 offset:27136\n"
"v_mfma_f32_16x16x32_bf16 v[28:31], v[200:203], a[44:47], v[28:31]\n"
"s_add_u32 s74, s74, 4096\n"
"s_mov_b32 s92, 0x3f80\n"
"v_writelane_b32 v232, s92, 49\n"
"v_mfma_f32_16x16x32_bf16 v[28:31], v[204:207], a[60:63], v[28:31]\n"
"buffer_load_dwordx4 a[80:83], v212, s[36:39], s73 offen offset:0 sc1\n"
"v_mfma_f32_16x16x32_bf16 v[24:27], v[204:207], a[56:59], v[24:27]\n"
"v_writelane_b32 v232, 1.0, 53\n"
"v_mfma_f32_16x16x32_bf16 v[24:27], v[200:203], a[40:43], v[24:27]\n"
"s_cmp_lt_u32 s69, 36\n"
"s_cselect_b64 s[24:25], s[20:21], s[14:15]\n"
"s_cselect_b32 s26, 5308416, s67\n"
"v_mfma_f32_16x16x32_bf16 v[20:23], v[200:203], a[36:39], v[20:23]\n"
"s_cmp_lt_u32 s69, 41\n"
"s_cselect_b32 s26, s26, 0\n"
"v_mfma_f32_16x16x32_bf16 v[20:23], v[204:207], a[52:55], v[20:23]\n"
"s_mov_b32 s27, 0x20000\n"
"s_mul_i32 s66, s65, 114688\n"
"s_add_u32 s66, s66, s70\n"
"v_mfma_f32_16x16x32_bf16 v[16:19], v[204:207], a[48:51], v[16:19]\n"
"s_add_u32 s81, s66, 2359296\n"
"ds_read_b64_tr_b16 v[204:205], v209 offset:19456\n"
"ds_read_b64_tr_b16 v[206:207], v209 offset:19968\n"
"v_mfma_f32_16x16x32_bf16 v[16:19], v[200:203], a[32:35], v[16:19]\n"
"s_waitcnt lgkmcnt(2)\n"
"ds_read_b64_tr_b16 v[200:201], v209 offset:27648\n"
"ds_read_b64_tr_b16 v[202:203], v209 offset:28160\n"
"v_mfma_f32_16x16x32_bf16 v[32:35], v[196:199], a[32:35], v[32:35]\n"
"buffer_load_dwordx4 a[84:87], v212, s[36:39], s74 offen offset:0 sc1\n"
"v_mfma_f32_16x16x32_bf16 v[32:35], v[192:195], a[48:51], v[32:35]\n"
"s_and_b32 s67, s64, 3\n"
"s_lshl2_add_u32 s66, s62, s67\n"
"s_mul_i32 s66, s66, 385024\n"
"v_mfma_f32_16x16x32_bf16 v[36:39], v[192:195], a[52:55], v[36:39]\n"
"s_add_u32 s66, s66, s70\n"
"s_add_u32 s82, s66, 6029312\n"
"v_mfma_f32_16x16x32_bf16 v[36:39], v[196:199], a[36:39], v[36:39]\n"
"s_cmp_ge_u32 s69, 18\n"
"s_cselect_b32 s68, 1, 0\n"
"v_mfma_f32_16x16x32_bf16 v[40:43], v[196:199], a[40:43], v[40:43]\n"
"buffer_load_dwordx4 a[88:91], v212, s[36:39], s73 offen offset:2048 sc1\n"
"v_mfma_f32_16x16x32_bf16 v[40:43], v[192:195], a[56:59], v[40:43]\n"
"s_mul_i32 s90, s68, 18\n"
"s_sub_u32 s90, s69, s90\n"
"s_lshr_b32 s91, s90, 2\n"
"v_mfma_f32_16x16x32_bf16 v[44:47], v[192:195], a[60:63], v[44:47]\n"
"s_lshl2_add_u32 s91, s68, s91\n"
"ds_read_b64_tr_b16 v[192:193], v210 offset:18432\n"
"ds_read_b64_tr_b16 v[194:195], v210 offset:18944\n"
"v_mfma_f32_16x16x32_bf16 v[44:47], v[196:199], a[44:47], v[44:47]\n"
"s_waitcnt lgkmcnt(2)\n"
"ds_read_b64_tr_b16 v[196:197], v210 offset:26624\n"
"ds_read_b64_tr_b16 v[198:199], v210 offset:27136\n"
"v_mfma_f32_16x16x32_bf16 v[60:63], v[204:207], a[44:47], v[60:63]\n"
"buffer_load_dwordx4 a[92:95], v212, s[36:39], s74 offen offset:2048 sc1\n"
"v_mfma_f32_16x16x32_bf16 v[60:63], v[200:203], a[60:63], v[60:63]\n"
"s_add_u32 s73, s73, 4096\n"
"s_and_b32 s96, s90, 1\n"
"s_lshl1_add_u32 s97, s91, s96\n"
"v_mfma_f32_16x16x32_bf16 v[56:59], v[200:203], a[56:59], v[56:59]\n"
"s_add_u32 s74, s74, 4096\n"
"s_lshl_b32 s96, s96, 3\n"
"s_sub_u32 s96, 28, s96\n"
"v_mfma_f32_16x16x32_bf16 v[56:59], v[204:207], a[40:43], v[56:59]\n"
"s_add_u32 s96, s96, s91\n"
"s_bitcmp1_b32 s90, 1\n"
"s_cselect_b32 s97, s96, s97\n"
"v_mfma_f32_16x16x32_bf16 v[52:55], v[204:207], a[36:39], v[52:55]\n"
"s_lshl1_add_u32 s96, s68, s90\n"
"s_cmp_ge_u32 s90, 16\n"
"s_cselect_b32 s97, s96, s97\n"
"v_mfma_f32_16x16x32_bf16 v[52:55], v[200:203], a[52:55], v[52:55]\n"
"buffer_load_dwordx4 a[96:99], v212, s[36:39], s73 offen offset:0 sc1\n"
"v_mfma_f32_16x16x32_bf16 v[48:51], v[200:203], a[48:51], v[48:51]\n"
"s_lshl1_add_u32 s66, s99, s68\n"
"ds_read_b64_tr_b16 v[200:201], v210 offset:19456\n"
"ds_read_b64_tr_b16 v[202:203], v210 offset:19968\n"
"v_mfma_f32_16x16x32_bf16 v[48:51], v[204:207], a[32:35], v[48:51]\n"
"s_waitcnt lgkmcnt(2)\n"
"ds_read_b64_tr_b16 v[204:205], v210 offset:27648\n"
"ds_read_b64_tr_b16 v[206:207], v210 offset:28160\n"
"v_mfma_f32_16x16x32_bf16 v[64:67], v[192:195], a[32:35], v[64:67]\n"
"s_lshr_b32 s91, s90, 1\n"
"s_xor_b32 s91, s91, 1\n"
"s_lshl3_add_u32 s96, s66, s91\n"
"v_mfma_f32_16x16x32_bf16 v[64:67], v[196:199], a[48:51], v[64:67]\n"
"buffer_load_dwordx4 a[100:103], v212, s[36:39], s74 offen offset:0 sc1\n"
"v_mfma_f32_16x16x32_bf16 v[68:71], v[196:199], a[52:55], v[68:71]\n"
"s_add_u32 s98, s66, 64\n"
"v_mfma_f32_16x16x32_bf16 v[68:71], v[192:195], a[36:39], v[68:71]\n"
"s_waitcnt vmcnt(10)\n"
"ds_write_b32 v214, v219 offset:1952\n"
"v_mfma_f32_16x16x32_bf16 v[72:75], v[192:195], a[40:43], v[72:75]\n"
"s_cmp_eq_u32 s91, 3\n"
"s_cselect_b32 s96, s98, s96\n"
"v_mfma_f32_16x16x32_bf16 v[72:75], v[196:199], a[56:59], v[72:75]\n"
"s_lshl3_add_u32 s98, s66, 3\n"
"ds_write_b32 v214, v219 offset:0\n"
"v_mfma_f32_16x16x32_bf16 v[76:79], v[196:199], a[60:63], v[76:79]\n"
"ds_read_b64_tr_b16 v[196:197], v211 offset:18432\n"
"ds_read_b64_tr_b16 v[198:199], v211 offset:18944\n"
"v_mfma_f32_16x16x32_bf16 v[76:79], v[192:195], a[44:47], v[76:79]\n"
"s_waitcnt lgkmcnt(4)\n"
"ds_read_b64_tr_b16 v[192:193], v211 offset:26624\n"
"ds_read_b64_tr_b16 v[194:195], v211 offset:27136\n"
"v_mfma_f32_16x16x32_bf16 v[92:95], v[200:203], a[44:47], v[92:95]\n"
"buffer_load_dwordx4 a[104:107], v212, s[36:39], s73 offen offset:2048 sc1\n"
"v_mfma_f32_16x16x32_bf16 v[92:95], v[204:207], a[60:63], v[92:95]\n"
"ds_read_b32 v191, v220 offset:1952\n"
"v_mfma_f32_16x16x32_bf16 v[88:91], v[204:207], a[56:59], v[88:91]\n"
"s_cmp_ge_u32 s90, 16\n"
"s_cselect_b32 s96, s98, s96\n"
"v_mfma_f32_16x16x32_bf16 v[88:91], v[200:203], a[40:43], v[88:91]\n"
"s_and_b32 s90, s90, 1\n"
"s_lshl1_add_u32 s90, s96, s90\n"
"s_lshl2_add_u32 s90, s90, s61\n"
"v_mfma_f32_16x16x32_bf16 v[84:87], v[200:203], a[36:39], v[84:87]\n"
"buffer_load_dwordx4 a[108:111], v212, s[36:39], s74 offen offset:2048 sc1\n"
"v_mfma_f32_16x16x32_bf16 v[84:87], v[204:207], a[52:55], v[84:87]\n"
"s_add_u32 s73, s73, 4096\n"
"ds_read_b32 v221, v220 offset:0\n"
"s_lshl_b32 s90, s90, 3\n"
"v_mfma_f32_16x16x32_bf16 v[80:83], v[204:207], a[48:51], v[80:83]\n"
"s_add_u32 s74, s74, 4096\n"
"ds_read_b64_tr_b16 v[204:205], v211 offset:19456\n"
"ds_read_b64_tr_b16 v[206:207], v211 offset:19968\n"
"v_mfma_f32_16x16x32_bf16 v[80:83], v[200:203], a[32:35], v[80:83]\n"
"s_waitcnt lgkmcnt(4)\n"
"ds_read_b64_tr_b16 v[200:201], v211 offset:27648\n"
"ds_read_b64_tr_b16 v[202:203], v211 offset:28160\n"
"v_mfma_f32_16x16x32_bf16 v[96:99], v[196:199], a[32:35], v[96:99]\n"
"buffer_load_dwordx4 a[112:115], v212, s[36:39], s73 offen offset:0 sc1\n"
"v_mfma_f32_16x16x32_bf16 v[96:99], v[192:195], a[48:51], v[96:99]\n"
"s_waitcnt lgkmcnt(5)\n"
"v_cmp_ne_u32 s[92:93], s75, v191\n"
"s_lshl2_add_u32 s91, s99, s69\n"
"v_mfma_f32_16x16x32_bf16 v[100:103], v[192:195], a[52:55], v[100:103]\n"
"s_waitcnt lgkmcnt(4)\n"
"v_cmp_ne_u32 vcc, s75, v221\n"
"s_mul_i32 s91, s91, 36\n"
"v_mfma_f32_16x16x32_bf16 v[100:103], v[196:199], a[36:39], v[100:103]\n"
"s_cmp_lg_u64 s[92:93], 0\n"
"s_cbranch_scc1 Lsb61_%=\n"
"Lsr61_%=:\n"
"v_mfma_f32_16x16x32_bf16 v[104:107], v[196:199], a[40:43], v[104:107]\n"
"s_cbranch_vccnz Lsb0_%=\n"
"Lsr0_%=:\n"
"ds_read2_b32 a[232:233], v186 offset0:0 offset1:1\n"
"v_mfma_f32_16x16x32_bf16 v[104:107], v[192:195], a[56:59], v[104:107]\n"
"buffer_load_dwordx4 a[116:119], v212, s[36:39], s74 offen offset:0 sc1\n"
"v_mfma_f32_16x16x32_bf16 v[108:111], v[192:195], a[60:63], v[108:111]\n"
"s_lshl3_add_u32 s91, s61, s91\n"
"ds_read_b64_tr_b16 v[192:193], v208 offset:34816\n"
"ds_read_b64_tr_b16 v[194:195], v208 offset:35328\n"
"v_mfma_f32_16x16x32_bf16 v[108:111], v[196:199], a[44:47], v[108:111]\n"
"s_waitcnt lgkmcnt(3)\n"
"ds_read_b64_tr_b16 v[196:197], v208 offset:43008\n"
"ds_read_b64_tr_b16 v[198:199], v208 offset:43520\n"
"v_mfma_f32_16x16x32_bf16 v[124:127], v[204:207], a[44:47], v[124:127]\n"
"s_and_b32 s66, s61, 2\n"
"ds_read2_b32 a[234:235], v186 offset0:2 offset1:3\n"
"ds_read_b64_tr_b16 v[160:161], v187 offset:6144\n"
"v_mfma_f32_16x16x32_bf16 v[124:127], v[200:203], a[60:63], v[124:127]\n"
"buffer_load_dwordx4 a[120:123], v212, s[36:39], s73 offen offset:2048 sc1\n"
"v_mfma_f32_16x16x32_bf16 v[120:123], v[200:203], a[56:59], v[120:123]\n"
"s_add_u32 s91, s91, s66\n"
"ds_read_b64_tr_b16 v[162:163], v187 offset:6656\n"
"v_add_u32 v186, 1024, v186\n"
"v_mfma_f32_16x16x32_bf16 v[120:123], v[204:207], a[40:43], v[120:123]\n"
"s_sub_u32 s91, s91, 1296\n"
"v_mfma_f32_16x16x32_bf16 v[116:119], v[204:207], a[36:39], v[116:119]\n"
"ds_read_b64_tr_b16 v[168:169], v187 offset:7168\n"
"ds_read_b64_tr_b16 v[170:171], v187 offset:7680\n"
"v_mfma_f32_16x16x32_bf16 v[116:119], v[200:203], a[52:55], v[116:119]\n"
"ds_read2_b32 a[240:241], v186 offset0:0 offset1:1\n"
"v_mfma_f32_16x16x32_bf16 v[112:115], v[200:203], a[48:51], v[112:115]\n"
"ds_read_b64_tr_b16 v[200:201], v208 offset:35840\n"
"ds_read_b64_tr_b16 v[202:203], v208 offset:36352\n"
"v_mfma_f32_16x16x32_bf16 v[112:115], v[204:207], a[32:35], v[112:115]\n"
"s_waitcnt vmcnt(14) lgkmcnt(8)\n"
"ds_read_b64_tr_b16 v[204:205], v208 offset:44032\n"
"ds_read_b64_tr_b16 v[206:207], v208 offset:44544\n"
"v_mfma_f32_16x16x32_bf16 v[0:3], v[192:195], a[64:67], v[0:3]\n"
"s_waitcnt vmcnt(10)\n"
"buffer_load_dwordx4 a[124:127], v212, s[36:39], s74 offen offset:2048 sc1\n"
"v_mfma_f32_16x16x32_bf16 v[0:3], v[196:199], a[80:83], v[0:3]\n"
"s_waitcnt vmcnt(10)\n"
"ds_read2_b32 a[242:243], v186 offset0:2 offset1:3\n"
"s_add_u32 s73, s73, 4096\n"
"v_mfma_f32_16x16x32_bf16 v[4:7], v[196:199], a[84:87], v[4:7]\n"
"s_add_u32 s74, s74, 4096\n"
"v_add_u32 v186, 1024, v186\n"
"v_mfma_f32_16x16x32_bf16 v[4:7], v[192:195], a[68:71], v[4:7]\n"
"s_waitcnt lgkmcnt(8)\n"
"buffer_store_dwordx4 v[160:163], v176, s[36:39], s79 offen offset:0 sc1\n"
"v_mfma_f32_16x16x32_bf16 v[8:11], v[192:195], a[72:75], v[8:11]\n"
"s_waitcnt vmcnt(10)\n"
"v_mfma_f32_16x16x32_bf16 v[8:11], v[196:199], a[88:91], v[8:11]\n"
"s_waitcnt vmcnt(9)\n"
"ds_read2_b32 a[248:249], v186 offset0:0 offset1:1\n"
"v_mfma_f32_16x16x32_bf16 v[12:15], v[196:199], a[92:95], v[12:15]\n"
"ds_read_b64_tr_b16 v[196:197], v209 offset:34816\n"
"ds_read_b64_tr_b16 v[198:199], v209 offset:35328\n"
"v_mfma_f32_16x16x32_bf16 v[12:15], v[192:195], a[76:79], v[12:15]\n"
"s_waitcnt lgkmcnt(4)\n"
"ds_read_b64_tr_b16 v[192:193], v209 offset:43008\n"
"ds_read_b64_tr_b16 v[194:195], v209 offset:43520\n"
"v_mfma_f32_16x16x32_bf16 v[28:31], v[200:203], a[76:79], v[28:31]\n"
"buffer_load_dwordx4 a[128:131], v212, s[36:39], s73 offen offset:0 sc1\n"
"v_mfma_f32_16x16x32_bf16 v[28:31], v[204:207], a[92:95], v[28:31]\n"
"ds_read2_b32 a[250:251], v186 offset0:2 offset1:3\n"
"v_mfma_f32_16x16x32_bf16 v[24:27], v[204:207], a[88:91], v[24:27]\n"
"v_add_u32 v186, 1024, v186\n"
"v_mfma_f32_16x16x32_bf16 v[24:27], v[200:203], a[72:75], v[24:27]\n"
"buffer_store_dwordx4 v[168:171], v176, s[36:39], s79 offen offset:1024 sc1\n"
"v_mfma_f32_16x16x32_bf16 v[128:131], a[232:235], v[224:227], 0\n"
"s_waitcnt lgkmcnt(6)\n"
"v_mfma_f32_16x16x32_bf16 v[128:131], a[240:243], v[236:239], v[128:131]\n"
"s_cmp_ge_u32 s69, 36\n"
"s_cselect_b32 s90, s91, s90\n"
"s_cselect_b32 s97, s69, s97\n"
"v_mfma_f32_16x16x32_bf16 v[20:23], v[200:203], a[68:71], v[20:23]\n"
"s_lshl1_add_u32 s91, s99, s61\n"
"s_mul_i32 s91, s91, 72\n"
"s_add_u32 s91, s91, 16\n"
"v_mfma_f32_16x16x32_bf16 v[20:23], v[204:207], a[84:87], v[20:23]\n"
"ds_read2_b32 a[236:237], v186 offset0:0 offset1:1\n"
"v_mfma_f32_16x16x32_bf16 v[136:139], a[232:235], v[228:231], 0\n"
"v_mfma_f32_16x16x32_bf16 v[136:139], a[240:243], v[240:243], v[136:139]\n"
"s_cmp_eq_u32 s69, 40\n"
"s_cselect_b32 s90, s91, s90\n"
"s_cselect_b32 s31, 18432, 0\n"
"s_cselect_b32 s94, 10, 11\n"
"v_mfma_f32_16x16x32_bf16 v[16:19], v[204:207], a[80:83], v[16:19]\n"
"s_mul_i32 s90, s90, 1152\n"
"ds_read_b64_tr_b16 v[204:205], v209 offset:35840\n"
"ds_read_b64_tr_b16 v[206:207], v209 offset:36352\n"
"v_mfma_f32_16x16x32_bf16 v[16:19], v[200:203], a[64:67], v[16:19]\n"
"ds_read_b64_tr_b16 v[200:201], v209 offset:44032\n"
"ds_read_b64_tr_b16 v[202:203], v209 offset:44544\n"
"v_mfma_f32_16x16x32_bf16 v[144:147], a[232:235], v[232:235], 0\n"
"v_mfma_f32_16x16x32_bf16 v[144:147], a[240:243], v[244:247], v[144:147]\n"
"s_waitcnt lgkmcnt(5)\n"
"s_cmp_lt_u32 s69, 41\n"
"s_cselect_b32 s83, 0, 0x40000000\n"
"v_mfma_f32_16x16x32_bf16 v[32:35], v[196:199], a[64:67], v[32:35]\n"
"s_lshl2_add_u32 s66, s99, s61\n"
"s_lshl_b32 s68, s66, 6\n"
"s_add_u32 s68, s69, s68\n"
"v_mfma_f32_16x16x32_bf16 v[32:35], v[192:195], a[80:83], v[32:35]\n"
"buffer_load_dwordx4 a[132:135], v212, s[36:39], s74 offen offset:0 sc1\n"
"v_mfma_f32_16x16x32_bf16 v[152:155], a[248:251], v[248:251], 0\n"
"s_lshl_b32 s68, s68, 7\n"
"s_add_u32 s69, s68, 44302336\n"
"s_mul_i32 s66, s66, 385024\n"
"v_mfma_f32_16x16x32_bf16 v[36:39], v[192:195], a[84:87], v[36:39]\n"
"s_and_b32 s68, s97, 1\n"
"ds_read2_b32 a[238:239], v186 offset0:2 offset1:3\n"
"v_mul_f32 v128, 0x3fb8aa3b, v128\n"
"v_mfma_f32_16x16x32_bf16 v[36:39], v[196:199], a[68:71], v[36:39]\n"
"s_mul_i32 s97, s97, 9216\n"
"v_add_u32 v186, 1024, v186\n"
"v_mul_f32 v129, 0x3fb8aa3b, v129\n"
"v_mfma_f32_16x16x32_bf16 v[40:43], v[196:199], a[72:75], v[40:43]\n"
"s_lshl_b32 s68, s68, 13\n"
"v_mul_f32 v130, 0x3fb8aa3b, v130\n"
"v_mul_f32 v131, 0x3fb8aa3b, v131\n"
"v_mfma_f32_16x16x32_bf16 v[40:43], v[192:195], a[88:91], v[40:43]\n"
"s_sub_u32 s97, s97, s68\n"
"v_cvt_pk_bf16_f32 v128, v128, v129\n"
"v_cvt_pk_bf16_f32 v129, v130, v131\n"
"v_mfma_f32_16x16x32_bf16 v[44:47], v[192:195], a[92:95], v[44:47]\n"
"s_add_u32 s97, s97, s66\n"
"ds_read_b64_tr_b16 v[192:193], v210 offset:34816\n"
"ds_read_b64_tr_b16 v[194:195], v210 offset:35328\n"
"v_mfma_f32_16x16x32_bf16 v[44:47], v[196:199], a[76:79], v[44:47]\n"
"s_waitcnt lgkmcnt(3)\n"
"ds_read_b64_tr_b16 v[196:197], v210 offset:43008\n"
"ds_read_b64_tr_b16 v[198:199], v210 offset:43520\n"
"v_mfma_f32_16x16x32_bf16 v[60:63], v[204:207], a[76:79], v[60:63]\n"
"buffer_load_dwordx4 a[136:139], v212, s[36:39], s73 offen offset:2048 sc1\n"
"v_mfma_f32_16x16x32_bf16 v[60:63], v[200:203], a[92:95], v[60:63]\n"
"s_add_u32 s97, s97, 6029312\n"
"ds_read2_b32 a[244:245], v186 offset0:0 offset1:1\n"
"v_mul_f32 v136, 0x3fb8aa3b, v136\n"
"v_mfma_f32_16x16x32_bf16 v[56:59], v[200:203], a[88:91], v[56:59]\n"
"v_mul_f32 v137, 0x3fb8aa3b, v137\n"
"v_mul_f32 v138, 0x3fb8aa3b, v138\n"
"v_mfma_f32_16x16x32_bf16 v[56:59], v[204:207], a[72:75], v[56:59]\n"
"v_mul_f32 v139, 0x3fb8aa3b, v139\n"
"v_cvt_pk_bf16_f32 v136, v136, v137\n"
"v_mfma_f32_16x16x32_bf16 v[52:55], v[204:207], a[68:71], v[52:55]\n"
"buffer_load_dwordx4 a[140:143], v212, s[36:39], s74 offen offset:2048 sc1\n"
"v_mfma_f32_16x16x32_bf16 v[52:55], v[200:203], a[84:87], v[52:55]\n"
"s_add_u32 s73, s73, 4096\n"
"ds_read2_b32 a[246:247], v186 offset0:2 offset1:3\n"
"v_cvt_pk_bf16_f32 v137, v138, v139\n"
"v_mfma_f32_16x16x32_bf16 v[48:51], v[200:203], a[80:83], v[48:51]\n"
"s_add_u32 s74, s74, 4096\n"
"ds_read_b64_tr_b16 v[200:201], v210 offset:35840\n"
"ds_read_b64_tr_b16 v[202:203], v210 offset:36352\n"
"v_mfma_f32_16x16x32_bf16 v[48:51], v[204:207], a[64:67], v[48:51]\n"
"s_waitcnt lgkmcnt(4)\n"
"ds_read_b64_tr_b16 v[204:205], v210 offset:44032\n"
"ds_read_b64_tr_b16 v[206:207], v210 offset:44544\n"
"v_mfma_f32_16x16x32_bf16 v[64:67], v[192:195], a[64:67], v[64:67]\n"
"buffer_load_dwordx4 a[144:147], v212, s[36:39], s73 offen offset:0 sc1\n"
"v_mfma_f32_16x16x32_bf16 v[64:67], v[196:199], a[80:83], v[64:67]\n"
"v_add_u32 v186, 1024, v186\n"
"v_mul_f32 v144, v177, v144\n"
"v_mfma_f32_16x16x32_bf16 v[68:71], v[196:199], a[84:87], v[68:71]\n"
"v_mul_f32 v145, v177, v145\n"
"v_mul_f32 v146, v177, v146\n"
"v_mfma_f32_16x16x32_bf16 v[68:71], v[192:195], a[68:71], v[68:71]\n"
"v_mul_f32 v147, v177, v147\n"
"v_cvt_pk_bf16_f32 v144, v144, v145\n"
"v_mfma_f32_16x16x32_bf16 v[72:75], v[192:195], a[72:75], v[72:75]\n"
"v_add_u32 v188, 256, v212\n"
"v_and_b32 v188, 1024, v188\n"
"v_mfma_f32_16x16x32_bf16 v[72:75], v[196:199], a[88:91], v[72:75]\n"
"buffer_load_dwordx4 a[148:151], v212, s[36:39], s74 offen offset:0 sc1\n"
"v_mfma_f32_16x16x32_bf16 v[76:79], v[196:199], a[92:95], v[76:79]\n"
"ds_read_b64_tr_b16 v[196:197], v211 offset:34816\n"
"ds_read_b64_tr_b16 v[198:199], v211 offset:35328\n"
"v_mfma_f32_16x16x32_bf16 v[76:79], v[192:195], a[76:79], v[76:79]\n"
"s_waitcnt lgkmcnt(2)\n"
"ds_read_b64_tr_b16 v[192:193], v211 offset:43008\n"
"ds_read_b64_tr_b16 v[194:195], v211 offset:43520\n"
"v_mfma_f32_16x16x32_bf16 v[92:95], v[200:203], a[76:79], v[92:95]\n"
"v_sub_u32 v186, v186, v188\n"
"v_cvt_pk_bf16_f32 v145, v146, v147\n"
"v_mfma_f32_16x16x32_bf16 v[92:95], v[204:207], a[92:95], v[92:95]\n"
"buffer_load_dwordx4 a[152:155], v212, s[36:39], s73 offen offset:2048 sc1\n"
"v_mfma_f32_16x16x32_bf16 v[88:91], v[204:207], a[88:91], v[88:91]\n"
"ds_read2_b32 a[252:253], v186 offset0:0 offset1:1\n"
"v_cvt_pk_bf16_f32 v152, v152, v153\n"
"v_mfma_f32_16x16x32_bf16 v[88:91], v[200:203], a[72:75], v[88:91]\n"
"v_cvt_pk_bf16_f32 v153, v154, v155\n"
"v_mfma_f32_16x16x32_bf16 v[132:135], a[236:239], v[224:227], 0\n"
"Lpad1_%=:\n"
"s_cbranch_execz Lpad5_%=\n"
"v_mfma_f32_16x16x32_bf16 v[132:135], a[244:247], v[236:239], v[132:135]\n"
"v_min_u32 v183, 272, v212\n"
"v_lshlrev_b32 v183, 3, v183\n"
"v_mfma_f32_16x16x32_bf16 v[84:87], v[200:203], a[68:71], v[84:87]\n"
"s_mul_i32 s91, s61, 9216\n"
"s_add_u32 s91, s91, 108544\n"
"v_lshrrev_b32 v218, 7, v212\n"
"v_mfma_f32_16x16x32_bf16 v[84:87], v[204:207], a[84:87], v[84:87]\n"
"v_mfma_f32_16x16x32_bf16 v[140:143], a[236:239], v[228:231], 0\n"
"v_mfma_f32_16x16x32_bf16 v[140:143], a[244:247], v[240:243], v[140:143]\n"
"v_mul_u32_u24 v218, 1152, v218\n"
"v_lshrrev_b32 v182, 8, v212\n"
"v_mfma_f32_16x16x32_bf16 v[80:83], v[204:207], a[80:83], v[80:83]\n"
"ds_read_b64_tr_b16 v[204:205], v211 offset:35840\n"
"ds_read_b64_tr_b16 v[206:207], v211 offset:36352\n"
"v_mfma_f32_16x16x32_bf16 v[80:83], v[200:203], a[64:67], v[80:83]\n"
"ds_read_b64_tr_b16 v[200:201], v211 offset:44032\n"
"ds_read_b64_tr_b16 v[202:203], v211 offset:44544\n"
"v_mfma_f32_16x16x32_bf16 v[148:151], a[236:239], v[232:235], 0\n"
"v_mfma_f32_16x16x32_bf16 v[148:151], a[244:247], v[244:247], v[148:151]\n"
"s_waitcnt lgkmcnt(4)\n"
"v_mad_u32_u24 v218, v182, s31, v218\n"
"v_lshrrev_b32 v182, 2, v212\n"
"v_mfma_f32_16x16x32_bf16 v[96:99], v[196:199], a[64:67], v[96:99]\n"
"buffer_load_dwordx4 a[156:159], v212, s[36:39], s74 offen offset:2048 sc1\n"
"v_mfma_f32_16x16x32_bf16 v[96:99], v[192:195], a[80:83], v[96:99]\n"
"s_add_u32 s73, s73, 4096\n"
"ds_read2_b32 a[254:255], v186 offset0:2 offset1:3\n"
"v_xor_b32 v182, v182, v212\n"
"v_mfma_f32_16x16x32_bf16 v[100:103], v[192:195], a[84:87], v[100:103]\n"
"s_add_u32 s74, s74, 4096\n"
"v_add_u32 v186, 1024, v186\n"
"v_and_b32 v182, 64, v182\n"
"v_mfma_f32_16x16x32_bf16 v[100:103], v[196:199], a[68:71], v[100:103]\n"
"v_and_b32 v179, 48, v212\n"
"v_add3_u32 v218, v218, v182, v179\n"
"v_mfma_f32_16x16x32_bf16 v[104:107], v[196:199], a[72:75], v[104:107]\n"
"buffer_load_dwordx4 v213, s[40:43], s71 offen offset:0 lds\n"
"v_mfma_f32_16x16x32_bf16 v[104:107], v[192:195], a[88:91], v[104:107]\n"
"s_lshl_b32 s94, 1, s94\n"
"v_add_u32 v208, 0xc000, v208\n"
"s_and_b32 s66, s99, 1\n"
"v_mfma_f32_16x16x32_bf16 v[108:111], v[192:195], a[92:95], v[108:111]\n"
"s_mul_i32 s66, s66, s94\n"
"ds_read_b64_tr_b16 v[192:193], v208 offset:2048\n"
"ds_read_b64_tr_b16 v[194:195], v208 offset:2560\n"
"v_mfma_f32_16x16x32_bf16 v[108:111], v[196:199], a[76:79], v[108:111]\n"
"s_waitcnt lgkmcnt(3)\n"
"ds_read_b64_tr_b16 v[196:197], v208 offset:10240\n"
"ds_read_b64_tr_b16 v[198:199], v208 offset:10752\n"
"v_mfma_f32_16x16x32_bf16 v[124:127], v[204:207], a[76:79], v[124:127]\n"
"s_waitcnt lgkmcnt(4)\n"
"buffer_load_dwordx4 v213, s[44:47], s71 offen offset:1024 lds\n"
"v_mfma_f32_16x16x32_bf16 v[156:159], a[252:255], v[248:251], 0\n"
"s_lshl_b32 s66, s66, 2\n"
"s_lshl3_add_u32 s96, s94, s97\n"
"s_lshl2_add_u32 s98, s94, s97\n"
"v_mfma_f32_16x16x32_bf16 v[124:127], v[200:203], a[92:95], v[124:127]\n"
"s_add_u32 m0, s70, 92160\n"
"s_add_u32 s71, s72, 3244160\n"
"v_mul_f32 v132, 0x3fb8aa3b, v132\n"
"v_mfma_f32_16x16x32_bf16 v[120:123], v[200:203], a[88:91], v[120:123]\n"
"s_sub_u32 s98, s98, s66\n"
"v_mul_f32 v133, 0x3fb8aa3b, v133\n"
"v_mul_f32 v134, 0x3fb8aa3b, v134\n"
"v_mfma_f32_16x16x32_bf16 v[120:123], v[204:207], a[72:75], v[120:123]\n"
"s_add_u32 s97, s97, s66\n"
"v_mul_f32 v135, 0x3fb8aa3b, v135\n"
"v_cvt_pk_bf16_f32 v130, v132, v133\n"
"v_mfma_f32_16x16x32_bf16 v[116:119], v[204:207], a[68:71], v[116:119]\n"
"s_and_b32 s67, s64, 3\n"
"v_cvt_pk_bf16_f32 v131, v134, v135\n"
"s_lshl2_add_u32 s66, s62, s67\n"
"v_mfma_f32_16x16x32_bf16 v[116:119], v[200:203], a[84:87], v[116:119]\n"
"buffer_load_dwordx4 v213, s[40:43], s71 offen offset:0 lds\n"
"v_mfma_f32_16x16x32_bf16 v[112:115], v[200:203], a[80:83], v[112:115]\n"
"s_lshl_b32 s99, s66, 13\n"
"ds_read_b64_tr_b16 v[200:201], v208 offset:3072\n"
"ds_read_b64_tr_b16 v[202:203], v208 offset:3584\n"
"v_mfma_f32_16x16x32_bf16 v[112:115], v[204:207], a[64:67], v[112:115]\n"
"s_waitcnt vmcnt(20) lgkmcnt(2)\n"
"ds_read_b64_tr_b16 v[204:205], v208 offset:11264\n"
"ds_read_b64_tr_b16 v[206:207], v208 offset:11776\n"
"v_mfma_f32_16x16x32_bf16 v[0:3], v[192:195], a[96:99], v[0:3]\n"
"s_waitcnt vmcnt(16)\n"
"buffer_store_dwordx4 v[128:131], v176, s[36:39], s78 offen offset:0 sc1\n"
"v_mfma_f32_16x16x32_bf16 v[0:3], v[196:199], a[112:115], v[0:3]\n"
"s_waitcnt vmcnt(16)\n"
"v_mul_f32 v140, 0x3fb8aa3b, v140\n"
"v_mul_f32 v141, 0x3fb8aa3b, v141\n"
"v_mfma_f32_16x16x32_bf16 v[4:7], v[196:199], a[116:119], v[4:7]\n"
"s_add_u32 s99, s99, 44302336\n"
"v_mul_f32 v142, 0x3fb8aa3b, v142\n"
"v_mul_f32 v143, 0x3fb8aa3b, v143\n"
"v_mfma_f32_16x16x32_bf16 v[4:7], v[192:195], a[100:103], v[4:7]\n"
"s_lshl3_add_u32 s68, s62, s63\n"
"v_cvt_pk_bf16_f32 v138, v140, v141\n"
"v_cvt_pk_bf16_f32 v139, v142, v143\n"
"v_mfma_f32_16x16x32_bf16 v[8:11], v[192:195], a[104:107], v[8:11]\n"
"s_waitcnt vmcnt(15)\n"
"buffer_store_dwordx4 v[136:139], v176, s[36:39], s78 offen offset:1024 sc1\n"
"v_mfma_f32_16x16x32_bf16 v[8:11], v[196:199], a[120:123], v[8:11]\n"
"s_waitcnt vmcnt(15)\n"
"v_add_u32 v209, 0xc000, v209\n"
"v_mul_f32 v148, v177, v148\n"
"v_mfma_f32_16x16x32_bf16 v[12:15], v[196:199], a[124:127], v[12:15]\n"
"s_lshl3_add_u32 s66, s68, s61\n"
"ds_read_b64_tr_b16 v[196:197], v209 offset:2048\n"
"ds_read_b64_tr_b16 v[198:199], v209 offset:2560\n"
"v_mfma_f32_16x16x32_bf16 v[12:15], v[192:195], a[108:111], v[12:15]\n"
"s_waitcnt lgkmcnt(2)\n"
"ds_read_b64_tr_b16 v[192:193], v209 offset:10240\n"
"ds_read_b64_tr_b16 v[194:195], v209 offset:10752\n"
"v_mfma_f32_16x16x32_bf16 v[28:31], v[200:203], a[108:111], v[28:31]\n"
"s_mul_i32 s66, s66, 83968\n"
"v_mul_f32 v149, v177, v149\n"
"v_mul_f32 v150, v177, v150\n"
"v_mfma_f32_16x16x32_bf16 v[28:31], v[204:207], a[124:127], v[28:31]\n"
"s_add_u32 s66, s66, 12189696\n"
"v_mul_f32 v151, v177, v151\n"
"v_cvt_pk_bf16_f32 v146, v148, v149\n"
"v_mfma_f32_16x16x32_bf16 v[24:27], v[204:207], a[120:123], v[24:27]\n"
"buffer_load_dwordx4 v213, s[44:47], s71 offen offset:1024 lds\n"
"v_mfma_f32_16x16x32_bf16 v[24:27], v[200:203], a[104:107], v[24:27]\n"
"s_add_u32 m0, s70, 2048\n"
"s_add_u32 s71, s72, 3539072\n"
"v_cvt_pk_bf16_f32 v147, v150, v151\n"
"v_mfma_f32_16x16x32_bf16 v[20:23], v[200:203], a[100:103], v[20:23]\n"
"buffer_store_dwordx4 v[144:147], v176, s[36:39], s78 offen offset:2048 sc1\n"
"v_mfma_f32_16x16x32_bf16 v[20:23], v[204:207], a[116:119], v[20:23]\n"
"s_lshr_b32 s67, s64, 2\n"
"v_cvt_pk_bf16_f32 v154, v156, v157\n"
"v_cvt_pk_bf16_f32 v155, v158, v159\n"
"v_mfma_f32_16x16x32_bf16 v[16:19], v[204:207], a[112:115], v[16:19]\n"
"s_mul_i32 s67, s67, 335872\n"
"ds_read_b64_tr_b16 v[204:205], v209 offset:3072\n"
"ds_read_b64_tr_b16 v[206:207], v209 offset:3584\n"
"v_mfma_f32_16x16x32_bf16 v[16:19], v[200:203], a[96:99], v[16:19]\n"
"s_waitcnt lgkmcnt(2)\n"
"ds_read_b64_tr_b16 v[200:201], v209 offset:11264\n"
"ds_read_b64_tr_b16 v[202:203], v209 offset:11776\n"
"v_mfma_f32_16x16x32_bf16 v[32:35], v[196:199], a[96:99], v[32:35]\n"
"buffer_store_dwordx4 v[152:155], v176, s[36:39], s78 offen offset:3072 sc1\n"
"v_mfma_f32_16x16x32_bf16 v[32:35], v[192:195], a[112:115], v[32:35]\n"
"s_lshl_b32 s78, s65, 12\n"
"v_lshrrev_b32 v176, 2, v176\n"
"v_and_b32 v176, 0x1000007f, v176\n"
"v_mfma_f32_16x16x32_bf16 v[36:39], v[192:195], a[116:119], v[36:39]\n"
"s_add_u32 s78, s78, 44171264\n"
"s_add_u32 s20, s66, s67\n"
"s_lshl_b32 s67, s64, 12\n"
"v_mfma_f32_16x16x32_bf16 v[36:39], v[196:199], a[100:103], v[36:39]\n"
"s_add_u32 s8, s66, s67\n"
"s_add_u32 s9, s8, 335872\n"
"s_lshl_b32 s67, s64, 11\n"
"v_mfma_f32_16x16x32_bf16 v[40:43], v[196:199], a[104:107], v[40:43]\n"
"s_add_u32 s67, s67, s66\n"
"s_add_u32 s10, s67, 40960\n"
"s_add_u32 s11, s10, 335872\n"
"v_mfma_f32_16x16x32_bf16 v[40:43], v[192:195], a[120:123], v[40:43]\n"
"s_add_u32 s12, s67, 57344\n"
"v_add_u32 v210, 0xc000, v210\n"
"s_add_u32 s13, s12, 335872\n"
"v_mfma_f32_16x16x32_bf16 v[44:47], v[192:195], a[124:127], v[44:47]\n"
"s_lshl_b32 s67, s64, 7\n"
"ds_read_b64_tr_b16 v[192:193], v210 offset:2048\n"
"ds_read_b64_tr_b16 v[194:195], v210 offset:2560\n"
"v_mfma_f32_16x16x32_bf16 v[44:47], v[196:199], a[108:111], v[44:47]\n"
"s_waitcnt lgkmcnt(2)\n"
"ds_read_b64_tr_b16 v[196:197], v210 offset:10240\n"
"ds_read_b64_tr_b16 v[198:199], v210 offset:10752\n"
"v_mfma_f32_16x16x32_bf16 v[60:63], v[204:207], a[108:111], v[60:63]\n"
"buffer_load_dwordx4 a[160:163], v212, s[36:39], s73 offen offset:0 sc1\n"
"v_mfma_f32_16x16x32_bf16 v[60:63], v[200:203], a[124:127], v[60:63]\n"
"s_add_u32 s67, s67, s66\n"
"s_add_u32 s18, s67, 81920\n"
"s_add_u32 s19, s18, 335872\n"
"v_mfma_f32_16x16x32_bf16 v[56:59], v[200:203], a[120:123], v[56:59]\n"
"s_lshr_b32 s67, s64, 1\n"
"s_lshl_b32 s67, s67, 11\n"
"s_add_u32 s66, s66, s67\n"
"v_mfma_f32_16x16x32_bf16 v[56:59], v[204:207], a[104:107], v[56:59]\n"
"s_and_b32 s67, s64, 1\n"
"s_lshl_b32 s67, s67, 9\n"
"s_add_u32 s66, s66, s67\n"
"v_mfma_f32_16x16x32_bf16 v[52:55], v[204:207], a[100:103], v[52:55]\n"
"buffer_load_dwordx4 a[164:167], v212, s[36:39], s74 offen offset:0 sc1\n"
"v_mfma_f32_16x16x32_bf16 v[52:55], v[200:203], a[116:119], v[52:55]\n"
"s_add_u32 s14, s66, 32768\n"
"s_add_u32 s15, s14, 335872\n"
"s_add_u32 s16, s66, 73728\n"
"v_mfma_f32_16x16x32_bf16 v[48:51], v[200:203], a[112:115], v[48:51]\n"
"s_add_u32 s17, s16, 335872\n"
"ds_read_b64_tr_b16 v[200:201], v210 offset:3072\n"
"ds_read_b64_tr_b16 v[202:203], v210 offset:3584\n"
"v_mfma_f32_16x16x32_bf16 v[48:51], v[204:207], a[96:99], v[48:51]\n"
"s_waitcnt lgkmcnt(2)\n"
"ds_read_b64_tr_b16 v[204:205], v210 offset:11264\n"
"ds_read_b64_tr_b16 v[206:207], v210 offset:11776\n"
"v_mfma_f32_16x16x32_bf16 v[64:67], v[192:195], a[96:99], v[64:67]\n"
"buffer_load_dwordx4 a[168:171], v212, s[36:39], s73 offen offset:2048 sc1\n"
"v_mfma_f32_16x16x32_bf16 v[64:67], v[196:199], a[112:115], v[64:67]\n"
"s_lshl_b32 s66, s68, 5\n"
"s_add_u32 s66, s66, s61\n"
"s_lshl_b32 s66, s66, 10\n"
"v_mfma_f32_16x16x32_bf16 v[68:71], v[196:199], a[116:119], v[68:71]\n"
"s_add_u32 s28, s66, 43122688\n"
"s_lshl_b32 s67, s64, 12\n"
"s_add_u32 s23, s28, s67\n"
"v_mfma_f32_16x16x32_bf16 v[68:71], v[192:195], a[100:103], v[68:71]\n"
"s_lshl_b32 s66, s62, 18\n"
"s_add_u32 s67, s67, s66\n"
"s_add_u32 s30, s67, 43122688\n"
"v_mfma_f32_16x16x32_bf16 v[72:75], v[192:195], a[104:107], v[72:75]\n"
"s_sub_u32 s29, s23, s66\n"
"s_lshl_b32 s66, s65, 15\n"
"s_add_u32 s22, s66, 33685504\n"
"v_mfma_f32_16x16x32_bf16 v[72:75], v[196:199], a[120:123], v[72:75]\n"
"s_lshl_b32 s67, s63, 12\n"
"v_add_u32 v211, 0xc000, v211\n"
"s_add_u32 s21, s22, s67\n"
"v_mfma_f32_16x16x32_bf16 v[76:79], v[196:199], a[124:127], v[76:79]\n"
"s_lshl_b32 s66, s61, 3\n"
"ds_read_b64_tr_b16 v[196:197], v211 offset:2048\n"
"ds_read_b64_tr_b16 v[198:199], v211 offset:2560\n"
"v_mfma_f32_16x16x32_bf16 v[76:79], v[192:195], a[108:111], v[76:79]\n"
"s_waitcnt lgkmcnt(2)\n"
"ds_read_b64_tr_b16 v[192:193], v211 offset:10240\n"
"ds_read_b64_tr_b16 v[194:195], v211 offset:10752\n"
"v_mfma_f32_16x16x32_bf16 v[92:95], v[200:203], a[108:111], v[92:95]\n"
"buffer_load_dwordx4 a[172:175], v212, s[36:39], s74 offen offset:2048 sc1\n"
"v_mfma_f32_16x16x32_bf16 v[92:95], v[204:207], a[124:127], v[92:95]\n"
"s_add_u32 s73, s73, 4096\n"
"s_add_u32 s66, s66, 108544\n"
"s_lshl_b32 s67, s61, 10\n"
"v_mfma_f32_16x16x32_bf16 v[88:91], v[204:207], a[120:123], v[88:91]\n"
"s_add_u32 s74, s74, 4096\n"
"s_lshl_b32 s68, s61, 8\n"
"s_add_u32 s68, s68, 2304\n"
"v_mfma_f32_16x16x32_bf16 v[88:91], v[200:203], a[104:107], v[88:91]\n"
"s_cmp_lt_u32 s61, 2\n"
"s_cselect_b32 s85, 63, 15\n"
"s_cselect_b32 s86, s66, 108608\n"
"s_cselect_b32 s87, 48, 0\n"
"s_cselect_b32 s5, 3, 7\n"
"v_mfma_f32_16x16x32_bf16 v[84:87], v[200:203], a[100:103], v[84:87]\n"
"buffer_load_dwordx4 a[176:179], v212, s[36:39], s73 offen offset:0 sc1\n"
"v_mfma_f32_16x16x32_bf16 v[84:87], v[204:207], a[116:119], v[84:87]\n"
"s_cmp_lt_u32 s61, 3\n"
"s_cselect_b32 s88, s67, 0x40000000\n"
"s_cselect_b32 s89, s68, 0x40000000\n"
"v_mfma_f32_16x16x32_bf16 v[80:83], v[204:207], a[112:115], v[80:83]\n"
"ds_read_b64_tr_b16 v[204:205], v211 offset:3072\n"
"ds_read_b64_tr_b16 v[206:207], v211 offset:3584\n"
"v_mfma_f32_16x16x32_bf16 v[80:83], v[200:203], a[96:99], v[80:83]\n"
"s_waitcnt lgkmcnt(2)\n"
"ds_read_b64_tr_b16 v[200:201], v211 offset:11264\n"
"ds_read_b64_tr_b16 v[202:203], v211 offset:11776\n"
"v_mfma_f32_16x16x32_bf16 v[96:99], v[196:199], a[96:99], v[96:99]\n"
"buffer_load_dwordx4 a[180:183], v212, s[36:39], s74 offen offset:0 sc1\n"
"v_mfma_f32_16x16x32_bf16 v[96:99], v[192:195], a[112:115], v[96:99]\n"
"v_bfe_u32 v178, v212, 4, 4\n"
"s_lshl_b32 s66, s61, 6\n"
"v_mfma_f32_16x16x32_bf16 v[100:103], v[192:195], a[116:119], v[100:103]\n"
"v_add_u32 v178, s66, v178\n"
"v_mul_u32_u24 v215, 80, v178\n"
"v_mfma_f32_16x16x32_bf16 v[100:103], v[196:199], a[100:103], v[100:103]\n"
"v_lshrrev_b32 v216, 9, v212\n"
"v_mul_u32_u24 v216, 32768, v216\n"
"v_mfma_f32_16x16x32_bf16 v[104:107], v[196:199], a[104:107], v[104:107]\n"
"v_sub_u32 v215, v215, v216\n"
"v_add_u32 v215, 141312, v215\n"
"v_mfma_f32_16x16x32_bf16 v[104:107], v[192:195], a[120:123], v[104:107]\n"
"buffer_load_dwordx4 a[184:187], v212, s[36:39], s73 offen offset:2048 sc1\n"
"v_mfma_f32_16x16x32_bf16 v[108:111], v[192:195], a[124:127], v[108:111]\n"
"ds_read_b64_tr_b16 v[192:193], v208 offset:18432\n"
"ds_read_b64_tr_b16 v[194:195], v208 offset:18944\n"
"v_mfma_f32_16x16x32_bf16 v[108:111], v[196:199], a[108:111], v[108:111]\n"
"s_waitcnt lgkmcnt(2)\n"
"ds_read_b64_tr_b16 v[196:197], v208 offset:26624\n"
"ds_read_b64_tr_b16 v[198:199], v208 offset:27136\n"
"v_mfma_f32_16x16x32_bf16 v[124:127], v[204:207], a[108:111], v[124:127]\n"
"v_bfe_u32 v217, v212, 8, 1\n"
"v_lshl_add_u32 v216, v217, 2, v215\n"
"v_mfma_f32_16x16x32_bf16 v[124:127], v[200:203], a[124:127], v[124:127]\n"
"buffer_load_dwordx4 a[188:191], v212, s[36:39], s74 offen offset:2048 sc1\n"
"v_mfma_f32_16x16x32_bf16 v[120:123], v[200:203], a[120:123], v[120:123]\n"
"s_add_u32 s73, s73, 4096\n"
"v_lshl_add_u32 v215, v217, 5, v215\n"
"v_mul_u32_u24 v217, 48, v178\n"
"v_mfma_f32_16x16x32_bf16 v[120:123], v[204:207], a[104:107], v[120:123]\n"
"s_add_u32 s74, s74, 4096\n"
"v_add_u32 v217, 129024, v217\n"
"v_lshrrev_b32 v185, 8, v212\n"
"v_mfma_f32_16x16x32_bf16 v[116:119], v[204:207], a[100:103], v[116:119]\n"
"v_lshl_add_u32 v217, v185, 3, v217\n"
"v_mfma_f32_16x16x32_bf16 v[116:119], v[200:203], a[116:119], v[116:119]\n"
"v_mfma_f32_16x16x32_bf16 v[112:115], v[200:203], a[112:115], v[112:115]\n"
"ds_read_b64_tr_b16 v[200:201], v208 offset:19456\n"
"ds_read_b64_tr_b16 v[202:203], v208 offset:19968\n"
"v_mfma_f32_16x16x32_bf16 v[112:115], v[204:207], a[96:99], v[112:115]\n"
"s_waitcnt vmcnt(24) lgkmcnt(2)\n"
"ds_read_b64_tr_b16 v[204:205], v208 offset:27648\n"
"ds_read_b64_tr_b16 v[206:207], v208 offset:28160\n"
"v_mfma_f32_16x16x32_bf16 v[0:3], v[192:195], a[128:131], v[0:3]\n"
"s_waitcnt vmcnt(19)\n"
"buffer_load_dwordx4 v213, s[40:43], s71 offen offset:0 lds\n"
"v_mfma_f32_16x16x32_bf16 v[0:3], v[196:199], a[144:147], v[0:3]\n"
"s_waitcnt vmcnt(19)\n"
"v_mfma_f32_16x16x32_bf16 v[4:7], v[196:199], a[148:151], v[4:7]\n"
"v_mfma_f32_16x16x32_bf16 v[4:7], v[192:195], a[132:135], v[4:7]\n"
"v_mfma_f32_16x16x32_bf16 v[8:11], v[192:195], a[136:139], v[8:11]\n"
"s_waitcnt vmcnt(18)\n"
"buffer_load_dwordx4 v213, s[44:47], s71 offen offset:1024 lds\n"
"v_mfma_f32_16x16x32_bf16 v[8:11], v[196:199], a[152:155], v[8:11]\n"
"s_waitcnt vmcnt(18)\n"
"s_add_u32 m0, s70, 10240\n"
"s_add_u32 s71, s72, 3833984\n"
"v_mfma_f32_16x16x32_bf16 v[12:15], v[196:199], a[156:159], v[12:15]\n"
"ds_read_b64_tr_b16 v[196:197], v209 offset:18432\n"
"ds_read_b64_tr_b16 v[198:199], v209 offset:18944\n"
"v_mfma_f32_16x16x32_bf16 v[12:15], v[192:195], a[140:143], v[12:15]\n"
"s_waitcnt lgkmcnt(2)\n"
"ds_read_b64_tr_b16 v[192:193], v209 offset:26624\n"
"ds_read_b64_tr_b16 v[194:195], v209 offset:27136\n"
"v_mfma_f32_16x16x32_bf16 v[28:31], v[200:203], a[140:143], v[28:31]\n"
"buffer_load_dwordx4 v213, s[40:43], s71 offen offset:0 lds\n"
"v_mfma_f32_16x16x32_bf16 v[28:31], v[204:207], a[156:159], v[28:31]\n"
"v_mfma_f32_16x16x32_bf16 v[24:27], v[204:207], a[152:155], v[24:27]\n"
"s_waitcnt vmcnt(11)\n"
"buffer_store_dword v219, v176, s[36:39], s80 offen offset:0 sc1\n"
"v_mfma_f32_16x16x32_bf16 v[24:27], v[200:203], a[136:139], v[24:27]\n"
"v_mfma_f32_16x16x32_bf16 v[20:23], v[200:203], a[132:135], v[20:23]\n"
"v_mfma_f32_16x16x32_bf16 v[20:23], v[204:207], a[148:151], v[20:23]\n"
"v_mfma_f32_16x16x32_bf16 v[16:19], v[204:207], a[144:147], v[16:19]\n"
"ds_read_b64_tr_b16 v[204:205], v209 offset:19456\n"
"ds_read_b64_tr_b16 v[206:207], v209 offset:19968\n"
"v_mfma_f32_16x16x32_bf16 v[16:19], v[200:203], a[128:131], v[16:19]\n"
"s_waitcnt lgkmcnt(2)\n"
"ds_read_b64_tr_b16 v[200:201], v209 offset:27648\n"
"ds_read_b64_tr_b16 v[202:203], v209 offset:28160\n"
"v_mfma_f32_16x16x32_bf16 v[32:35], v[196:199], a[128:131], v[32:35]\n"
"buffer_load_dwordx4 v213, s[44:47], s71 offen offset:1024 lds\n"
"v_mfma_f32_16x16x32_bf16 v[32:35], v[192:195], a[144:147], v[32:35]\n"
"v_mfma_f32_16x16x32_bf16 v[36:39], v[192:195], a[148:151], v[36:39]\n"
"v_mfma_f32_16x16x32_bf16 v[36:39], v[196:199], a[132:135], v[36:39]\n"
"v_mfma_f32_16x16x32_bf16 v[40:43], v[196:199], a[136:139], v[40:43]\n"
"buffer_load_dwordx4 a[192:195], v212, s[36:39], s73 offen offset:0 sc1\n"
"v_mfma_f32_16x16x32_bf16 v[40:43], v[192:195], a[152:155], v[40:43]\n"
"v_mfma_f32_16x16x32_bf16 v[44:47], v[192:195], a[156:159], v[44:47]\n"
"ds_read_b64_tr_b16 v[192:193], v210 offset:18432\n"
"ds_read_b64_tr_b16 v[194:195], v210 offset:18944\n"
"v_mfma_f32_16x16x32_bf16 v[44:47], v[196:199], a[140:143], v[44:47]\n"
"s_waitcnt lgkmcnt(2)\n"
"ds_read_b64_tr_b16 v[196:197], v210 offset:26624\n"
"ds_read_b64_tr_b16 v[198:199], v210 offset:27136\n"
"v_mfma_f32_16x16x32_bf16 v[60:63], v[204:207], a[140:143], v[60:63]\n"
"buffer_load_dwordx4 a[196:199], v212, s[36:39], s74 offen offset:0 sc1\n"
"v_mfma_f32_16x16x32_bf16 v[60:63], v[200:203], a[156:159], v[60:63]\n"
"v_mfma_f32_16x16x32_bf16 v[56:59], v[200:203], a[152:155], v[56:59]\n"
"v_mfma_f32_16x16x32_bf16 v[56:59], v[204:207], a[136:139], v[56:59]\n"
"v_mfma_f32_16x16x32_bf16 v[52:55], v[204:207], a[132:135], v[52:55]\n"
"v_mfma_f32_16x16x32_bf16 v[52:55], v[200:203], a[148:151], v[52:55]\n"
"buffer_load_dwordx4 a[200:203], v212, s[36:39], s73 offen offset:2048 sc1\n"
"v_mfma_f32_16x16x32_bf16 v[48:51], v[200:203], a[144:147], v[48:51]\n"
"ds_read_b64_tr_b16 v[200:201], v210 offset:19456\n"
"ds_read_b64_tr_b16 v[202:203], v210 offset:19968\n"
"v_mfma_f32_16x16x32_bf16 v[48:51], v[204:207], a[128:131], v[48:51]\n"
"s_waitcnt lgkmcnt(2)\n"
"ds_read_b64_tr_b16 v[204:205], v210 offset:27648\n"
"ds_read_b64_tr_b16 v[206:207], v210 offset:28160\n"
"v_mfma_f32_16x16x32_bf16 v[64:67], v[192:195], a[128:131], v[64:67]\n"
"v_mfma_f32_16x16x32_bf16 v[64:67], v[196:199], a[144:147], v[64:67]\n"
"buffer_load_dwordx4 a[204:207], v212, s[36:39], s74 offen offset:2048 sc1\n"
"v_mfma_f32_16x16x32_bf16 v[68:71], v[196:199], a[148:151], v[68:71]\n"
"s_add_u32 s73, s73, 4096\n"
"v_mfma_f32_16x16x32_bf16 v[68:71], v[192:195], a[132:135], v[68:71]\n"
"s_add_u32 s74, s74, 4096\n"
"v_mfma_f32_16x16x32_bf16 v[72:75], v[192:195], a[136:139], v[72:75]\n"
"v_mfma_f32_16x16x32_bf16 v[72:75], v[196:199], a[152:155], v[72:75]\n"
"ds_write_b32 v214, v219 offset:32\n"
"v_mfma_f32_16x16x32_bf16 v[76:79], v[196:199], a[156:159], v[76:79]\n"
"ds_read_b64_tr_b16 v[196:197], v211 offset:18432\n"
"ds_read_b64_tr_b16 v[198:199], v211 offset:18944\n"
"v_mfma_f32_16x16x32_bf16 v[76:79], v[192:195], a[140:143], v[76:79]\n"
"s_waitcnt lgkmcnt(3)\n"
"ds_read_b64_tr_b16 v[192:193], v211 offset:26624\n"
"ds_read_b64_tr_b16 v[194:195], v211 offset:27136\n"
"v_mfma_f32_16x16x32_bf16 v[92:95], v[200:203], a[140:143], v[92:95]\n"
"buffer_load_dwordx4 a[208:211], v212, s[36:39], s73 offen offset:0 sc1\n"
"v_mfma_f32_16x16x32_bf16 v[92:95], v[204:207], a[156:159], v[92:95]\n"
"v_mfma_f32_16x16x32_bf16 v[88:91], v[204:207], a[152:155], v[88:91]\n"
"v_mfma_f32_16x16x32_bf16 v[88:91], v[200:203], a[136:139], v[88:91]\n"
"v_mfma_f32_16x16x32_bf16 v[84:87], v[200:203], a[132:135], v[84:87]\n"
"buffer_load_dwordx4 a[212:215], v212, s[36:39], s74 offen offset:0 sc1\n"
"v_mfma_f32_16x16x32_bf16 v[84:87], v[204:207], a[148:151], v[84:87]\n"
"ds_read_b32 v221, v220 offset:32\n"
"v_mfma_f32_16x16x32_bf16 v[80:83], v[204:207], a[144:147], v[80:83]\n"
"ds_read_b64_tr_b16 v[204:205], v211 offset:19456\n"
"ds_read_b64_tr_b16 v[206:207], v211 offset:19968\n"
"v_mfma_f32_16x16x32_bf16 v[80:83], v[200:203], a[128:131], v[80:83]\n"
"s_waitcnt lgkmcnt(3)\n"
"ds_read_b64_tr_b16 v[200:201], v211 offset:27648\n"
"ds_read_b64_tr_b16 v[202:203], v211 offset:28160\n"
"v_mfma_f32_16x16x32_bf16 v[96:99], v[196:199], a[128:131], v[96:99]\n"
"buffer_load_dwordx4 a[216:219], v212, s[36:39], s73 offen offset:2048 sc1\n"
"v_mfma_f32_16x16x32_bf16 v[96:99], v[192:195], a[144:147], v[96:99]\n"
"v_mfma_f32_16x16x32_bf16 v[100:103], v[192:195], a[148:151], v[100:103]\n"
"s_waitcnt lgkmcnt(4)\n"
"v_cmp_ne_u32 vcc, s75, v221\n"
"v_mfma_f32_16x16x32_bf16 v[100:103], v[196:199], a[132:135], v[100:103]\n"
"v_mfma_f32_16x16x32_bf16 v[104:107], v[196:199], a[136:139], v[104:107]\n"
"s_cbranch_vccnz Lsb1_%=\n"
"Lsr1_%=:\n"
"s_add_u32 m0, s70, 18432\n"
"v_mfma_f32_16x16x32_bf16 v[104:107], v[192:195], a[152:155], v[104:107]\n"
"buffer_load_dwordx4 a[220:223], v212, s[36:39], s74 offen offset:2048 sc1\n"
"v_mfma_f32_16x16x32_bf16 v[108:111], v[192:195], a[156:159], v[108:111]\n"
"s_add_u32 s71, s72, 4128896\n"
"ds_read_b64_tr_b16 v[192:193], v208 offset:34816\n"
"ds_read_b64_tr_b16 v[194:195], v208 offset:35328\n"
"v_mfma_f32_16x16x32_bf16 v[108:111], v[196:199], a[140:143], v[108:111]\n"
"s_waitcnt lgkmcnt(2)\n"
"ds_read_b64_tr_b16 v[196:197], v208 offset:43008\n"
"ds_read_b64_tr_b16 v[198:199], v208 offset:43520\n"
"v_mfma_f32_16x16x32_bf16 v[124:127], v[204:207], a[140:143], v[124:127]\n"
"s_add_u32 s73, s73, 4096\n"
"v_mfma_f32_16x16x32_bf16 v[124:127], v[200:203], a[156:159], v[124:127]\n"
"buffer_load_dwordx4 v213, s[40:43], s71 offen offset:0 lds\n"
"v_mfma_f32_16x16x32_bf16 v[120:123], v[200:203], a[152:155], v[120:123]\n"
"s_add_u32 s74, s74, 4096\n"
"v_mfma_f32_16x16x32_bf16 v[120:123], v[204:207], a[136:139], v[120:123]\n"
"v_mfma_f32_16x16x32_bf16 v[116:119], v[204:207], a[132:135], v[116:119]\n"
"v_mfma_f32_16x16x32_bf16 v[116:119], v[200:203], a[148:151], v[116:119]\n"
"buffer_load_dwordx4 v213, s[44:47], s71 offen offset:1024 lds\n"
"v_mfma_f32_16x16x32_bf16 v[112:115], v[200:203], a[144:147], v[112:115]\n"
"s_add_u32 m0, s70, 26624\n"
"ds_read_b64_tr_b16 v[200:201], v208 offset:35840\n"
"ds_read_b64_tr_b16 v[202:203], v208 offset:36352\n"
"v_mfma_f32_16x16x32_bf16 v[112:115], v[204:207], a[128:131], v[112:115]\n"
"s_waitcnt vmcnt(22) lgkmcnt(2)\n"
"ds_read_b64_tr_b16 v[204:205], v208 offset:44032\n"
"ds_read_b64_tr_b16 v[206:207], v208 offset:44544\n"
"v_mfma_f32_16x16x32_bf16 v[0:3], v[192:195], a[160:163], v[0:3]\n"
"s_waitcnt vmcnt(18)\n"
"s_add_u32 s71, s72, 4423808\n"
"v_mfma_f32_16x16x32_bf16 v[0:3], v[196:199], a[176:179], v[0:3]\n"
"s_waitcnt vmcnt(17)\n"
"v_mfma_f32_16x16x32_bf16 v[4:7], v[196:199], a[180:183], v[4:7]\n"
"buffer_load_dwordx4 v213, s[40:43], s71 offen offset:0 lds\n"
"v_mfma_f32_16x16x32_bf16 v[4:7], v[192:195], a[164:167], v[4:7]\n"
"v_mfma_f32_16x16x32_bf16 v[8:11], v[192:195], a[168:171], v[8:11]\n"
"s_waitcnt vmcnt(17)\n"
"v_mfma_f32_16x16x32_bf16 v[8:11], v[196:199], a[184:187], v[8:11]\n"
"s_waitcnt vmcnt(16)\n"
"v_mfma_f32_16x16x32_bf16 v[12:15], v[196:199], a[188:191], v[12:15]\n"
"ds_read_b64_tr_b16 v[196:197], v209 offset:34816\n"
"ds_read_b64_tr_b16 v[198:199], v209 offset:35328\n"
"v_mfma_f32_16x16x32_bf16 v[12:15], v[192:195], a[172:175], v[12:15]\n"
"s_waitcnt lgkmcnt(2)\n"
"ds_read_b64_tr_b16 v[192:193], v209 offset:43008\n"
"ds_read_b64_tr_b16 v[194:195], v209 offset:43520\n"
"v_mfma_f32_16x16x32_bf16 v[28:31], v[200:203], a[172:175], v[28:31]\n"
"buffer_load_dwordx4 v213, s[44:47], s71 offen offset:1024 lds\n"
"v_mfma_f32_16x16x32_bf16 v[28:31], v[204:207], a[188:191], v[28:31]\n"
"s_add_u32 m0, s70, 34816\n"
"s_add_u32 s71, s72, 4718720\n"
"v_mfma_f32_16x16x32_bf16 v[24:27], v[204:207], a[184:187], v[24:27]\n"
"v_mfma_f32_16x16x32_bf16 v[24:27], v[200:203], a[168:171], v[24:27]\n"
"v_mfma_f32_16x16x32_bf16 v[20:23], v[200:203], a[164:167], v[20:23]\n"
"buffer_load_dwordx4 a[224:227], v212, s[36:39], s73 offen offset:0 sc1\n"
"v_mfma_f32_16x16x32_bf16 v[20:23], v[204:207], a[180:183], v[20:23]\n"
"v_mfma_f32_16x16x32_bf16 v[16:19], v[204:207], a[176:179], v[16:19]\n"
"ds_read_b64_tr_b16 v[204:205], v209 offset:35840\n"
"ds_read_b64_tr_b16 v[206:207], v209 offset:36352\n"
"v_mfma_f32_16x16x32_bf16 v[16:19], v[200:203], a[160:163], v[16:19]\n"
"s_waitcnt lgkmcnt(2)\n"
"ds_read_b64_tr_b16 v[200:201], v209 offset:44032\n"
"ds_read_b64_tr_b16 v[202:203], v209 offset:44544\n"
"v_mfma_f32_16x16x32_bf16 v[32:35], v[196:199], a[160:163], v[32:35]\n"
"buffer_load_dwordx4 a[228:231], v212, s[36:39], s74 offen offset:0 sc1\n"
"v_mfma_f32_16x16x32_bf16 v[32:35], v[192:195], a[176:179], v[32:35]\n"
"v_mfma_f32_16x16x32_bf16 v[36:39], v[192:195], a[180:183], v[36:39]\n"
"v_mfma_f32_16x16x32_bf16 v[36:39], v[196:199], a[164:167], v[36:39]\n"
"v_mfma_f32_16x16x32_bf16 v[40:43], v[196:199], a[168:171], v[40:43]\n"
"v_mfma_f32_16x16x32_bf16 v[40:43], v[192:195], a[184:187], v[40:43]\n"
"buffer_load_dwordx4 a[232:235], v212, s[36:39], s73 offen offset:2048 sc1\n"
"v_mfma_f32_16x16x32_bf16 v[44:47], v[192:195], a[188:191], v[44:47]\n"
"ds_read_b64_tr_b16 v[192:193], v210 offset:34816\n"
"ds_read_b64_tr_b16 v[194:195], v210 offset:35328\n"
"v_mfma_f32_16x16x32_bf16 v[44:47], v[196:199], a[172:175], v[44:47]\n"
"s_waitcnt lgkmcnt(2)\n"
"ds_read_b64_tr_b16 v[196:197], v210 offset:43008\n"
"ds_read_b64_tr_b16 v[198:199], v210 offset:43520\n"
"v_mfma_f32_16x16x32_bf16 v[60:63], v[204:207], a[172:175], v[60:63]\n"
"v_mfma_f32_16x16x32_bf16 v[60:63], v[200:203], a[188:191], v[60:63]\n"
"buffer_load_dwordx4 a[236:239], v212, s[36:39], s74 offen offset:2048 sc1\n"
"v_mfma_f32_16x16x32_bf16 v[56:59], v[200:203], a[184:187], v[56:59]\n"
"s_add_u32 s73, s73, 4096\n"
"v_mfma_f32_16x16x32_bf16 v[56:59], v[204:207], a[168:171], v[56:59]\n"
"s_add_u32 s74, s74, 4096\n"
"v_mfma_f32_16x16x32_bf16 v[52:55], v[204:207], a[164:167], v[52:55]\n"
"v_mfma_f32_16x16x32_bf16 v[52:55], v[200:203], a[180:183], v[52:55]\n"
"buffer_load_dwordx4 a[240:243], v212, s[36:39], s73 offen offset:0 sc1\n"
"v_mfma_f32_16x16x32_bf16 v[48:51], v[200:203], a[176:179], v[48:51]\n"
"ds_read_b64_tr_b16 v[200:201], v210 offset:35840\n"
"ds_read_b64_tr_b16 v[202:203], v210 offset:36352\n"
"v_mfma_f32_16x16x32_bf16 v[48:51], v[204:207], a[160:163], v[48:51]\n"
"s_waitcnt lgkmcnt(2)\n"
"ds_read_b64_tr_b16 v[204:205], v210 offset:44032\n"
"ds_read_b64_tr_b16 v[206:207], v210 offset:44544\n"
"v_mfma_f32_16x16x32_bf16 v[64:67], v[192:195], a[160:163], v[64:67]\n"
"v_mfma_f32_16x16x32_bf16 v[64:67], v[196:199], a[176:179], v[64:67]\n"
"v_mfma_f32_16x16x32_bf16 v[68:71], v[196:199], a[180:183], v[68:71]\n"
"buffer_load_dwordx4 a[244:247], v212, s[36:39], s74 offen offset:0 sc1\n"
"v_mfma_f32_16x16x32_bf16 v[68:71], v[192:195], a[164:167], v[68:71]\n"
"v_mfma_f32_16x16x32_bf16 v[72:75], v[192:195], a[168:171], v[72:75]\n"
"v_mfma_f32_16x16x32_bf16 v[72:75], v[196:199], a[184:187], v[72:75]\n"
"s_waitcnt vmcnt(18)\n"
"ds_write_b32 v214, v219 offset:64\n"
"v_mfma_f32_16x16x32_bf16 v[76:79], v[196:199], a[188:191], v[76:79]\n"
"ds_read_b64_tr_b16 v[196:197], v211 offset:34816\n"
"ds_read_b64_tr_b16 v[198:199], v211 offset:35328\n"
"v_mfma_f32_16x16x32_bf16 v[76:79], v[192:195], a[172:175], v[76:79]\n"
"s_waitcnt lgkmcnt(3)\n"
"ds_read_b64_tr_b16 v[192:193], v211 offset:43008\n"
"ds_read_b64_tr_b16 v[194:195], v211 offset:43520\n"
"v_mfma_f32_16x16x32_bf16 v[92:95], v[200:203], a[172:175], v[92:95]\n"
"buffer_load_dwordx4 a[248:251], v212, s[36:39], s73 offen offset:2048 sc1\n"
"v_mfma_f32_16x16x32_bf16 v[92:95], v[204:207], a[188:191], v[92:95]\n"
"v_mfma_f32_16x16x32_bf16 v[88:91], v[204:207], a[184:187], v[88:91]\n"
"v_mfma_f32_16x16x32_bf16 v[88:91], v[200:203], a[168:171], v[88:91]\n"
"v_mfma_f32_16x16x32_bf16 v[84:87], v[200:203], a[164:167], v[84:87]\n"
"buffer_load_dwordx4 a[252:255], v212, s[36:39], s74 offen offset:2048 sc1\n"
"v_mfma_f32_16x16x32_bf16 v[84:87], v[204:207], a[180:183], v[84:87]\n"
"s_add_u32 s73, s73, 4096\n"
"ds_read_b32 v221, v220 offset:64\n"
"v_mfma_f32_16x16x32_bf16 v[80:83], v[204:207], a[176:179], v[80:83]\n"
"s_add_u32 s74, s74, 4096\n"
"ds_read_b64_tr_b16 v[204:205], v211 offset:35840\n"
"ds_read_b64_tr_b16 v[206:207], v211 offset:36352\n"
"v_mfma_f32_16x16x32_bf16 v[80:83], v[200:203], a[160:163], v[80:83]\n"
"s_waitcnt lgkmcnt(3)\n"
"ds_read_b64_tr_b16 v[200:201], v211 offset:44032\n"
"ds_read_b64_tr_b16 v[202:203], v211 offset:44544\n"
"v_mfma_f32_16x16x32_bf16 v[96:99], v[196:199], a[160:163], v[96:99]\n"
"buffer_load_dwordx4 v213, s[40:43], s71 offen offset:0 lds\n"
"v_mfma_f32_16x16x32_bf16 v[96:99], v[192:195], a[176:179], v[96:99]\n"
"v_mfma_f32_16x16x32_bf16 v[100:103], v[192:195], a[180:183], v[100:103]\n"
"s_waitcnt lgkmcnt(4)\n"
"v_cmp_ne_u32 vcc, s75, v221\n"
"v_mfma_f32_16x16x32_bf16 v[100:103], v[196:199], a[164:167], v[100:103]\n"
"v_mfma_f32_16x16x32_bf16 v[104:107], v[196:199], a[168:171], v[104:107]\n"
"s_cbranch_vccnz Lsb2_%=\n"
"Lsr2_%=:\n"
"v_mfma_f32_16x16x32_bf16 v[104:107], v[192:195], a[184:187], v[104:107]\n"
"v_add_u32 v208, 0xffff4000, v208\n"
"v_mfma_f32_16x16x32_bf16 v[108:111], v[192:195], a[188:191], v[108:111]\n"
"ds_read_b64_tr_b16 v[192:193], v208 offset:2048\n"
"ds_read_b64_tr_b16 v[194:195], v208 offset:2560\n"
"v_mfma_f32_16x16x32_bf16 v[108:111], v[196:199], a[172:175], v[108:111]\n"
"s_waitcnt lgkmcnt(2)\n"
"ds_read_b64_tr_b16 v[196:197], v208 offset:10240\n"
"ds_read_b64_tr_b16 v[198:199], v208 offset:10752\n"
"v_mfma_f32_16x16x32_bf16 v[124:127], v[204:207], a[172:175], v[124:127]\n"
"buffer_load_dwordx4 v213, s[44:47], s71 offen offset:1024 lds\n"
"v_mfma_f32_16x16x32_bf16 v[124:127], v[200:203], a[188:191], v[124:127]\n"
"s_add_u32 m0, s70, 43008\n"
"s_add_u32 s71, s72, 5013632\n"
"v_mfma_f32_16x16x32_bf16 v[120:123], v[200:203], a[184:187], v[120:123]\n"
"v_mfma_f32_16x16x32_bf16 v[120:123], v[204:207], a[168:171], v[120:123]\n"
"Lpad2_%=:\n"
"s_cbranch_execz Lpad6_%=\n"
"v_mfma_f32_16x16x32_bf16 v[116:119], v[204:207], a[164:167], v[116:119]\n"
"buffer_load_dwordx4 v213, s[40:43], s71 offen offset:0 lds\n"
"v_mfma_f32_16x16x32_bf16 v[116:119], v[200:203], a[180:183], v[116:119]\n"
"v_mfma_f32_16x16x32_bf16 v[112:115], v[200:203], a[176:179], v[112:115]\n"
"ds_read_b64_tr_b16 v[200:201], v208 offset:3072\n"
"ds_read_b64_tr_b16 v[202:203], v208 offset:3584\n"
"v_mfma_f32_16x16x32_bf16 v[112:115], v[204:207], a[160:163], v[112:115]\n"
"s_waitcnt vmcnt(22) lgkmcnt(2)\n"
"ds_read_b64_tr_b16 v[204:205], v208 offset:11264\n"
"ds_read_b64_tr_b16 v[206:207], v208 offset:11776\n"
"v_mfma_f32_16x16x32_bf16 v[0:3], v[192:195], a[192:195], v[0:3]\n"
"s_waitcnt vmcnt(18)\n"
"buffer_load_dwordx4 v213, s[44:47], s71 offen offset:1024 lds\n"
"v_mfma_f32_16x16x32_bf16 v[0:3], v[196:199], a[208:211], v[0:3]\n"
"s_waitcnt vmcnt(18)\n"
"v_mfma_f32_16x16x32_bf16 v[4:7], v[196:199], a[212:215], v[4:7]\n"
"buffer_load_dword v184, v183, s[36:39], s78 offen offset:0 sc1\n"
"v_mfma_f32_16x16x32_bf16 v[4:7], v[192:195], a[196:199], v[4:7]\n"
"v_mfma_f32_16x16x32_bf16 v[8:11], v[192:195], a[200:203], v[8:11]\n"
"s_waitcnt vmcnt(18)\n"
"v_mfma_f32_16x16x32_bf16 v[8:11], v[196:199], a[216:219], v[8:11]\n"
"s_waitcnt vmcnt(17)\n"
"v_add_u32 v209, 0xffff4000, v209\n"
"v_mfma_f32_16x16x32_bf16 v[12:15], v[196:199], a[220:223], v[12:15]\n"
"ds_read_b64_tr_b16 v[196:197], v209 offset:2048\n"
"ds_read_b64_tr_b16 v[198:199], v209 offset:2560\n"
"v_mfma_f32_16x16x32_bf16 v[12:15], v[192:195], a[204:207], v[12:15]\n"
"s_waitcnt lgkmcnt(2)\n"
"ds_read_b64_tr_b16 v[192:193], v209 offset:10240\n"
"ds_read_b64_tr_b16 v[194:195], v209 offset:10752\n"
"v_mfma_f32_16x16x32_bf16 v[28:31], v[200:203], a[204:207], v[28:31]\n"
"v_mfma_f32_16x16x32_bf16 v[28:31], v[204:207], a[220:223], v[28:31]\n"
"v_mfma_f32_16x16x32_bf16 v[24:27], v[204:207], a[216:219], v[24:27]\n"
"v_mfma_f32_16x16x32_bf16 v[24:27], v[200:203], a[200:203], v[24:27]\n"
"v_mfma_f32_16x16x32_bf16 v[20:23], v[200:203], a[196:199], v[20:23]\n"
"v_mfma_f32_16x16x32_bf16 v[20:23], v[204:207], a[212:215], v[20:23]\n"
"v_mfma_f32_16x16x32_bf16 v[16:19], v[204:207], a[208:211], v[16:19]\n"
"ds_read_b64_tr_b16 v[204:205], v209 offset:3072\n"
"ds_read_b64_tr_b16 v[206:207], v209 offset:3584\n"
"v_mfma_f32_16x16x32_bf16 v[16:19], v[200:203], a[192:195], v[16:19]\n"
"s_waitcnt lgkmcnt(2)\n"
"ds_read_b64_tr_b16 v[200:201], v209 offset:11264\n"
"ds_read_b64_tr_b16 v[202:203], v209 offset:11776\n"
"v_mfma_f32_16x16x32_bf16 v[32:35], v[196:199], a[192:195], v[32:35]\n"
"buffer_load_dwordx4 v[224:227], v212, s[36:39], s73 offen offset:0 sc1\n"
"v_mfma_f32_16x16x32_bf16 v[32:35], v[192:195], a[208:211], v[32:35]\n"
"v_mfma_f32_16x16x32_bf16 v[36:39], v[192:195], a[212:215], v[36:39]\n"
"v_mfma_f32_16x16x32_bf16 v[36:39], v[196:199], a[196:199], v[36:39]\n"
"v_mfma_f32_16x16x32_bf16 v[40:43], v[196:199], a[200:203], v[40:43]\n"
"buffer_load_dwordx4 v[228:231], v212, s[36:39], s74 offen offset:0 sc1\n"
"v_mfma_f32_16x16x32_bf16 v[40:43], v[192:195], a[216:219], v[40:43]\n"
"v_add_u32 v210, 0xffff4000, v210\n"
"v_mfma_f32_16x16x32_bf16 v[44:47], v[192:195], a[220:223], v[44:47]\n"
"ds_read_b64_tr_b16 v[192:193], v210 offset:2048\n"
"ds_read_b64_tr_b16 v[194:195], v210 offset:2560\n"
"v_mfma_f32_16x16x32_bf16 v[44:47], v[196:199], a[204:207], v[44:47]\n"
"s_waitcnt lgkmcnt(2)\n"
"ds_read_b64_tr_b16 v[196:197], v210 offset:10240\n"
"ds_read_b64_tr_b16 v[198:199], v210 offset:10752\n"
"v_mfma_f32_16x16x32_bf16 v[60:63], v[204:207], a[204:207], v[60:63]\n"
"buffer_load_dwordx4 v[232:235], v212, s[36:39], s73 offen offset:2048 sc1\n"
"v_mfma_f32_16x16x32_bf16 v[60:63], v[200:203], a[220:223], v[60:63]\n"
"v_mfma_f32_16x16x32_bf16 v[56:59], v[200:203], a[216:219], v[56:59]\n"
"v_mfma_f32_16x16x32_bf16 v[56:59], v[204:207], a[200:203], v[56:59]\n"
"v_mfma_f32_16x16x32_bf16 v[52:55], v[204:207], a[196:199], v[52:55]\n"
"v_mfma_f32_16x16x32_bf16 v[52:55], v[200:203], a[212:215], v[52:55]\n"
"buffer_load_dwordx4 v[236:239], v212, s[36:39], s74 offen offset:2048 sc1\n"
"v_mfma_f32_16x16x32_bf16 v[48:51], v[200:203], a[208:211], v[48:51]\n"
"s_add_u32 s73, s73, 4096\n"
"ds_read_b64_tr_b16 v[200:201], v210 offset:3072\n"
"ds_read_b64_tr_b16 v[202:203], v210 offset:3584\n"
"v_mfma_f32_16x16x32_bf16 v[48:51], v[204:207], a[192:195], v[48:51]\n"
"s_waitcnt lgkmcnt(2)\n"
"ds_read_b64_tr_b16 v[204:205], v210 offset:11264\n"
"ds_read_b64_tr_b16 v[206:207], v210 offset:11776\n"
"v_mfma_f32_16x16x32_bf16 v[64:67], v[192:195], a[192:195], v[64:67]\n"
"s_add_u32 s74, s74, 4096\n"
"v_mfma_f32_16x16x32_bf16 v[64:67], v[196:199], a[208:211], v[64:67]\n"
"buffer_load_dwordx4 v[240:243], v212, s[36:39], s73 offen offset:0 sc1\n"
"v_mfma_f32_16x16x32_bf16 v[68:71], v[196:199], a[212:215], v[68:71]\n"
"v_mfma_f32_16x16x32_bf16 v[68:71], v[192:195], a[196:199], v[68:71]\n"
"v_mfma_f32_16x16x32_bf16 v[72:75], v[192:195], a[200:203], v[72:75]\n"
"s_waitcnt vmcnt(18)\n"
"ds_write_b32 v214, v219 offset:96\n"
"v_mfma_f32_16x16x32_bf16 v[72:75], v[196:199], a[216:219], v[72:75]\n"
"v_add_u32 v211, 0xffff4000, v211\n"
"v_mfma_f32_16x16x32_bf16 v[76:79], v[196:199], a[220:223], v[76:79]\n"
"ds_read_b64_tr_b16 v[196:197], v211 offset:2048\n"
"ds_read_b64_tr_b16 v[198:199], v211 offset:2560\n"
"v_mfma_f32_16x16x32_bf16 v[76:79], v[192:195], a[204:207], v[76:79]\n"
"s_waitcnt lgkmcnt(3)\n"
"ds_read_b64_tr_b16 v[192:193], v211 offset:10240\n"
"ds_read_b64_tr_b16 v[194:195], v211 offset:10752\n"
"v_mfma_f32_16x16x32_bf16 v[92:95], v[200:203], a[204:207], v[92:95]\n"
"buffer_load_dwordx4 v[244:247], v212, s[36:39], s74 offen offset:0 sc1\n"
"v_mfma_f32_16x16x32_bf16 v[92:95], v[204:207], a[220:223], v[92:95]\n"
"v_mfma_f32_16x16x32_bf16 v[88:91], v[204:207], a[216:219], v[88:91]\n"
"v_mfma_f32_16x16x32_bf16 v[88:91], v[200:203], a[200:203], v[88:91]\n"
"v_mfma_f32_16x16x32_bf16 v[84:87], v[200:203], a[196:199], v[84:87]\n"
"buffer_load_dwordx4 v[248:251], v212, s[36:39], s73 offen offset:2048 sc1\n"
"v_mfma_f32_16x16x32_bf16 v[84:87], v[204:207], a[212:215], v[84:87]\n"
"ds_read_b32 v221, v220 offset:96\n"
"v_mfma_f32_16x16x32_bf16 v[80:83], v[204:207], a[208:211], v[80:83]\n"
"ds_read_b64_tr_b16 v[204:205], v211 offset:3072\n"
"ds_read_b64_tr_b16 v[206:207], v211 offset:3584\n"
"v_mfma_f32_16x16x32_bf16 v[80:83], v[200:203], a[192:195], v[80:83]\n"
"s_waitcnt lgkmcnt(3)\n"
"ds_read_b64_tr_b16 v[200:201], v211 offset:11264\n"
"ds_read_b64_tr_b16 v[202:203], v211 offset:11776\n"
"v_mfma_f32_16x16x32_bf16 v[96:99], v[196:199], a[192:195], v[96:99]\n"
"buffer_load_dwordx4 v[252:255], v212, s[36:39], s74 offen offset:2048 sc1\n"
"v_mfma_f32_16x16x32_bf16 v[96:99], v[192:195], a[208:211], v[96:99]\n"
"v_mfma_f32_16x16x32_bf16 v[100:103], v[192:195], a[212:215], v[100:103]\n"
"s_waitcnt lgkmcnt(4)\n"
"v_cmp_ne_u32 vcc, s75, v221\n"
"v_mfma_f32_16x16x32_bf16 v[100:103], v[196:199], a[196:199], v[100:103]\n"
"v_mfma_f32_16x16x32_bf16 v[104:107], v[196:199], a[200:203], v[104:107]\n"
"s_cbranch_vccnz Lsb3_%=\n"
"Lsr3_%=:\n"
"v_mfma_f32_16x16x32_bf16 v[104:107], v[192:195], a[216:219], v[104:107]\n"
"s_waitcnt vmcnt(8)\n"
"v_cmp_ne_u32 s[92:93], s75, v184\n"
"v_mfma_f32_16x16x32_bf16 v[108:111], v[192:195], a[220:223], v[108:111]\n"
"ds_read_b64_tr_b16 v[192:193], v208 offset:18432\n"
"ds_read_b64_tr_b16 v[194:195], v208 offset:18944\n"
"v_mfma_f32_16x16x32_bf16 v[108:111], v[196:199], a[204:207], v[108:111]\n"
"s_waitcnt lgkmcnt(2)\n"
"ds_read_b64_tr_b16 v[196:197], v208 offset:26624\n"
"ds_read_b64_tr_b16 v[198:199], v208 offset:27136\n"
"v_mfma_f32_16x16x32_bf16 v[124:127], v[204:207], a[204:207], v[124:127]\n"
"s_cmp_lg_u64 s[92:93], 0\n"
"s_cbranch_scc1 Lslow0_%=\n"
"Lback0_%=:\n"
"v_mfma_f32_16x16x32_bf16 v[124:127], v[200:203], a[220:223], v[124:127]\n"
"s_add_u32 m0, s70, 51200\n"
"s_add_u32 s71, s81, 0\n"
"v_mfma_f32_16x16x32_bf16 v[120:123], v[200:203], a[216:219], v[120:123]\n"
"buffer_load_dwordx4 v212, s[36:39], s71 offen offset:0 sc1 lds\n"
"v_mfma_f32_16x16x32_bf16 v[120:123], v[204:207], a[200:203], v[120:123]\n"
"v_mfma_f32_16x16x32_bf16 v[116:119], v[204:207], a[196:199], v[116:119]\n"
"v_mfma_f32_16x16x32_bf16 v[116:119], v[200:203], a[212:215], v[116:119]\n"
"v_mfma_f32_16x16x32_bf16 v[112:115], v[200:203], a[208:211], v[112:115]\n"
"ds_read_b64_tr_b16 v[200:201], v208 offset:19456\n"
"ds_read_b64_tr_b16 v[202:203], v208 offset:19968\n"
"v_mfma_f32_16x16x32_bf16 v[112:115], v[204:207], a[192:195], v[112:115]\n"
"s_waitcnt lgkmcnt(2)\n"
"ds_read_b64_tr_b16 v[204:205], v208 offset:27648\n"
"ds_read_b64_tr_b16 v[206:207], v208 offset:28160\n"
"v_mfma_f32_16x16x32_bf16 v[0:3], v[192:195], a[224:227], v[0:3]\n"
"buffer_load_dwordx4 v212, s[36:39], s71 offen offset:1024 sc1 lds\n"
"v_mfma_f32_16x16x32_bf16 v[0:3], v[196:199], a[240:243], v[0:3]\n"
"s_add_u32 m0, s70, 59392\n"
"s_add_u32 s71, s81, 8192\n"
"v_mfma_f32_16x16x32_bf16 v[4:7], v[196:199], a[244:247], v[4:7]\n"
"v_mfma_f32_16x16x32_bf16 v[4:7], v[192:195], a[228:231], v[4:7]\n"
"v_mfma_f32_16x16x32_bf16 v[8:11], v[192:195], a[232:235], v[8:11]\n"
"buffer_load_dwordx4 v212, s[36:39], s71 offen offset:0 sc1 lds\n"
"v_mfma_f32_16x16x32_bf16 v[8:11], v[196:199], a[248:251], v[8:11]\n"
"v_mfma_f32_16x16x32_bf16 v[12:15], v[196:199], a[252:255], v[12:15]\n"
"ds_read_b64_tr_b16 v[196:197], v209 offset:18432\n"
"ds_read_b64_tr_b16 v[198:199], v209 offset:18944\n"
"v_mfma_f32_16x16x32_bf16 v[12:15], v[192:195], a[236:239], v[12:15]\n"
"s_waitcnt lgkmcnt(2)\n"
"ds_read_b64_tr_b16 v[192:193], v209 offset:26624\n"
"ds_read_b64_tr_b16 v[194:195], v209 offset:27136\n"
"v_mfma_f32_16x16x32_bf16 v[28:31], v[200:203], a[236:239], v[28:31]\n"
"buffer_load_dwordx4 v212, s[36:39], s71 offen offset:1024 sc1 lds\n"
"v_mfma_f32_16x16x32_bf16 v[28:31], v[204:207], a[252:255], v[28:31]\n"
"s_add_u32 m0, s70, 67584\n"
"s_add_u32 s71, s81, 16384\n"
"v_mfma_f32_16x16x32_bf16 v[24:27], v[204:207], a[248:251], v[24:27]\n"
"v_mfma_f32_16x16x32_bf16 v[24:27], v[200:203], a[232:235], v[24:27]\n"
"v_mfma_f32_16x16x32_bf16 v[20:23], v[200:203], a[228:231], v[20:23]\n"
"v_mfma_f32_16x16x32_bf16 v[20:23], v[204:207], a[244:247], v[20:23]\n"
"buffer_load_dwordx4 v212, s[36:39], s71 offen offset:0 sc1 lds\n"
"v_mfma_f32_16x16x32_bf16 v[16:19], v[204:207], a[240:243], v[16:19]\n"
"ds_read_b64_tr_b16 v[204:205], v209 offset:19456\n"
"ds_read_b64_tr_b16 v[206:207], v209 offset:19968\n"
"v_mfma_f32_16x16x32_bf16 v[16:19], v[200:203], a[224:227], v[16:19]\n"
"s_waitcnt lgkmcnt(2)\n"
"ds_read_b64_tr_b16 v[200:201], v209 offset:27648\n"
"ds_read_b64_tr_b16 v[202:203], v209 offset:28160\n"
"v_mfma_f32_16x16x32_bf16 v[32:35], v[196:199], a[224:227], v[32:35]\n"
"v_mfma_f32_16x16x32_bf16 v[32:35], v[192:195], a[240:243], v[32:35]\n"
"buffer_load_dwordx4 v212, s[36:39], s71 offen offset:1024 sc1 lds\n"
"v_mfma_f32_16x16x32_bf16 v[36:39], v[192:195], a[244:247], v[36:39]\n"
"s_add_u32 m0, s91, 0\n"
"v_mfma_f32_16x16x32_bf16 v[36:39], v[196:199], a[228:231], v[36:39]\n"
"v_mfma_f32_16x16x32_bf16 v[40:43], v[196:199], a[232:235], v[40:43]\n"
"v_mfma_f32_16x16x32_bf16 v[40:43], v[192:195], a[248:251], v[40:43]\n"
"buffer_load_dwordx4 v218, s[24:27], s90 offen offset:0 lds\n"
"v_mfma_f32_16x16x32_bf16 v[44:47], v[192:195], a[252:255], v[44:47]\n"
"s_add_u32 m0, s91, 896\n"
"ds_read_b64_tr_b16 v[192:193], v210 offset:18432\n"
"ds_read_b64_tr_b16 v[194:195], v210 offset:18944\n"
"v_mfma_f32_16x16x32_bf16 v[44:47], v[196:199], a[236:239], v[44:47]\n"
"s_waitcnt lgkmcnt(2)\n"
"ds_read_b64_tr_b16 v[196:197], v210 offset:26624\n"
"ds_read_b64_tr_b16 v[198:199], v210 offset:27136\n"
"v_mfma_f32_16x16x32_bf16 v[60:63], v[204:207], a[236:239], v[60:63]\n"
"v_mfma_f32_16x16x32_bf16 v[60:63], v[200:203], a[252:255], v[60:63]\n"
"v_mfma_f32_16x16x32_bf16 v[56:59], v[200:203], a[248:251], v[56:59]\n"
"v_mfma_f32_16x16x32_bf16 v[56:59], v[204:207], a[232:235], v[56:59]\n"
"v_mfma_f32_16x16x32_bf16 v[52:55], v[204:207], a[228:231], v[52:55]\n"
"v_mfma_f32_16x16x32_bf16 v[52:55], v[200:203], a[244:247], v[52:55]\n"
"v_mfma_f32_16x16x32_bf16 v[48:51], v[200:203], a[240:243], v[48:51]\n"
"ds_read_b64_tr_b16 v[200:201], v210 offset:19456\n"
"ds_read_b64_tr_b16 v[202:203], v210 offset:19968\n"
"v_mfma_f32_16x16x32_bf16 v[48:51], v[204:207], a[224:227], v[48:51]\n"
"s_waitcnt lgkmcnt(2)\n"
"ds_read_b64_tr_b16 v[204:205], v210 offset:27648\n"
"ds_read_b64_tr_b16 v[206:207], v210 offset:28160\n"
"v_mfma_f32_16x16x32_bf16 v[64:67], v[192:195], a[224:227], v[64:67]\n"
"buffer_load_dwordx4 v218, s[24:27], s90 offen offset:128 lds\n"
"v_mfma_f32_16x16x32_bf16 v[64:67], v[196:199], a[240:243], v[64:67]\n"
"s_add_u32 m0, s91, 1792\n"
"v_mfma_f32_16x16x32_bf16 v[68:71], v[196:199], a[244:247], v[68:71]\n"
"v_mfma_f32_16x16x32_bf16 v[68:71], v[192:195], a[228:231], v[68:71]\n"
"v_mfma_f32_16x16x32_bf16 v[72:75], v[192:195], a[232:235], v[72:75]\n"
"v_mfma_f32_16x16x32_bf16 v[72:75], v[196:199], a[248:251], v[72:75]\n"
"ds_write_b32 v214, v219 offset:128\n"
"v_mfma_f32_16x16x32_bf16 v[76:79], v[196:199], a[252:255], v[76:79]\n"
"ds_read_b64_tr_b16 v[196:197], v211 offset:18432\n"
"ds_read_b64_tr_b16 v[198:199], v211 offset:18944\n"
"v_mfma_f32_16x16x32_bf16 v[76:79], v[192:195], a[236:239], v[76:79]\n"
"s_waitcnt lgkmcnt(3)\n"
"ds_read_b64_tr_b16 v[192:193], v211 offset:26624\n"
"ds_read_b64_tr_b16 v[194:195], v211 offset:27136\n"
"v_mfma_f32_16x16x32_bf16 v[92:95], v[200:203], a[236:239], v[92:95]\n"
"buffer_load_dwordx4 v218, s[24:27], s90 offen offset:256 lds\n"
"v_mfma_f32_16x16x32_bf16 v[92:95], v[204:207], a[252:255], v[92:95]\n"
"s_add_u32 m0, s91, 2688\n"
"v_mfma_f32_16x16x32_bf16 v[88:91], v[204:207], a[248:251], v[88:91]\n"
"v_mfma_f32_16x16x32_bf16 v[88:91], v[200:203], a[232:235], v[88:91]\n"
"v_mfma_f32_16x16x32_bf16 v[84:87], v[200:203], a[228:231], v[84:87]\n"
"v_mfma_f32_16x16x32_bf16 v[84:87], v[204:207], a[244:247], v[84:87]\n"
"ds_read_b32 v221, v220 offset:128\n"
"v_mfma_f32_16x16x32_bf16 v[80:83], v[204:207], a[240:243], v[80:83]\n"
"ds_read_b64_tr_b16 v[204:205], v211 offset:19456\n"
"ds_read_b64_tr_b16 v[206:207], v211 offset:19968\n"
"v_mfma_f32_16x16x32_bf16 v[80:83], v[200:203], a[224:227], v[80:83]\n"
"s_waitcnt lgkmcnt(3)\n"
"ds_read_b64_tr_b16 v[200:201], v211 offset:27648\n"
"ds_read_b64_tr_b16 v[202:203], v211 offset:28160\n"
"v_mfma_f32_16x16x32_bf16 v[96:99], v[196:199], a[224:227], v[96:99]\n"
"buffer_load_dwordx4 v218, s[24:27], s90 offen offset:384 lds\n"
"v_mfma_f32_16x16x32_bf16 v[96:99], v[192:195], a[240:243], v[96:99]\n"
"s_add_u32 m0, s91, 3584\n"
"v_mfma_f32_16x16x32_bf16 v[100:103], v[192:195], a[244:247], v[100:103]\n"
"s_waitcnt lgkmcnt(4)\n"
"v_cmp_ne_u32 vcc, s75, v221\n"
"v_mfma_f32_16x16x32_bf16 v[100:103], v[196:199], a[228:231], v[100:103]\n"
"v_mfma_f32_16x16x32_bf16 v[104:107], v[196:199], a[232:235], v[104:107]\n"
"s_cbranch_vccnz Lsb4_%=\n"
"Lsr4_%=:\n"
"v_mfma_f32_16x16x32_bf16 v[104:107], v[192:195], a[248:251], v[104:107]\n"
"v_mfma_f32_16x16x32_bf16 v[108:111], v[192:195], a[252:255], v[108:111]\n"
"ds_read_b64_tr_b16 v[192:193], v208 offset:34816\n"
"ds_read_b64_tr_b16 v[194:195], v208 offset:35328\n"
"v_mfma_f32_16x16x32_bf16 v[108:111], v[196:199], a[236:239], v[108:111]\n"
"s_waitcnt lgkmcnt(2)\n"
"ds_read_b64_tr_b16 v[196:197], v208 offset:43008\n"
"ds_read_b64_tr_b16 v[198:199], v208 offset:43520\n"
"v_mfma_f32_16x16x32_bf16 v[124:127], v[204:207], a[236:239], v[124:127]\n"
"buffer_load_dwordx4 v218, s[24:27], s90 offen offset:512 lds\n"
"v_mfma_f32_16x16x32_bf16 v[124:127], v[200:203], a[252:255], v[124:127]\n"
"s_add_u32 m0, s91, 4480\n"
"v_mfma_f32_16x16x32_bf16 v[120:123], v[200:203], a[248:251], v[120:123]\n"
"v_mfma_f32_16x16x32_bf16 v[120:123], v[204:207], a[232:235], v[120:123]\n"
"v_mfma_f32_16x16x32_bf16 v[116:119], v[204:207], a[228:231], v[116:119]\n"
"v_mfma_f32_16x16x32_bf16 v[116:119], v[200:203], a[244:247], v[116:119]\n"
"v_mfma_f32_16x16x32_bf16 v[112:115], v[200:203], a[240:243], v[112:115]\n"
"ds_read_b64_tr_b16 v[200:201], v208 offset:35840\n"
"ds_read_b64_tr_b16 v[202:203], v208 offset:36352\n"
"v_mfma_f32_16x16x32_bf16 v[112:115], v[204:207], a[224:227], v[112:115]\n"
"s_waitcnt vmcnt(18) lgkmcnt(2)\n"
"ds_read_b64_tr_b16 v[204:205], v208 offset:44032\n"
"ds_read_b64_tr_b16 v[206:207], v208 offset:44544\n"
"v_mfma_f32_16x16x32_bf16 v[0:3], v[192:195], v[224:227], v[0:3]\n"
"s_waitcnt vmcnt(14)\n"
"buffer_load_dwordx4 v218, s[24:27], s90 offen offset:640 lds\n"
"v_mfma_f32_16x16x32_bf16 v[0:3], v[196:199], v[240:243], v[0:3]\n"
"s_waitcnt vmcnt(14)\n"
"s_add_u32 m0, s91, 5376\n"
"v_mfma_f32_16x16x32_bf16 v[4:7], v[196:199], v[244:247], v[4:7]\n"
"v_mfma_f32_16x16x32_bf16 v[4:7], v[192:195], v[228:231], v[4:7]\n"
"v_mfma_f32_16x16x32_bf16 v[8:11], v[192:195], v[232:235], v[8:11]\n"
"s_waitcnt vmcnt(13)\n"
"v_mfma_f32_16x16x32_bf16 v[8:11], v[196:199], v[248:251], v[8:11]\n"
"s_waitcnt vmcnt(12)\n"
"v_mfma_f32_16x16x32_bf16 v[12:15], v[196:199], v[252:255], v[12:15]\n"
"ds_read_b64_tr_b16 v[196:197], v209 offset:34816\n"
"ds_read_b64_tr_b16 v[198:199], v209 offset:35328\n"
"v_mfma_f32_16x16x32_bf16 v[12:15], v[192:195], v[236:239], v[12:15]\n"
"s_waitcnt lgkmcnt(2)\n"
"ds_read_b64_tr_b16 v[192:193], v209 offset:43008\n"
"ds_read_b64_tr_b16 v[194:195], v209 offset:43520\n"
"v_mfma_f32_16x16x32_bf16 v[28:31], v[200:203], v[236:239], v[28:31]\n"
"v_mfma_f32_16x16x32_bf16 v[28:31], v[204:207], v[252:255], v[28:31]\n"
"buffer_load_dwordx4 v218, s[24:27], s90 offen offset:768 lds\n"
"v_mfma_f32_16x16x32_bf16 v[24:27], v[204:207], v[248:251], v[24:27]\n"
"s_add_u32 m0, s91, 6272\n"
"v_mfma_f32_16x16x32_bf16 v[24:27], v[200:203], v[232:235], v[24:27]\n"
"v_mfma_f32_16x16x32_bf16 v[20:23], v[200:203], v[228:231], v[20:23]\n"
"v_mfma_f32_16x16x32_bf16 v[20:23], v[204:207], v[244:247], v[20:23]\n"
"v_mfma_f32_16x16x32_bf16 v[16:19], v[204:207], v[240:243], v[16:19]\n"
"ds_read_b64_tr_b16 v[204:205], v209 offset:35840\n"
"ds_read_b64_tr_b16 v[206:207], v209 offset:36352\n"
"v_mfma_f32_16x16x32_bf16 v[16:19], v[200:203], v[224:227], v[16:19]\n"
"s_waitcnt lgkmcnt(2)\n"
"ds_read_b64_tr_b16 v[200:201], v209 offset:44032\n"
"ds_read_b64_tr_b16 v[202:203], v209 offset:44544\n"
"v_mfma_f32_16x16x32_bf16 v[32:35], v[196:199], v[224:227], v[32:35]\n"
"v_mfma_f32_16x16x32_bf16 v[32:35], v[192:195], v[240:243], v[32:35]\n"
"buffer_load_dwordx4 v218, s[24:27], s90 offen offset:896 lds\n"
"v_mfma_f32_16x16x32_bf16 v[36:39], v[192:195], v[244:247], v[36:39]\n"
"s_add_u32 m0, s91, 7168\n"
"v_mfma_f32_16x16x32_bf16 v[36:39], v[196:199], v[228:231], v[36:39]\n"
"v_mfma_f32_16x16x32_bf16 v[40:43], v[196:199], v[232:235], v[40:43]\n"
"v_mfma_f32_16x16x32_bf16 v[40:43], v[192:195], v[248:251], v[40:43]\n"
"v_mfma_f32_16x16x32_bf16 v[44:47], v[192:195], v[252:255], v[44:47]\n"
"ds_read_b64_tr_b16 v[192:193], v210 offset:34816\n"
"ds_read_b64_tr_b16 v[194:195], v210 offset:35328\n"
"v_mfma_f32_16x16x32_bf16 v[44:47], v[196:199], v[236:239], v[44:47]\n"
"s_waitcnt lgkmcnt(2)\n"
"ds_read_b64_tr_b16 v[196:197], v210 offset:43008\n"
"ds_read_b64_tr_b16 v[198:199], v210 offset:43520\n"
"v_mfma_f32_16x16x32_bf16 v[60:63], v[204:207], v[236:239], v[60:63]\n"
"v_mfma_f32_16x16x32_bf16 v[60:63], v[200:203], v[252:255], v[60:63]\n"
"buffer_load_dwordx4 v218, s[24:27], s90 offen offset:1024 lds\n"
"v_mfma_f32_16x16x32_bf16 v[56:59], v[200:203], v[248:251], v[56:59]\n"
"s_add_u32 m0, s70, 75776\n"
"s_add_u32 s71, s81, 24576\n"
"v_mfma_f32_16x16x32_bf16 v[56:59], v[204:207], v[232:235], v[56:59]\n"
"v_mfma_f32_16x16x32_bf16 v[52:55], v[204:207], v[228:231], v[52:55]\n"
"v_mfma_f32_16x16x32_bf16 v[52:55], v[200:203], v[244:247], v[52:55]\n"
"v_mfma_f32_16x16x32_bf16 v[48:51], v[200:203], v[240:243], v[48:51]\n"
"ds_read_b64_tr_b16 v[200:201], v210 offset:35840\n"
"ds_read_b64_tr_b16 v[202:203], v210 offset:36352\n"
"v_mfma_f32_16x16x32_bf16 v[48:51], v[204:207], v[224:227], v[48:51]\n"
"s_waitcnt lgkmcnt(2)\n"
"ds_read_b64_tr_b16 v[204:205], v210 offset:44032\n"
"ds_read_b64_tr_b16 v[206:207], v210 offset:44544\n"
"v_mfma_f32_16x16x32_bf16 v[64:67], v[192:195], v[224:227], v[64:67]\n"
"v_mfma_f32_16x16x32_bf16 v[64:67], v[196:199], v[240:243], v[64:67]\n"
"buffer_load_dwordx4 v212, s[36:39], s71 offen offset:0 sc1 lds\n"
"v_mfma_f32_16x16x32_bf16 v[68:71], v[196:199], v[244:247], v[68:71]\n"
"v_mfma_f32_16x16x32_bf16 v[68:71], v[192:195], v[228:231], v[68:71]\n"
"v_mfma_f32_16x16x32_bf16 v[72:75], v[192:195], v[232:235], v[72:75]\n"
"v_mfma_f32_16x16x32_bf16 v[72:75], v[196:199], v[248:251], v[72:75]\n"
"s_waitcnt vmcnt(14)\n"
"ds_write_b32 v214, v219 offset:160\n"
"v_mfma_f32_16x16x32_bf16 v[76:79], v[196:199], v[252:255], v[76:79]\n"
"ds_read_b64_tr_b16 v[196:197], v211 offset:34816\n"
"ds_read_b64_tr_b16 v[198:199], v211 offset:35328\n"
"v_mfma_f32_16x16x32_bf16 v[76:79], v[192:195], v[236:239], v[76:79]\n"
"s_waitcnt lgkmcnt(3)\n"
"ds_read_b64_tr_b16 v[192:193], v211 offset:43008\n"
"ds_read_b64_tr_b16 v[194:195], v211 offset:43520\n"
"v_mfma_f32_16x16x32_bf16 v[92:95], v[200:203], v[236:239], v[92:95]\n"
"buffer_load_dwordx4 v212, s[36:39], s71 offen offset:1024 sc1 lds\n"
"v_mfma_f32_16x16x32_bf16 v[92:95], v[204:207], v[252:255], v[92:95]\n"
"s_add_u32 m0, s70, 83968\n"
"s_add_u32 s71, s81, 32768\n"
"v_mfma_f32_16x16x32_bf16 v[88:91], v[204:207], v[248:251], v[88:91]\n"
"v_mfma_f32_16x16x32_bf16 v[88:91], v[200:203], v[232:235], v[88:91]\n"
"v_mfma_f32_16x16x32_bf16 v[84:87], v[200:203], v[228:231], v[84:87]\n"
"buffer_load_dwordx4 v212, s[36:39], s71 offen offset:0 sc1 lds\n"
"v_mfma_f32_16x16x32_bf16 v[84:87], v[204:207], v[244:247], v[84:87]\n"
"ds_read_b32 v221, v220 offset:160\n"
"v_mfma_f32_16x16x32_bf16 v[80:83], v[204:207], v[240:243], v[80:83]\n"
"ds_read_b64_tr_b16 v[204:205], v211 offset:35840\n"
"ds_read_b64_tr_b16 v[206:207], v211 offset:36352\n"
"v_mfma_f32_16x16x32_bf16 v[80:83], v[200:203], v[224:227], v[80:83]\n"
"s_waitcnt lgkmcnt(3)\n"
"ds_read_b64_tr_b16 v[200:201], v211 offset:44032\n"
"ds_read_b64_tr_b16 v[202:203], v211 offset:44544\n"
"v_mfma_f32_16x16x32_bf16 v[96:99], v[196:199], v[224:227], v[96:99]\n"
"buffer_load_dwordx4 v212, s[36:39], s71 offen offset:1024 sc1 lds\n"
"v_mfma_f32_16x16x32_bf16 v[96:99], v[192:195], v[240:243], v[96:99]\n"
"s_add_u32 m0, s70, 92160\n"
"s_add_u32 s71, s81, 40960\n"
"v_mfma_f32_16x16x32_bf16 v[100:103], v[192:195], v[244:247], v[100:103]\n"
"v_mfma_f32_16x16x32_bf16 v[100:103], v[196:199], v[228:231], v[100:103]\n"
"s_waitcnt lgkmcnt(4)\n"
"v_cmp_ne_u32 vcc, s75, v221\n"
"v_mfma_f32_16x16x32_bf16 v[104:107], v[196:199], v[232:235], v[104:107]\n"
"v_mfma_f32_16x16x32_bf16 v[104:107], v[192:195], v[248:251], v[104:107]\n"
"s_cbranch_vccnz Lsb5_%=\n"
"Lsr5_%=:\n"
"v_mfma_f32_16x16x32_bf16 v[108:111], v[192:195], v[252:255], v[108:111]\n"
"v_add_u32 v222, 0xc000, v222\n"
"v_mfma_f32_16x16x32_bf16 v[108:111], v[196:199], v[236:239], v[108:111]\n"
"s_waitcnt lgkmcnt(0)\n"
"ds_read_b128 v[192:195], v222 offset:2048\n"
"ds_read_b128 v[196:199], v222 offset:6144\n"
"v_mfma_f32_16x16x32_bf16 v[124:127], v[204:207], v[236:239], v[124:127]\n"
"buffer_load_dwordx4 v212, s[36:39], s71 offen offset:0 sc1 lds\n"
"v_mfma_f32_16x16x32_bf16 v[124:127], v[200:203], v[252:255], v[124:127]\n"
"v_mfma_f32_16x16x32_bf16 v[120:123], v[200:203], v[248:251], v[120:123]\n"
"v_mfma_f32_16x16x32_bf16 v[120:123], v[204:207], v[232:235], v[120:123]\n"
"v_mfma_f32_16x16x32_bf16 v[116:119], v[204:207], v[228:231], v[116:119]\n"
"buffer_load_dwordx4 v212, s[36:39], s71 offen offset:1024 sc1 lds\n"
"v_mfma_f32_16x16x32_bf16 v[116:119], v[200:203], v[244:247], v[116:119]\n"
"s_add_u32 m0, s70, 2048\n"
"s_add_u32 s71, s81, 49152\n"
"v_mfma_f32_16x16x32_bf16 v[112:115], v[200:203], v[240:243], v[112:115]\n"
"v_mfma_f32_16x16x32_bf16 v[112:115], v[204:207], v[224:227], v[112:115]\n"
"s_waitcnt lgkmcnt(0)\n"
"ds_read_b128 v[200:203], v222 offset:3072\n"
"ds_read_b128 v[204:207], v222 offset:7168\n"
"v_mfma_f32_16x16x32_bf16 v[128:131], v[192:195], a[0:3], 0\n"
"buffer_load_dwordx4 v212, s[36:39], s71 offen offset:0 sc1 lds\n"
"v_mfma_f32_16x16x32_bf16 v[128:131], v[196:199], a[16:19], v[128:131]\n"
"v_max_f32 v0, 0, v0\n"
"v_max_f32 v1, 0, v1\n"
"v_mfma_f32_16x16x32_bf16 v[144:147], v[196:199], a[20:23], 0\n"
"v_max_f32 v2, 0, v2\n"
"v_max_f32 v3, 0, v3\n"
"v_mfma_f32_16x16x32_bf16 v[144:147], v[192:195], a[4:7], v[144:147]\n"
"v_mul_f32 v16, v0, v16\n"
"v_mul_f32 v17, v1, v17\n"
"v_mfma_f32_16x16x32_bf16 v[160:163], v[192:195], a[8:11], 0\n"
"v_mul_f32 v18, v2, v18\n"
"v_mul_f32 v19, v3, v19\n"
"v_mfma_f32_16x16x32_bf16 v[160:163], v[196:199], a[24:27], v[160:163]\n"
"buffer_load_dwordx4 v212, s[36:39], s71 offen offset:1024 sc1 lds\n"
"v_mfma_f32_16x16x32_bf16 v[176:179], v[196:199], a[28:31], 0\n"
"s_add_u32 m0, s70, 10240\n"
"s_add_u32 s71, s81, 57344\n"
"v_cvt_pk_bf16_f32 v0, v16, v17\n"
"v_mfma_f32_16x16x32_bf16 v[176:179], v[192:195], a[12:15], v[176:179]\n"
"s_waitcnt lgkmcnt(0)\n"
"ds_read_b128 v[196:199], v222 offset:4096\n"
"ds_read_b128 v[192:195], v222 offset:8192\n"
"v_mfma_f32_16x16x32_bf16 v[180:183], v[200:203], a[12:15], 0\n"
"s_waitcnt vmcnt(17)\n"
"ds_write_b32 v214, v219 offset:192\n"
"v_mfma_f32_16x16x32_bf16 v[180:183], v[204:207], a[28:31], v[180:183]\n"
"v_cvt_pk_bf16_f32 v1, v18, v19\n"
"v_max_f32 v32, 0, v32\n"
"v_mfma_f32_16x16x32_bf16 v[164:167], v[204:207], a[24:27], 0\n"
"v_max_f32 v33, 0, v33\n"
"v_max_f32 v34, 0, v34\n"
"v_mfma_f32_16x16x32_bf16 v[164:167], v[200:203], a[8:11], v[164:167]\n"
"buffer_load_dwordx4 v212, s[36:39], s71 offen offset:0 sc1 lds\n"
"v_mfma_f32_16x16x32_bf16 v[148:151], v[200:203], a[4:7], 0\n"
"v_max_f32 v35, 0, v35\n"
"v_mul_f32 v48, v32, v48\n"
"v_mfma_f32_16x16x32_bf16 v[148:151], v[204:207], a[20:23], v[148:151]\n"
"v_mul_f32 v49, v33, v49\n"
"v_mul_f32 v50, v34, v50\n"
"v_mfma_f32_16x16x32_bf16 v[132:135], v[204:207], a[16:19], 0\n"
"ds_read_b32 v221, v220 offset:192\n"
"v_mul_f32 v51, v35, v51\n"
"v_mfma_f32_16x16x32_bf16 v[132:135], v[200:203], a[0:3], v[132:135]\n"
"s_waitcnt lgkmcnt(2)\n"
"ds_read_b128 v[204:207], v222 offset:5120\n"
"ds_read_b128 v[200:203], v222 offset:9216\n"
"v_mfma_f32_16x16x32_bf16 v[136:139], v[196:199], a[0:3], 0\n"
"v_cvt_pk_bf16_f32 v2, v48, v49\n"
"v_cvt_pk_bf16_f32 v3, v50, v51\n"
"v_mfma_f32_16x16x32_bf16 v[136:139], v[192:195], a[16:19], v[136:139]\n"
"buffer_store_dwordx4 v[0:3], v212, s[36:39], s8 offen offset:0 sc1\n"
"v_mfma_f32_16x16x32_bf16 v[152:155], v[192:195], a[20:23], 0\n"
"v_mfma_f32_16x16x32_bf16 v[152:155], v[196:199], a[4:7], v[152:155]\n"
"s_waitcnt lgkmcnt(2)\n"
"v_cmp_ne_u32 vcc, s75, v221\n"
"v_mfma_f32_16x16x32_bf16 v[168:171], v[196:199], a[8:11], 0\n"
"v_mfma_f32_16x16x32_bf16 v[168:171], v[192:195], a[24:27], v[168:171]\n"
"s_cbranch_vccnz Lsb6_%=\n"
"Lsr6_%=:\n"
"v_mfma_f32_16x16x32_bf16 v[184:187], v[192:195], a[28:31], 0\n"
"v_mfma_f32_16x16x32_bf16 v[184:187], v[196:199], a[12:15], v[184:187]\n"
"s_waitcnt lgkmcnt(0)\n"
"ds_read_b128 v[192:195], v222 offset:10240\n"
"ds_read_b128 v[196:199], v222 offset:14336\n"
"v_mfma_f32_16x16x32_bf16 v[188:191], v[204:207], a[12:15], 0\n"
"v_mfma_f32_16x16x32_bf16 v[188:191], v[200:203], a[28:31], v[188:191]\n"
"buffer_load_dwordx4 v212, s[36:39], s71 offen offset:1024 sc1 lds\n"
"v_mfma_f32_16x16x32_bf16 v[172:175], v[200:203], a[24:27], 0\n"
"s_add_u32 m0, s70, 18432\n"
"s_add_u32 s71, s81, 65536\n"
"v_mfma_f32_16x16x32_bf16 v[172:175], v[204:207], a[8:11], v[172:175]\n"
"v_max_f32 v4, 0, v4\n"
"v_max_f32 v5, 0, v5\n"
"v_mfma_f32_16x16x32_bf16 v[156:159], v[204:207], a[4:7], 0\n"
"v_max_f32 v6, 0, v6\n"
"v_max_f32 v7, 0, v7\n"
"v_mfma_f32_16x16x32_bf16 v[156:159], v[200:203], a[20:23], v[156:159]\n"
"v_mul_f32 v20, v4, v20\n"
"v_mul_f32 v21, v5, v21\n"
"v_mfma_f32_16x16x32_bf16 v[140:143], v[200:203], a[16:19], 0\n"
"v_mul_f32 v22, v6, v22\n"
"v_mul_f32 v23, v7, v23\n"
"v_mfma_f32_16x16x32_bf16 v[140:143], v[204:207], a[0:3], v[140:143]\n"
"s_waitcnt lgkmcnt(0)\n"
"ds_read_b128 v[200:203], v222 offset:11264\n"
"ds_read_b128 v[204:207], v222 offset:15360\n"
"v_mfma_f32_16x16x32_bf16 v[128:131], v[192:195], a[32:35], v[128:131]\n"
"buffer_load_dwordx4 v212, s[36:39], s71 offen offset:0 sc1 lds\n"
"v_mfma_f32_16x16x32_bf16 v[128:131], v[196:199], a[48:51], v[128:131]\n"
"v_cvt_pk_bf16_f32 v4, v20, v21\n"
"v_cvt_pk_bf16_f32 v5, v22, v23\n"
"v_mfma_f32_16x16x32_bf16 v[144:147], v[196:199], a[52:55], v[144:147]\n"
"v_max_f32 v36, 0, v36\n"
"v_max_f32 v37, 0, v37\n"
"v_mfma_f32_16x16x32_bf16 v[144:147], v[192:195], a[36:39], v[144:147]\n"
"v_max_f32 v38, 0, v38\n"
"v_max_f32 v39, 0, v39\n"
"v_mfma_f32_16x16x32_bf16 v[160:163], v[192:195], a[40:43], v[160:163]\n"
"v_mul_f32 v52, v36, v52\n"
"v_mul_f32 v53, v37, v53\n"
"v_mfma_f32_16x16x32_bf16 v[160:163], v[196:199], a[56:59], v[160:163]\n"
"v_mul_f32 v54, v38, v54\n"
"v_mul_f32 v55, v39, v55\n"
"v_mfma_f32_16x16x32_bf16 v[176:179], v[196:199], a[60:63], v[176:179]\n"
"v_cvt_pk_bf16_f32 v6, v52, v53\n"
"v_cvt_pk_bf16_f32 v7, v54, v55\n"
"v_mfma_f32_16x16x32_bf16 v[176:179], v[192:195], a[44:47], v[176:179]\n"
"s_waitcnt lgkmcnt(0)\n"
"ds_read_b128 v[196:199], v222 offset:12288\n"
"ds_read_b128 v[192:195], v222 offset:16384\n"
"v_mfma_f32_16x16x32_bf16 v[180:183], v[200:203], a[44:47], v[180:183]\n"
"buffer_load_dwordx4 v212, s[36:39], s71 offen offset:1024 sc1 lds\n"
"v_mfma_f32_16x16x32_bf16 v[180:183], v[204:207], a[60:63], v[180:183]\n"
"s_add_u32 m0, s70, 26624\n"
"s_add_u32 s71, s81, 73728\n"
"v_mfma_f32_16x16x32_bf16 v[164:167], v[204:207], a[56:59], v[164:167]\n"
"v_mfma_f32_16x16x32_bf16 v[164:167], v[200:203], a[40:43], v[164:167]\n"
"buffer_store_dwordx4 v[4:7], v212, s[36:39], s8 offen offset:1024 sc1\n"
"v_mfma_f32_16x16x32_bf16 v[148:151], v[200:203], a[36:39], v[148:151]\n"
"v_mfma_f32_16x16x32_bf16 v[148:151], v[204:207], a[52:55], v[148:151]\n"
"v_mfma_f32_16x16x32_bf16 v[132:135], v[204:207], a[48:51], v[132:135]\n"
"v_mfma_f32_16x16x32_bf16 v[132:135], v[200:203], a[32:35], v[132:135]\n"
"s_waitcnt lgkmcnt(0)\n"
"ds_read_b128 v[204:207], v222 offset:13312\n"
"ds_read_b128 v[200:203], v222 offset:17408\n"
"v_mfma_f32_16x16x32_bf16 v[136:139], v[196:199], a[32:35], v[136:139]\n"
"v_mfma_f32_16x16x32_bf16 v[136:139], v[192:195], a[48:51], v[136:139]\n"
"v_mfma_f32_16x16x32_bf16 v[152:155], v[192:195], a[52:55], v[152:155]\n"
"v_mfma_f32_16x16x32_bf16 v[152:155], v[196:199], a[36:39], v[152:155]\n"
"v_mfma_f32_16x16x32_bf16 v[168:171], v[196:199], a[40:43], v[168:171]\n"
"v_mfma_f32_16x16x32_bf16 v[168:171], v[192:195], a[56:59], v[168:171]\n"
"buffer_load_dwordx4 v212, s[36:39], s71 offen offset:0 sc1 lds\n"
"v_mfma_f32_16x16x32_bf16 v[184:187], v[192:195], a[60:63], v[184:187]\n"
"v_max_f32 v8, 0, v8\n"
"v_max_f32 v9, 0, v9\n"
"v_mfma_f32_16x16x32_bf16 v[184:187], v[196:199], a[44:47], v[184:187]\n"
"s_waitcnt lgkmcnt(0)\n"
"ds_read_b128 v[192:195], v222 offset:18432\n"
"ds_read_b128 v[196:199], v222 offset:22528\n"
"v_mfma_f32_16x16x32_bf16 v[188:191], v[204:207], a[44:47], v[188:191]\n"
"v_max_f32 v10, 0, v10\n"
"v_max_f32 v11, 0, v11\n"
"v_mfma_f32_16x16x32_bf16 v[188:191], v[200:203], a[60:63], v[188:191]\n"
"v_mul_f32 v24, v8, v24\n"
"v_mul_f32 v25, v9, v25\n"
"v_mfma_f32_16x16x32_bf16 v[172:175], v[200:203], a[56:59], v[172:175]\n"
"v_mul_f32 v26, v10, v26\n"
"v_mul_f32 v27, v11, v27\n"
"v_mfma_f32_16x16x32_bf16 v[172:175], v[204:207], a[40:43], v[172:175]\n"
"v_cvt_pk_bf16_f32 v8, v24, v25\n"
"v_cvt_pk_bf16_f32 v9, v26, v27\n"
"v_mfma_f32_16x16x32_bf16 v[156:159], v[204:207], a[36:39], v[156:159]\n"
"buffer_load_dwordx4 v212, s[36:39], s71 offen offset:1024 sc1 lds\n"
"v_mfma_f32_16x16x32_bf16 v[156:159], v[200:203], a[52:55], v[156:159]\n"
"Lpad3_%=:\n"
"s_cbranch_execz Lpad7_%=\n"
"s_add_u32 m0, s70, 34816\n"
"s_add_u32 s71, s81, 81920\n"
"v_max_f32 v40, 0, v40\n"
"v_mfma_f32_16x16x32_bf16 v[140:143], v[200:203], a[48:51], v[140:143]\n"
"v_max_f32 v41, 0, v41\n"
"v_max_f32 v42, 0, v42\n"
"v_mfma_f32_16x16x32_bf16 v[140:143], v[204:207], a[32:35], v[140:143]\n"
"s_waitcnt lgkmcnt(0)\n"
"ds_read_b128 v[200:203], v222 offset:19456\n"
"ds_read_b128 v[204:207], v222 offset:23552\n"
"v_mfma_f32_16x16x32_bf16 v[128:131], v[192:195], a[64:67], v[128:131]\n"
"v_max_f32 v43, 0, v43\n"
"v_mul_f32 v56, v40, v56\n"
"v_mfma_f32_16x16x32_bf16 v[128:131], v[196:199], a[80:83], v[128:131]\n"
"v_mul_f32 v57, v41, v57\n"
"v_mul_f32 v58, v42, v58\n"
"v_mfma_f32_16x16x32_bf16 v[144:147], v[196:199], a[84:87], v[144:147]\n"
"v_mul_f32 v59, v43, v59\n"
"v_cvt_pk_bf16_f32 v10, v56, v57\n"
"v_mfma_f32_16x16x32_bf16 v[144:147], v[192:195], a[68:71], v[144:147]\n"
"buffer_load_dwordx4 v212, s[36:39], s71 offen offset:0 sc1 lds\n"
"v_mfma_f32_16x16x32_bf16 v[160:163], v[192:195], a[72:75], v[160:163]\n"
"v_cvt_pk_bf16_f32 v11, v58, v59\n"
"v_mfma_f32_16x16x32_bf16 v[160:163], v[196:199], a[88:91], v[160:163]\n"
"v_mfma_f32_16x16x32_bf16 v[176:179], v[196:199], a[92:95], v[176:179]\n"
"buffer_store_dwordx4 v[8:11], v212, s[36:39], s9 offen offset:0 sc1\n"
"v_mfma_f32_16x16x32_bf16 v[176:179], v[192:195], a[76:79], v[176:179]\n"
"s_waitcnt lgkmcnt(0)\n"
"ds_read_b128 v[196:199], v222 offset:20480\n"
"ds_read_b128 v[192:195], v222 offset:24576\n"
"v_mfma_f32_16x16x32_bf16 v[180:183], v[200:203], a[76:79], v[180:183]\n"
"s_waitcnt vmcnt(14)\n"
"ds_write_b32 v214, v219 offset:224\n"
"v_mfma_f32_16x16x32_bf16 v[180:183], v[204:207], a[92:95], v[180:183]\n"
"v_mfma_f32_16x16x32_bf16 v[164:167], v[204:207], a[88:91], v[164:167]\n"
"v_mfma_f32_16x16x32_bf16 v[164:167], v[200:203], a[72:75], v[164:167]\n"
"v_mfma_f32_16x16x32_bf16 v[148:151], v[200:203], a[68:71], v[148:151]\n"
"v_mfma_f32_16x16x32_bf16 v[148:151], v[204:207], a[84:87], v[148:151]\n"
"v_mfma_f32_16x16x32_bf16 v[132:135], v[204:207], a[80:83], v[132:135]\n"
"ds_read_b32 v221, v220 offset:224\n"
"v_mfma_f32_16x16x32_bf16 v[132:135], v[200:203], a[64:67], v[132:135]\n"
"s_waitcnt lgkmcnt(2)\n"
"ds_read_b128 v[204:207], v222 offset:21504\n"
"ds_read_b128 v[200:203], v222 offset:25600\n"
"v_mfma_f32_16x16x32_bf16 v[136:139], v[196:199], a[64:67], v[136:139]\n"
"buffer_load_dwordx4 v212, s[36:39], s71 offen offset:1024 sc1 lds\n"
"v_mfma_f32_16x16x32_bf16 v[136:139], v[192:195], a[80:83], v[136:139]\n"
"s_add_u32 m0, s70, 43008\n"
"s_add_u32 s71, s81, 90112\n"
"v_max_f32 v12, 0, v12\n"
"v_mfma_f32_16x16x32_bf16 v[152:155], v[192:195], a[84:87], v[152:155]\n"
"v_max_f32 v13, 0, v13\n"
"v_max_f32 v14, 0, v14\n"
"v_mfma_f32_16x16x32_bf16 v[152:155], v[196:199], a[68:71], v[152:155]\n"
"s_waitcnt lgkmcnt(2)\n"
"v_cmp_ne_u32 vcc, s75, v221\n"
"v_max_f32 v15, 0, v15\n"
"v_mfma_f32_16x16x32_bf16 v[168:171], v[196:199], a[72:75], v[168:171]\n"
"v_mul_f32 v28, v12, v28\n"
"v_mul_f32 v29, v13, v29\n"
"v_mfma_f32_16x16x32_bf16 v[168:171], v[192:195], a[88:91], v[168:171]\n"
"s_cbranch_vccnz Lsb7_%=\n"
"Lsr7_%=:\n"
"v_mul_f32 v30, v14, v30\n"
"v_mfma_f32_16x16x32_bf16 v[184:187], v[192:195], a[92:95], v[184:187]\n"
"v_mul_f32 v31, v15, v31\n"
"v_cvt_pk_bf16_f32 v12, v28, v29\n"
"v_mfma_f32_16x16x32_bf16 v[184:187], v[196:199], a[76:79], v[184:187]\n"
"s_waitcnt lgkmcnt(0)\n"
"ds_read_b128 v[192:195], v222 offset:26624\n"
"ds_read_b128 v[196:199], v222 offset:30720\n"
"v_mfma_f32_16x16x32_bf16 v[188:191], v[204:207], a[76:79], v[188:191]\n"
"buffer_load_dwordx4 v212, s[36:39], s71 offen offset:0 sc1 lds\n"
"v_mfma_f32_16x16x32_bf16 v[188:191], v[200:203], a[92:95], v[188:191]\n"
"v_cvt_pk_bf16_f32 v13, v30, v31\n"
"v_max_f32 v44, 0, v44\n"
"v_mfma_f32_16x16x32_bf16 v[172:175], v[200:203], a[88:91], v[172:175]\n"
"v_max_f32 v45, 0, v45\n"
"v_max_f32 v46, 0, v46\n"
"v_mfma_f32_16x16x32_bf16 v[172:175], v[204:207], a[72:75], v[172:175]\n"
"v_max_f32 v47, 0, v47\n"
"v_mul_f32 v60, v44, v60\n"
"v_mfma_f32_16x16x32_bf16 v[156:159], v[204:207], a[68:71], v[156:159]\n"
"v_mul_f32 v61, v45, v61\n"
"v_mul_f32 v62, v46, v62\n"
"v_mfma_f32_16x16x32_bf16 v[156:159], v[200:203], a[84:87], v[156:159]\n"
"v_mul_f32 v63, v47, v63\n"
"v_cvt_pk_bf16_f32 v14, v60, v61\n"
"v_mfma_f32_16x16x32_bf16 v[140:143], v[200:203], a[80:83], v[140:143]\n"
"buffer_load_dwordx4 v212, s[36:39], s71 offen offset:1024 sc1 lds\n"
"v_mfma_f32_16x16x32_bf16 v[140:143], v[204:207], a[64:67], v[140:143]\n"
"s_waitcnt lgkmcnt(0)\n"
"ds_read_b128 v[200:203], v222 offset:27648\n"
"ds_read_b128 v[204:207], v222 offset:31744\n"
"v_mfma_f32_16x16x32_bf16 v[128:131], v[192:195], a[96:99], v[128:131]\n"
"s_add_u32 m0, s70, 51200\n"
"s_add_u32 s71, s81, 98304\n"
"v_cvt_pk_bf16_f32 v15, v62, v63\n"
"v_mfma_f32_16x16x32_bf16 v[128:131], v[196:199], a[112:115], v[128:131]\n"
"v_mfma_f32_16x16x32_bf16 v[144:147], v[196:199], a[116:119], v[144:147]\n"
"buffer_store_dwordx4 v[12:15], v212, s[36:39], s9 offen offset:1024 sc1\n"
"v_mfma_f32_16x16x32_bf16 v[144:147], v[192:195], a[100:103], v[144:147]\n"
"s_lshl_b32 s84, s61, 5\n"
"v_min_u32 v46, 176, v212\n"
"v_lshrrev_b32 v45, 6, v46\n"
"v_mfma_f32_16x16x32_bf16 v[160:163], v[192:195], a[104:107], v[160:163]\n"
"v_mul_u32_u24 v45, 9216, v45\n"
"v_bfe_u32 v46, v46, 4, 2\n"
"v_mfma_f32_16x16x32_bf16 v[160:163], v[196:199], a[120:123], v[160:163]\n"
"v_lshl_add_u32 v45, v46, 8, v45\n"
"s_lshr_b32 s66, s60, 3\n"
"v_mfma_f32_16x16x32_bf16 v[176:179], v[196:199], a[124:127], v[176:179]\n"
"s_lshr_b32 s67, s61, 1\n"
"s_lshl1_add_u32 s66, s66, s67\n"
"s_mul_i32 s66, s66, 27648\n"
"v_mfma_f32_16x16x32_bf16 v[176:179], v[192:195], a[108:111], v[176:179]\n"
"s_waitcnt lgkmcnt(0)\n"
"ds_read_b128 v[196:199], v222 offset:28672\n"
"ds_read_b128 v[192:195], v222 offset:32768\n"
"v_mfma_f32_16x16x32_bf16 v[180:183], v[200:203], a[108:111], v[180:183]\n"
"s_lshr_b32 s67, s65, 2\n"
"s_lshl_b32 s67, s67, 10\n"
"s_add_u32 s90, s66, s67\n"
"v_mfma_f32_16x16x32_bf16 v[180:183], v[204:207], a[124:127], v[180:183]\n"
"s_add_u32 s90, s90, 3538944\n"
"v_and_b32 v47, 896, v212\n"
"v_add_u32 v44, s83, v212\n"
"v_mfma_f32_16x16x32_bf16 v[164:167], v[204:207], a[120:123], v[164:167]\n"
"v_lshrrev_b32 v39, 9, v212\n"
"v_mul_u32_u24 v39, 9216, v39\n"
"v_mfma_f32_16x16x32_bf16 v[164:167], v[200:203], a[104:107], v[164:167]\n"
"v_bfe_u32 v40, v212, 6, 3\n"
"v_lshl_add_u32 v39, v40, 7, v39\n"
"v_mfma_f32_16x16x32_bf16 v[148:151], v[200:203], a[100:103], v[148:151]\n"
"buffer_load_dwordx4 v212, s[36:39], s71 offen offset:0 sc1 lds\n"
"v_mfma_f32_16x16x32_bf16 v[148:151], v[204:207], a[116:119], v[148:151]\n"
"v_add_u32 v39, 108544, v39\n"
"v_lshrrev_b32 v41, 1, v212\n"
"v_mfma_f32_16x16x32_bf16 v[132:135], v[204:207], a[112:115], v[132:135]\n"
"v_and_b32 v41, 64, v41\n"
"v_and_b32 v40, 48, v212\n"
"v_mfma_f32_16x16x32_bf16 v[132:135], v[200:203], a[96:99], v[132:135]\n"
"s_waitcnt lgkmcnt(0)\n"
"ds_read_b128 v[204:207], v222 offset:29696\n"
"ds_read_b128 v[200:203], v222 offset:33792\n"
"v_mfma_f32_16x16x32_bf16 v[136:139], v[196:199], a[96:99], v[136:139]\n"
"v_lshrrev_b32 v34, 1, v40\n"
"v_add_u32 v34, s84, v34\n"
"v_mfma_f32_16x16x32_bf16 v[136:139], v[192:195], a[112:115], v[136:139]\n"
"v_xor_b32 v34, v34, v41\n"
"v_add_u32 v34, v34, v39\n"
"v_mfma_f32_16x16x32_bf16 v[152:155], v[192:195], a[116:119], v[152:155]\n"
"v_add3_u32 v39, v39, v40, s70\n"
"v_add_u32 v32, v39, v41\n"
"v_mfma_f32_16x16x32_bf16 v[152:155], v[196:199], a[100:103], v[152:155]\n"
"buffer_load_dwordx4 v212, s[36:39], s71 offen offset:1024 sc1 lds\n"
"v_mfma_f32_16x16x32_bf16 v[168:171], v[196:199], a[104:107], v[168:171]\n"
"s_add_u32 m0, s70, 59392\n"
"s_add_u32 s71, s81, 106496\n"
"v_xor_b32 v33, 64, v32\n"
"v_mfma_f32_16x16x32_bf16 v[168:171], v[192:195], a[120:123], v[168:171]\n"
"v_lshrrev_b32 v218, 8, v212\n"
"v_mad_i32_i24 v218, v218, -4, v217\n"
"v_mfma_f32_16x16x32_bf16 v[184:187], v[192:195], a[124:127], v[184:187]\n"
"v_max_f32 v64, 0, v64\n"
"v_max_f32 v65, 0, v65\n"
"v_mfma_f32_16x16x32_bf16 v[184:187], v[196:199], a[108:111], v[184:187]\n"
"s_waitcnt lgkmcnt(0)\n"
"ds_read_b128 v[192:195], v222 offset:34816\n"
"ds_read_b128 v[196:199], v222 offset:38912\n"
"v_mfma_f32_16x16x32_bf16 v[188:191], v[204:207], a[108:111], v[188:191]\n"
"v_max_f32 v66, 0, v66\n"
"v_max_f32 v67, 0, v67\n"
"v_mfma_f32_16x16x32_bf16 v[188:191], v[200:203], a[124:127], v[188:191]\n"
"buffer_load_dwordx4 v212, s[36:39], s71 offen offset:0 sc1 lds\n"
"v_mfma_f32_16x16x32_bf16 v[172:175], v[200:203], a[120:123], v[172:175]\n"
"v_mul_f32 v80, v64, v80\n"
"v_mul_f32 v81, v65, v81\n"
"v_mfma_f32_16x16x32_bf16 v[172:175], v[204:207], a[104:107], v[172:175]\n"
"v_mul_f32 v82, v66, v82\n"
"v_mul_f32 v83, v67, v83\n"
"v_mfma_f32_16x16x32_bf16 v[156:159], v[204:207], a[100:103], v[156:159]\n"
"v_cvt_pk_bf16_f32 v64, v80, v81\n"
"v_cvt_pk_bf16_f32 v65, v82, v83\n"
"v_mfma_f32_16x16x32_bf16 v[156:159], v[200:203], a[116:119], v[156:159]\n"
"buffer_load_dword v47, v47, s[36:39], s23 offen offset:0 sc1\n"
"v_mfma_f32_16x16x32_bf16 v[140:143], v[200:203], a[112:115], v[140:143]\n"
"v_max_f32 v96, 0, v96\n"
"v_max_f32 v97, 0, v97\n"
"v_mfma_f32_16x16x32_bf16 v[140:143], v[204:207], a[96:99], v[140:143]\n"
"s_waitcnt lgkmcnt(0)\n"
"ds_read_b128 v[200:203], v222 offset:35840\n"
"ds_read_b128 v[204:207], v222 offset:39936\n"
"v_mfma_f32_16x16x32_bf16 v[128:131], v[192:195], a[128:131], v[128:131]\n"
"v_max_f32 v98, 0, v98\n"
"v_max_f32 v99, 0, v99\n"
"v_mfma_f32_16x16x32_bf16 v[128:131], v[196:199], a[144:147], v[128:131]\n"
"v_mul_f32 v112, v96, v112\n"
"v_mul_f32 v113, v97, v113\n"
"v_mfma_f32_16x16x32_bf16 v[144:147], v[196:199], a[148:151], v[144:147]\n"
"v_mul_f32 v114, v98, v114\n"
"v_mul_f32 v115, v99, v115\n"
"v_mfma_f32_16x16x32_bf16 v[144:147], v[192:195], a[132:135], v[144:147]\n"
"v_cvt_pk_bf16_f32 v66, v112, v113\n"
"v_cvt_pk_bf16_f32 v67, v114, v115\n"
"v_mfma_f32_16x16x32_bf16 v[160:163], v[192:195], a[136:139], v[160:163]\n"
"buffer_load_dword v46, v45, s[56:59], s90 offen offset:0\n"
"v_mfma_f32_16x16x32_bf16 v[160:163], v[196:199], a[152:155], v[160:163]\n"
"s_add_u32 s90, s90, 1769472\n"
"v_mfma_f32_16x16x32_bf16 v[176:179], v[196:199], a[156:159], v[176:179]\n"
"v_mfma_f32_16x16x32_bf16 v[176:179], v[192:195], a[140:143], v[176:179]\n"
"s_waitcnt lgkmcnt(0)\n"
"ds_read_b128 v[196:199], v222 offset:36864\n"
"ds_read_b128 v[192:195], v222 offset:40960\n"
"v_mfma_f32_16x16x32_bf16 v[180:183], v[200:203], a[140:143], v[180:183]\n"
"s_waitcnt vmcnt(19)\n"
"ds_write_b32 v214, v219 offset:256\n"
"v_mfma_f32_16x16x32_bf16 v[180:183], v[204:207], a[156:159], v[180:183]\n"
"v_mfma_f32_16x16x32_bf16 v[164:167], v[204:207], a[152:155], v[164:167]\n"
"v_mfma_f32_16x16x32_bf16 v[164:167], v[200:203], a[136:139], v[164:167]\n"
"v_mfma_f32_16x16x32_bf16 v[148:151], v[200:203], a[132:135], v[148:151]\n"
"v_mfma_f32_16x16x32_bf16 v[148:151], v[204:207], a[148:151], v[148:151]\n"
"v_mfma_f32_16x16x32_bf16 v[132:135], v[204:207], a[144:147], v[132:135]\n"
"ds_read_b32 v221, v220 offset:256\n"
"v_mfma_f32_16x16x32_bf16 v[132:135], v[200:203], a[128:131], v[132:135]\n"
"s_waitcnt lgkmcnt(2)\n"
"ds_read_b128 v[204:207], v222 offset:37888\n"
"ds_read_b128 v[200:203], v222 offset:41984\n"
"v_mfma_f32_16x16x32_bf16 v[136:139], v[196:199], a[128:131], v[136:139]\n"
"v_mfma_f32_16x16x32_bf16 v[136:139], v[192:195], a[144:147], v[136:139]\n"
"v_mfma_f32_16x16x32_bf16 v[152:155], v[192:195], a[148:151], v[152:155]\n"
"v_mfma_f32_16x16x32_bf16 v[152:155], v[196:199], a[132:135], v[152:155]\n"
"s_waitcnt lgkmcnt(2)\n"
"v_cmp_ne_u32 vcc, s75, v221\n"
"v_mfma_f32_16x16x32_bf16 v[168:171], v[196:199], a[136:139], v[168:171]\n"
"v_mfma_f32_16x16x32_bf16 v[168:171], v[192:195], a[152:155], v[168:171]\n"
"s_cbranch_vccnz Lsb8_%=\n"
"Lsr8_%=:\n"
"v_mfma_f32_16x16x32_bf16 v[184:187], v[192:195], a[156:159], v[184:187]\n"
"v_mfma_f32_16x16x32_bf16 v[184:187], v[196:199], a[140:143], v[184:187]\n"
"s_waitcnt lgkmcnt(0)\n"
"ds_read_b128 v[192:195], v222 offset:43008\n"
"ds_read_b128 v[196:199], v222 offset:47104\n"
"v_mfma_f32_16x16x32_bf16 v[188:191], v[204:207], a[140:143], v[188:191]\n"
"v_mfma_f32_16x16x32_bf16 v[188:191], v[200:203], a[156:159], v[188:191]\n"
"v_mfma_f32_16x16x32_bf16 v[172:175], v[200:203], a[152:155], v[172:175]\n"
"v_mfma_f32_16x16x32_bf16 v[172:175], v[204:207], a[136:139], v[172:175]\n"
"v_mfma_f32_16x16x32_bf16 v[156:159], v[204:207], a[132:135], v[156:159]\n"
"v_mfma_f32_16x16x32_bf16 v[156:159], v[200:203], a[148:151], v[156:159]\n"
"v_mfma_f32_16x16x32_bf16 v[140:143], v[200:203], a[144:147], v[140:143]\n"
"v_mfma_f32_16x16x32_bf16 v[140:143], v[204:207], a[128:131], v[140:143]\n"
"s_waitcnt lgkmcnt(0)\n"
"ds_read_b128 v[200:203], v222 offset:44032\n"
"ds_read_b128 v[204:207], v222 offset:48128\n"
"v_mfma_f32_16x16x32_bf16 v[128:131], v[192:195], a[160:163], v[128:131]\n"
"v_mfma_f32_16x16x32_bf16 v[128:131], v[196:199], a[176:179], v[128:131]\n"
"v_mfma_f32_16x16x32_bf16 v[144:147], v[196:199], a[180:183], v[144:147]\n"
"v_mfma_f32_16x16x32_bf16 v[144:147], v[192:195], a[164:167], v[144:147]\n"
"v_mfma_f32_16x16x32_bf16 v[160:163], v[192:195], a[168:171], v[160:163]\n"
"v_mfma_f32_16x16x32_bf16 v[160:163], v[196:199], a[184:187], v[160:163]\n"
"v_mfma_f32_16x16x32_bf16 v[176:179], v[196:199], a[188:191], v[176:179]\n"
"v_mfma_f32_16x16x32_bf16 v[176:179], v[192:195], a[172:175], v[176:179]\n"
"s_waitcnt lgkmcnt(0)\n"
"ds_read_b128 v[196:199], v222 offset:45056\n"
"ds_read_b128 v[192:195], v222 offset:49152\n"
"v_mfma_f32_16x16x32_bf16 v[180:183], v[200:203], a[172:175], v[180:183]\n"
"buffer_store_dwordx4 v[64:67], v212, s[36:39], s8 offen offset:2048 sc1\n"
"v_mfma_f32_16x16x32_bf16 v[180:183], v[204:207], a[188:191], v[180:183]\n"
"v_max_f32 v68, 0, v68\n"
"v_max_f32 v69, 0, v69\n"
"v_mfma_f32_16x16x32_bf16 v[164:167], v[204:207], a[184:187], v[164:167]\n"
"v_max_f32 v70, 0, v70\n"
"v_max_f32 v71, 0, v71\n"
"v_mfma_f32_16x16x32_bf16 v[164:167], v[200:203], a[168:171], v[164:167]\n"
"v_mul_f32 v84, v68, v84\n"
"v_mul_f32 v85, v69, v85\n"
"v_mfma_f32_16x16x32_bf16 v[148:151], v[200:203], a[164:167], v[148:151]\n"
"v_mul_f32 v86, v70, v86\n"
"v_mul_f32 v87, v71, v87\n"
"v_mfma_f32_16x16x32_bf16 v[148:151], v[204:207], a[180:183], v[148:151]\n"
"v_cvt_pk_bf16_f32 v68, v84, v85\n"
"v_cvt_pk_bf16_f32 v69, v86, v87\n"
"v_mfma_f32_16x16x32_bf16 v[132:135], v[204:207], a[176:179], v[132:135]\n"
"v_max_f32 v100, 0, v100\n"
"v_max_f32 v101, 0, v101\n"
"v_mfma_f32_16x16x32_bf16 v[132:135], v[200:203], a[160:163], v[132:135]\n"
"s_waitcnt lgkmcnt(0)\n"
"ds_read_b128 v[204:207], v222 offset:46080\n"
"ds_read_b128 v[200:203], v222 offset:50176\n"
"v_mfma_f32_16x16x32_bf16 v[136:139], v[196:199], a[160:163], v[136:139]\n"
"v_max_f32 v102, 0, v102\n"
"v_max_f32 v103, 0, v103\n"
"v_mfma_f32_16x16x32_bf16 v[136:139], v[192:195], a[176:179], v[136:139]\n"
"v_mul_f32 v116, v100, v116\n"
"v_mul_f32 v117, v101, v117\n"
"v_mfma_f32_16x16x32_bf16 v[152:155], v[192:195], a[180:183], v[152:155]\n"
"buffer_load_dwordx4 v212, s[36:39], s71 offen offset:1024 sc1 lds\n"
"v_mfma_f32_16x16x32_bf16 v[152:155], v[196:199], a[164:167], v[152:155]\n"
"s_lshl_b32 s72, s65, 8\n"
"s_add_u32 m0, s70, 67584\n"
"v_mul_f32 v118, v102, v118\n"
"v_mfma_f32_16x16x32_bf16 v[168:171], v[196:199], a[168:171], v[168:171]\n"
"s_add_u32 s71, s72, 0\n"
"v_mul_f32 v119, v103, v119\n"
"v_cvt_pk_bf16_f32 v70, v116, v117\n"
"v_mfma_f32_16x16x32_bf16 v[168:171], v[192:195], a[184:187], v[168:171]\n"
"v_cvt_pk_bf16_f32 v71, v118, v119\n"
"v_mfma_f32_16x16x32_bf16 v[184:187], v[192:195], a[188:191], v[184:187]\n"
"v_add_u32 v222, 0xffff4000, v222\n"
"v_mfma_f32_16x16x32_bf16 v[184:187], v[196:199], a[172:175], v[184:187]\n"
"s_waitcnt lgkmcnt(0)\n"
"ds_read_b128 v[192:195], v222 offset:2048\n"
"ds_read_b128 v[196:199], v222 offset:6144\n"
"v_mfma_f32_16x16x32_bf16 v[188:191], v[204:207], a[172:175], v[188:191]\n"
"buffer_load_dwordx4 v213, s[40:43], s71 offen offset:0 lds\n"
"v_mfma_f32_16x16x32_bf16 v[188:191], v[200:203], a[188:191], v[188:191]\n"
"v_mfma_f32_16x16x32_bf16 v[172:175], v[200:203], a[184:187], v[172:175]\n"
"v_mfma_f32_16x16x32_bf16 v[172:175], v[204:207], a[168:171], v[172:175]\n"
"v_mfma_f32_16x16x32_bf16 v[156:159], v[204:207], a[164:167], v[156:159]\n"
"buffer_store_dwordx4 v[68:71], v212, s[36:39], s8 offen offset:3072 sc1\n"
"v_mfma_f32_16x16x32_bf16 v[156:159], v[200:203], a[180:183], v[156:159]\n"
"ds_read_b64_tr_b16 v[80:81], v32 offset:0\n"
"ds_read_b64_tr_b16 v[82:83], v32 offset:18432\n"
"v_mfma_f32_16x16x32_bf16 v[140:143], v[200:203], a[176:179], v[140:143]\n"
"v_max_f32 v72, 0, v72\n"
"v_max_f32 v73, 0, v73\n"
"v_mfma_f32_16x16x32_bf16 v[140:143], v[204:207], a[160:163], v[140:143]\n"
"s_waitcnt lgkmcnt(2)\n"
"ds_read_b128 v[200:203], v222 offset:3072\n"
"ds_read_b128 v[204:207], v222 offset:7168\n"
"v_mfma_f32_16x16x32_bf16 v[128:131], v[192:195], a[192:195], v[128:131]\n"
"ds_read_b64_tr_b16 v[96:97], v32 offset:8\n"
"ds_read_b64_tr_b16 v[98:99], v32 offset:18440\n"
"v_mfma_f32_16x16x32_bf16 v[128:131], v[196:199], a[208:211], v[128:131]\n"
"v_max_f32 v74, 0, v74\n"
"v_max_f32 v75, 0, v75\n"
"v_mfma_f32_16x16x32_bf16 v[144:147], v[196:199], a[212:215], v[144:147]\n"
"v_mul_f32 v88, v72, v88\n"
"v_mul_f32 v89, v73, v89\n"
"v_mfma_f32_16x16x32_bf16 v[144:147], v[192:195], a[196:199], v[144:147]\n"
"ds_read_b64_tr_b16 v[112:113], v33 offset:0\n"
"ds_read_b64_tr_b16 v[114:115], v33 offset:18432\n"
"v_mfma_f32_16x16x32_bf16 v[160:163], v[192:195], a[200:203], v[160:163]\n"
"v_mul_f32 v90, v74, v90\n"
"v_mul_f32 v91, v75, v91\n"
"v_mfma_f32_16x16x32_bf16 v[160:163], v[196:199], a[216:219], v[160:163]\n"
"v_cvt_pk_bf16_f32 v72, v88, v89\n"
"v_cvt_pk_bf16_f32 v73, v90, v91\n"
"v_mfma_f32_16x16x32_bf16 v[176:179], v[196:199], a[220:223], v[176:179]\n"
"buffer_load_dwordx4 v213, s[44:47], s71 offen offset:1024 lds\n"
"v_mfma_f32_16x16x32_bf16 v[176:179], v[192:195], a[204:207], v[176:179]\n"
"s_waitcnt lgkmcnt(4)\n"
"ds_read_b128 v[196:199], v222 offset:4096\n"
"ds_read_b128 v[192:195], v222 offset:8192\n"
"v_mfma_f32_16x16x32_bf16 v[180:183], v[200:203], a[204:207], v[180:183]\n"
"s_waitcnt vmcnt(19)\n"
"ds_write_b32 v214, v219 offset:288\n"
"v_mfma_f32_16x16x32_bf16 v[180:183], v[204:207], a[220:223], v[180:183]\n"
"s_add_u32 m0, s70, 75776\n"
"ds_read_b64_tr_b16 v[84:85], v33 offset:8\n"
"s_add_u32 s71, s72, 294912\n"
"v_mfma_f32_16x16x32_bf16 v[164:167], v[204:207], a[216:219], v[164:167]\n"
"ds_read_b64_tr_b16 v[86:87], v33 offset:18440\n"
"v_max_f32 v104, 0, v104\n"
"v_mfma_f32_16x16x32_bf16 v[164:167], v[200:203], a[200:203], v[164:167]\n"
"v_max_f32 v105, 0, v105\n"
"v_max_f32 v106, 0, v106\n"
"v_mfma_f32_16x16x32_bf16 v[148:151], v[200:203], a[196:199], v[148:151]\n"
"v_max_f32 v107, 0, v107\n"
"v_mul_f32 v120, v104, v120\n"
"v_mfma_f32_16x16x32_bf16 v[148:151], v[204:207], a[212:215], v[148:151]\n"
"buffer_load_dwordx4 v213, s[40:43], s71 offen offset:0 lds\n"
"v_mfma_f32_16x16x32_bf16 v[132:135], v[204:207], a[208:211], v[132:135]\n"
"ds_read_b32 v221, v220 offset:288\n"
"ds_read_b64_tr_b16 v[100:101], v32 offset:1024\n"
"v_mfma_f32_16x16x32_bf16 v[132:135], v[200:203], a[192:195], v[132:135]\n"
"s_waitcnt lgkmcnt(5)\n"
"ds_read_b128 v[204:207], v222 offset:5120\n"
"ds_read_b128 v[200:203], v222 offset:9216\n"
"v_mfma_f32_16x16x32_bf16 v[136:139], v[196:199], a[192:195], v[136:139]\n"
"ds_read_b64_tr_b16 v[102:103], v32 offset:19456\n"
"v_mul_f32 v121, v105, v121\n"
"v_mfma_f32_16x16x32_bf16 v[136:139], v[192:195], a[208:211], v[136:139]\n"
"v_mul_f32 v122, v106, v122\n"
"v_mul_f32 v123, v107, v123\n"
"v_mfma_f32_16x16x32_bf16 v[152:155], v[192:195], a[212:215], v[152:155]\n"
"v_cvt_pk_bf16_f32 v74, v120, v121\n"
"v_cvt_pk_bf16_f32 v75, v122, v123\n"
"v_mfma_f32_16x16x32_bf16 v[152:155], v[196:199], a[196:199], v[152:155]\n"
"s_waitcnt lgkmcnt(4)\n"
"ds_read_b64_tr_b16 v[48:49], v32 offset:1032\n"
"v_cmp_ne_u32 vcc, s75, v221\n"
"v_mfma_f32_16x16x32_bf16 v[168:171], v[196:199], a[200:203], v[168:171]\n"
"buffer_load_dwordx4 v213, s[44:47], s71 offen offset:1024 lds\n"
"v_mfma_f32_16x16x32_bf16 v[168:171], v[192:195], a[216:219], v[168:171]\n"
"s_cbranch_vccnz Lsb9_%=\n"
"Lsr9_%=:\n"
"s_add_u32 m0, s70, 83968\n"
"v_mfma_f32_16x16x32_bf16 v[184:187], v[192:195], a[220:223], v[184:187]\n"
"s_add_u32 s71, s72, 589824\n"
"ds_read_b64_tr_b16 v[50:51], v32 offset:19464\n"
"v_mfma_f32_16x16x32_bf16 v[184:187], v[196:199], a[204:207], v[184:187]\n"
"s_waitcnt lgkmcnt(3)\n"
"ds_read_b128 v[192:195], v222 offset:10240\n"
"ds_read_b128 v[196:199], v222 offset:14336\n"
"v_mfma_f32_16x16x32_bf16 v[188:191], v[204:207], a[204:207], v[188:191]\n"
"buffer_store_dwordx4 v[72:75], v212, s[36:39], s9 offen offset:2048 sc1\n"
"v_mfma_f32_16x16x32_bf16 v[188:191], v[200:203], a[220:223], v[188:191]\n"
"ds_read_b64_tr_b16 v[52:53], v33 offset:1024\n"
"ds_read_b64_tr_b16 v[54:55], v33 offset:19456\n"
"v_mfma_f32_16x16x32_bf16 v[172:175], v[200:203], a[216:219], v[172:175]\n"
"v_max_f32 v76, 0, v76\n"
"v_max_f32 v77, 0, v77\n"
"v_mfma_f32_16x16x32_bf16 v[172:175], v[204:207], a[200:203], v[172:175]\n"
"v_max_f32 v78, 0, v78\n"
"v_max_f32 v79, 0, v79\n"
"v_mfma_f32_16x16x32_bf16 v[156:159], v[204:207], a[196:199], v[156:159]\n"
"ds_read_b64_tr_b16 v[56:57], v33 offset:1032\n"
"ds_read_b64_tr_b16 v[58:59], v33 offset:19464\n"
"v_mfma_f32_16x16x32_bf16 v[156:159], v[200:203], a[212:215], v[156:159]\n"
"v_mul_f32 v92, v76, v92\n"
"v_mul_f32 v93, v77, v93\n"
"v_mfma_f32_16x16x32_bf16 v[140:143], v[200:203], a[208:211], v[140:143]\n"
"v_mul_f32 v94, v78, v94\n"
"v_mul_f32 v95, v79, v95\n"
"v_mfma_f32_16x16x32_bf16 v[140:143], v[204:207], a[192:195], v[140:143]\n"
"s_waitcnt lgkmcnt(4)\n"
"ds_read_b128 v[200:203], v222 offset:11264\n"
"ds_read_b128 v[204:207], v222 offset:15360\n"
"v_mfma_f32_16x16x32_bf16 v[128:131], v[192:195], a[224:227], v[128:131]\n"
"ds_read_b64_tr_b16 v[60:61], v34 offset:8192\n"
"ds_read_b64_tr_b16 v[62:63], v34 offset:26624\n"
"v_mfma_f32_16x16x32_bf16 v[128:131], v[196:199], a[240:243], v[128:131]\n"
"buffer_load_dwordx4 v213, s[40:43], s71 offen offset:0 lds\n"
"v_mfma_f32_16x16x32_bf16 v[144:147], v[196:199], a[244:247], v[144:147]\n"
"v_cvt_pk_bf16_f32 v76, v92, v93\n"
"v_cvt_pk_bf16_f32 v77, v94, v95\n"
"v_mfma_f32_16x16x32_bf16 v[144:147], v[192:195], a[228:231], v[144:147]\n"
"v_max_f32 v108, 0, v108\n"
"v_max_f32 v109, 0, v109\n"
"v_mfma_f32_16x16x32_bf16 v[160:163], v[192:195], a[232:235], v[160:163]\n"
"v_max_f32 v110, 0, v110\n"
"v_max_f32 v111, 0, v111\n"
"v_mfma_f32_16x16x32_bf16 v[160:163], v[196:199], a[248:251], v[160:163]\n"
"v_mul_f32 v124, v108, v124\n"
"v_mul_f32 v125, v109, v125\n"
"v_mfma_f32_16x16x32_bf16 v[176:179], v[196:199], a[252:255], v[176:179]\n"
"v_mul_f32 v126, v110, v126\n"
"v_mul_f32 v127, v111, v127\n"
"v_mfma_f32_16x16x32_bf16 v[176:179], v[192:195], a[236:239], v[176:179]\n"
"s_waitcnt lgkmcnt(2)\n"
"ds_read_b128 v[196:199], v222 offset:12288\n"
"ds_read_b128 v[192:195], v222 offset:16384\n"
"v_mfma_f32_16x16x32_bf16 v[180:183], v[200:203], a[236:239], v[180:183]\n"
"buffer_load_dwordx4 v213, s[44:47], s71 offen offset:1024 lds\n"
"v_mfma_f32_16x16x32_bf16 v[180:183], v[204:207], a[252:255], v[180:183]\n"
"s_add_u32 m0, s70, 92160\n"
"s_add_u32 s71, s72, 884736\n"
"v_cvt_pk_bf16_f32 v78, v124, v125\n"
"v_mfma_f32_16x16x32_bf16 v[164:167], v[204:207], a[248:251], v[164:167]\n"
"v_cvt_pk_bf16_f32 v79, v126, v127\n"
"v_mfma_f32_16x16x32_bf16 v[164:167], v[200:203], a[232:235], v[164:167]\n"
"v_mfma_f32_16x16x32_bf16 v[148:151], v[200:203], a[228:231], v[148:151]\n"
"buffer_store_dwordx4 v[76:79], v212, s[36:39], s9 offen offset:3072 sc1\n"
"v_mfma_f32_16x16x32_bf16 v[148:151], v[204:207], a[244:247], v[148:151]\n"
"v_mfma_f32_16x16x32_bf16 v[132:135], v[204:207], a[240:243], v[132:135]\n"
"v_mfma_f32_16x16x32_bf16 v[132:135], v[200:203], a[224:227], v[132:135]\n"
"s_waitcnt lgkmcnt(0)\n"
"ds_read_b128 v[204:207], v222 offset:13312\n"
"ds_read_b128 v[200:203], v222 offset:17408\n"
"v_mfma_f32_16x16x32_bf16 v[136:139], v[196:199], a[224:227], v[136:139]\n"
"v_mfma_f32_16x16x32_bf16 v[136:139], v[192:195], a[240:243], v[136:139]\n"
"v_mfma_f32_16x16x32_bf16 v[152:155], v[192:195], a[244:247], v[152:155]\n"
"v_mfma_f32_16x16x32_bf16 v[152:155], v[196:199], a[228:231], v[152:155]\n"
"v_mfma_f32_16x16x32_bf16 v[168:171], v[196:199], a[232:235], v[168:171]\n"
"v_mfma_f32_16x16x32_bf16 v[168:171], v[192:195], a[248:251], v[168:171]\n"
"v_mfma_f32_16x16x32_bf16 v[184:187], v[192:195], a[252:255], v[184:187]\n"
"buffer_load_dwordx4 v213, s[40:43], s71 offen offset:0 lds\n"
"v_mfma_f32_16x16x32_bf16 v[184:187], v[196:199], a[236:239], v[184:187]\n"
"s_waitcnt lgkmcnt(0)\n"
"ds_read_b128 v[192:195], v222 offset:18432\n"
"ds_read_b128 v[196:199], v222 offset:22528\n"
"v_mfma_f32_16x16x32_bf16 v[188:191], v[204:207], a[236:239], v[188:191]\n"
"v_mfma_f32_16x16x32_bf16 v[188:191], v[200:203], a[252:255], v[188:191]\n"
"v_mfma_f32_16x16x32_bf16 v[172:175], v[200:203], a[248:251], v[172:175]\n"
"v_mfma_f32_16x16x32_bf16 v[172:175], v[204:207], a[232:235], v[172:175]\n"
"v_mfma_f32_16x16x32_bf16 v[156:159], v[204:207], a[228:231], v[156:159]\n"
"buffer_load_dwordx4 v213, s[44:47], s71 offen offset:1024 lds\n"
"v_mfma_f32_16x16x32_bf16 v[156:159], v[200:203], a[244:247], v[156:159]\n"
"v_mfma_f32_16x16x32_bf16 v[140:143], v[200:203], a[240:243], v[140:143]\n"
"v_mfma_f32_16x16x32_bf16 v[140:143], v[204:207], a[224:227], v[140:143]\n"
"s_waitcnt lgkmcnt(0)\n"
"ds_read_b128 v[200:203], v222 offset:19456\n"
"ds_read_b128 v[204:207], v222 offset:23552\n"
"v_mfma_f32_16x16x32_bf16 v[128:131], v[192:195], v[224:227], v[128:131]\n"
"v_mfma_f32_16x16x32_bf16 v[128:131], v[196:199], v[240:243], v[128:131]\n"
"v_mfma_f32_16x16x32_bf16 v[144:147], v[196:199], v[244:247], v[144:147]\n"
"v_mfma_f32_16x16x32_bf16 v[144:147], v[192:195], v[228:231], v[144:147]\n"
"v_mfma_f32_16x16x32_bf16 v[160:163], v[192:195], v[232:235], v[160:163]\n"
"v_mfma_f32_16x16x32_bf16 v[160:163], v[196:199], v[248:251], v[160:163]\n"
"v_mfma_f32_16x16x32_bf16 v[176:179], v[196:199], v[252:255], v[176:179]\n"
"v_mfma_f32_16x16x32_bf16 v[176:179], v[192:195], v[236:239], v[176:179]\n"
"s_waitcnt lgkmcnt(0)\n"
"ds_read_b128 v[196:199], v222 offset:20480\n"
"ds_read_b128 v[192:195], v222 offset:24576\n"
"v_mfma_f32_16x16x32_bf16 v[180:183], v[200:203], v[236:239], v[180:183]\n"
"s_waitcnt vmcnt(21)\n"
"ds_write_b32 v214, v219 offset:320\n"
"v_mfma_f32_16x16x32_bf16 v[180:183], v[204:207], v[252:255], v[180:183]\n"
"buffer_store_dwordx4 v[80:83], v44, s[36:39], s97 offen offset:0 sc1\n"
"v_mfma_f32_16x16x32_bf16 v[164:167], v[204:207], v[248:251], v[164:167]\n"
"s_add_u32 s97, s97, s94\n"
"v_mfma_f32_16x16x32_bf16 v[164:167], v[200:203], v[232:235], v[164:167]\n"
"v_mfma_f32_16x16x32_bf16 v[148:151], v[200:203], v[228:231], v[148:151]\n"
"v_mfma_f32_16x16x32_bf16 v[148:151], v[204:207], v[244:247], v[148:151]\n"
"v_mfma_f32_16x16x32_bf16 v[132:135], v[204:207], v[240:243], v[132:135]\n"
"ds_read_b32 v221, v220 offset:320\n"
"v_mfma_f32_16x16x32_bf16 v[132:135], v[200:203], v[224:227], v[132:135]\n"
"s_waitcnt lgkmcnt(2)\n"
"ds_read_b128 v[204:207], v222 offset:21504\n"
"ds_read_b128 v[200:203], v222 offset:25600\n"
"v_mfma_f32_16x16x32_bf16 v[136:139], v[196:199], v[224:227], v[136:139]\n"
"v_mfma_f32_16x16x32_bf16 v[136:139], v[192:195], v[240:243], v[136:139]\n"
"v_mfma_f32_16x16x32_bf16 v[152:155], v[192:195], v[244:247], v[152:155]\n"
"v_mfma_f32_16x16x32_bf16 v[152:155], v[196:199], v[228:231], v[152:155]\n"
"s_waitcnt lgkmcnt(2)\n"
"v_cmp_ne_u32 vcc, s75, v221\n"
"v_mfma_f32_16x16x32_bf16 v[168:171], v[196:199], v[232:235], v[168:171]\n"
"v_mfma_f32_16x16x32_bf16 v[168:171], v[192:195], v[248:251], v[168:171]\n"
"s_cbranch_vccnz Lsb10_%=\n"
"Lsr10_%=:\n"
"s_add_u32 m0, s70, 2048\n"
"v_mfma_f32_16x16x32_bf16 v[184:187], v[192:195], v[252:255], v[184:187]\n"
"s_add_u32 s71, s72, 1179648\n"
"s_nop 0\n"
"v_mfma_f32_16x16x32_bf16 v[184:187], v[196:199], v[236:239], v[184:187]\n"
"s_waitcnt lgkmcnt(0)\n"
"ds_read_b128 v[192:195], v222 offset:26624\n"
"ds_read_b128 v[196:199], v222 offset:28672\n"
"v_mfma_f32_16x16x32_bf16 v[188:191], v[204:207], v[236:239], v[188:191]\n"
"buffer_load_dwordx4 v213, s[40:43], s71 offen offset:0 lds\n"
"v_mfma_f32_16x16x32_bf16 v[188:191], v[200:203], v[252:255], v[188:191]\n"
"v_mfma_f32_16x16x32_bf16 v[172:175], v[200:203], v[248:251], v[172:175]\n"
"v_mfma_f32_16x16x32_bf16 v[172:175], v[204:207], v[232:235], v[172:175]\n"
"v_mfma_f32_16x16x32_bf16 v[156:159], v[204:207], v[228:231], v[156:159]\n"
"v_mfma_f32_16x16x32_bf16 v[156:159], v[200:203], v[244:247], v[156:159]\n"
"v_mfma_f32_16x16x32_bf16 v[140:143], v[200:203], v[240:243], v[140:143]\n"
"buffer_load_dwordx4 v213, s[44:47], s71 offen offset:1024 lds\n"
"v_mfma_f32_16x16x32_bf16 v[140:143], v[204:207], v[224:227], v[140:143]\n"
"s_waitcnt lgkmcnt(0)\n"
"ds_read_b128 v[200:203], v222 offset:27648\n"
"ds_read_b128 v[204:207], v222 offset:29696\n"
"v_mfma_f32_16x16x32_bf16 v[0:3], v[192:195], a[0:3], 0\n"
"s_add_u32 m0, s70, 10240\n"
"s_add_u32 s71, s72, 1474560\n"
"v_mfma_f32_16x16x32_bf16 v[0:3], v[196:199], a[16:19], v[0:3]\n"
"v_max3_f32 v138, v128, v129, v130\n"
"v_max3_f32 v139, -v128, -v129, -v130\n"
"v_mfma_f32_16x16x32_bf16 v[4:7], v[196:199], a[20:23], 0\n"
"v_max3_f32 v138, v138, v131, v132\n"
"v_max3_f32 v139, v139, -v131, -v132\n"
"v_mfma_f32_16x16x32_bf16 v[4:7], v[192:195], a[4:7], v[4:7]\n"
"v_max3_f32 v138, v138, v133, v134\n"
"v_max3_f32 v139, v139, -v133, -v134\n"
"v_mfma_f32_16x16x32_bf16 v[8:11], v[192:195], a[8:11], 0\n"
"v_max3_f32 v138, v138, v135, v136\n"
"v_max3_f32 v139, v139, -v135, -v136\n"
"v_mfma_f32_16x16x32_bf16 v[8:11], v[196:199], a[24:27], v[8:11]\n"
"buffer_load_dwordx4 v213, s[40:43], s71 offen offset:0 lds\n"
"v_mfma_f32_16x16x32_bf16 v[12:15], v[196:199], a[28:31], 0\n"
"v_cvt_pk_bf16_f32 v140, v140, v141\n"
"v_cvt_pk_bf16_f32 v141, v142, v143\n"
"v_mfma_f32_16x16x32_bf16 v[12:15], v[192:195], a[12:15], v[12:15]\n"
"s_waitcnt lgkmcnt(0)\n"
"ds_read_b128 v[196:199], v222 offset:30720\n"
"ds_read_b128 v[192:195], v222 offset:32768\n"
"v_mfma_f32_16x16x32_bf16 v[28:31], v[200:203], a[12:15], 0\n"
"v_permlane16_swap_b32 v138, v139\n"
"v_mfma_f32_16x16x32_bf16 v[28:31], v[204:207], a[28:31], v[28:31]\n"
"buffer_store_dwordx4 v[96:99], v44, s[36:39], s97 offen offset:0 sc1\n"
"v_mfma_f32_16x16x32_bf16 v[24:27], v[204:207], a[24:27], 0\n"
"s_add_u32 s97, s97, s94\n"
"v_max_f32 v138, v138, v139\n"
"v_mov_b32 v139, v138\n"
"v_mfma_f32_16x16x32_bf16 v[24:27], v[200:203], a[8:11], v[24:27]\n"
"v_cvt_pk_bf16_f32 v137, v137, 1.0\n"
"v_mfma_f32_16x16x32_bf16 v[20:23], v[200:203], a[4:7], 0\n"
"ds_write_b64 v217, v[140:141] offset:0\n"
"v_mfma_f32_16x16x32_bf16 v[20:23], v[204:207], a[20:23], v[20:23]\n"
"v_permlane16_swap_b32 v138, v139\n"
"v_mfma_f32_16x16x32_bf16 v[16:19], v[204:207], a[16:19], 0\n"
"ds_write_b32 v218, v137 offset:32\n"
"v_mfma_f32_16x16x32_bf16 v[16:19], v[200:203], a[0:3], v[16:19]\n"
"s_waitcnt lgkmcnt(2)\n"
"ds_read_b128 v[204:207], v222 offset:31744\n"
"ds_read_b128 v[200:203], v222 offset:33792\n"
"v_mfma_f32_16x16x32_bf16 v[0:3], v[196:199], a[32:35], v[0:3]\n"
"Lpad4_%=:\n"
"s_cbranch_execz Lpad8_%=\n"
"v_sub_f32 v32, -v128, v139\n"
"v_sub_f32 v128, v128, v138\n"
"v_mfma_f32_16x16x32_bf16 v[0:3], v[192:195], a[48:51], v[0:3]\n"
"v_exp_f32 v32, v32\n"
"v_mfma_f32_16x16x32_bf16 v[4:7], v[192:195], a[52:55], v[4:7]\n"
"v_exp_f32 v128, v128\n"
"v_mfma_f32_16x16x32_bf16 v[4:7], v[196:199], a[36:39], v[4:7]\n"
"buffer_load_dwordx4 v213, s[44:47], s71 offen offset:1024 lds\n"
"v_mfma_f32_16x16x32_bf16 v[8:11], v[196:199], a[40:43], v[8:11]\n"
"v_sub_f32 v33, -v129, v139\n"
"v_sub_f32 v129, v129, v138\n"
"v_mfma_f32_16x16x32_bf16 v[8:11], v[192:195], a[56:59], v[8:11]\n"
"v_exp_f32 v33, v33\n"
"v_mfma_f32_16x16x32_bf16 v[12:15], v[192:195], a[60:63], v[12:15]\n"
"v_exp_f32 v129, v129\n"
"v_mfma_f32_16x16x32_bf16 v[12:15], v[196:199], a[44:47], v[12:15]\n"
"s_waitcnt lgkmcnt(0)\n"
"ds_read_b128 v[192:195], v222 offset:34816\n"
"ds_read_b128 v[196:199], v222 offset:36864\n"
"v_mfma_f32_16x16x32_bf16 v[28:31], v[204:207], a[44:47], v[28:31]\n"
"v_sub_f32 v34, -v130, v139\n"
"v_sub_f32 v130, v130, v138\n"
"v_mfma_f32_16x16x32_bf16 v[28:31], v[200:203], a[60:63], v[28:31]\n"
"v_exp_f32 v34, v34\n"
"v_mfma_f32_16x16x32_bf16 v[24:27], v[200:203], a[56:59], v[24:27]\n"
"v_exp_f32 v130, v130\n"
"v_mfma_f32_16x16x32_bf16 v[24:27], v[204:207], a[40:43], v[24:27]\n"
"v_sub_f32 v35, -v131, v139\n"
"v_sub_f32 v131, v131, v138\n"
"v_mfma_f32_16x16x32_bf16 v[20:23], v[204:207], a[36:39], v[20:23]\n"
"v_exp_f32 v35, v35\n"
"v_mfma_f32_16x16x32_bf16 v[20:23], v[200:203], a[52:55], v[20:23]\n"
"v_exp_f32 v131, v131\n"
"v_mfma_f32_16x16x32_bf16 v[16:19], v[200:203], a[48:51], v[16:19]\n"
"v_sub_f32 v36, -v132, v139\n"
"v_sub_f32 v132, v132, v138\n"
"v_mfma_f32_16x16x32_bf16 v[16:19], v[204:207], a[32:35], v[16:19]\n"
"s_waitcnt lgkmcnt(0)\n"
"ds_read_b128 v[200:203], v222 offset:35840\n"
"ds_read_b128 v[204:207], v222 offset:37888\n"
"v_mfma_f32_16x16x32_bf16 v[0:3], v[192:195], a[64:67], v[0:3]\n"
"v_exp_f32 v36, v36\n"
"v_mfma_f32_16x16x32_bf16 v[0:3], v[196:199], a[80:83], v[0:3]\n"
"v_exp_f32 v132, v132\n"
"v_mfma_f32_16x16x32_bf16 v[4:7], v[196:199], a[84:87], v[4:7]\n"
"v_sub_f32 v37, -v133, v139\n"
"v_sub_f32 v133, v133, v138\n"
"v_mfma_f32_16x16x32_bf16 v[4:7], v[192:195], a[68:71], v[4:7]\n"
"v_exp_f32 v37, v37\n"
"v_mfma_f32_16x16x32_bf16 v[8:11], v[192:195], a[72:75], v[8:11]\n"
"v_exp_f32 v133, v133\n"
"v_mfma_f32_16x16x32_bf16 v[8:11], v[196:199], a[88:91], v[8:11]\n"
"v_sub_f32 v38, -v134, v139\n"
"v_sub_f32 v134, v134, v138\n"
"v_mfma_f32_16x16x32_bf16 v[12:15], v[196:199], a[92:95], v[12:15]\n"
"buffer_store_dwordx4 v[112:115], v44, s[36:39], s97 offen offset:0 sc1\n"
"v_mfma_f32_16x16x32_bf16 v[12:15], v[192:195], a[76:79], v[12:15]\n"
"s_waitcnt lgkmcnt(0)\n"
"ds_read_b128 v[196:199], v222 offset:38912\n"
"ds_read_b128 v[192:195], v222 offset:40960\n"
"v_mfma_f32_16x16x32_bf16 v[28:31], v[200:203], a[76:79], v[28:31]\n"
"s_waitcnt vmcnt(18)\n"
"ds_write_b32 v214, v219 offset:352\n"
"v_mfma_f32_16x16x32_bf16 v[28:31], v[204:207], a[92:95], v[28:31]\n"
"s_add_u32 s97, s97, s94\n"
"v_exp_f32 v38, v38\n"
"v_mfma_f32_16x16x32_bf16 v[24:27], v[204:207], a[88:91], v[24:27]\n"
"v_exp_f32 v134, v134\n"
"v_mfma_f32_16x16x32_bf16 v[24:27], v[200:203], a[72:75], v[24:27]\n"
"v_sub_f32 v39, -v135, v139\n"
"v_sub_f32 v135, v135, v138\n"
"v_mfma_f32_16x16x32_bf16 v[20:23], v[200:203], a[68:71], v[20:23]\n"
"v_exp_f32 v39, v39\n"
"v_mfma_f32_16x16x32_bf16 v[20:23], v[204:207], a[84:87], v[20:23]\n"
"v_exp_f32 v135, v135\n"
"v_mfma_f32_16x16x32_bf16 v[16:19], v[204:207], a[80:83], v[16:19]\n"
"ds_read_b32 v221, v220 offset:352\n"
"v_sub_f32 v40, -v136, v139\n"
"v_mfma_f32_16x16x32_bf16 v[16:19], v[200:203], a[64:67], v[16:19]\n"
"s_waitcnt lgkmcnt(2)\n"
"ds_read_b128 v[204:207], v222 offset:39936\n"
"ds_read_b128 v[200:203], v222 offset:41984\n"
"v_mfma_f32_16x16x32_bf16 v[0:3], v[196:199], a[96:99], v[0:3]\n"
"v_sub_f32 v136, v136, v138\n"
"v_mfma_f32_16x16x32_bf16 v[0:3], v[192:195], a[112:115], v[0:3]\n"
"v_exp_f32 v40, v40\n"
"v_mfma_f32_16x16x32_bf16 v[4:7], v[192:195], a[116:119], v[4:7]\n"
"v_exp_f32 v136, v136\n"
"v_mfma_f32_16x16x32_bf16 v[4:7], v[196:199], a[100:103], v[4:7]\n"
"s_waitcnt lgkmcnt(2)\n"
"v_cmp_ne_u32 vcc, s75, v221\n"
"v_add_f32 v138, v128, v129\n"
"v_mfma_f32_16x16x32_bf16 v[8:11], v[196:199], a[104:107], v[8:11]\n"
"v_add_f32 v139, v32, v33\n"
"v_add_f32 v138, v138, v130\n"
"v_mfma_f32_16x16x32_bf16 v[8:11], v[192:195], a[120:123], v[8:11]\n"
"s_cbranch_vccnz Lsb11_%=\n"
"Lsr11_%=:\n"
"s_add_u32 m0, s70, 18432\n"
"v_mfma_f32_16x16x32_bf16 v[12:15], v[192:195], a[124:127], v[12:15]\n"
"s_add_u32 s71, s72, 1769472\n"
"v_add_f32 v139, v139, v34\n"
"v_add_f32 v138, v138, v131\n"
"v_mfma_f32_16x16x32_bf16 v[12:15], v[196:199], a[108:111], v[12:15]\n"
"s_waitcnt lgkmcnt(0)\n"
"ds_read_b128 v[192:195], v222 offset:43008\n"
"ds_read_b128 v[196:199], v222 offset:45056\n"
"v_mfma_f32_16x16x32_bf16 v[28:31], v[204:207], a[108:111], v[28:31]\n"
"buffer_load_dwordx4 v213, s[40:43], s71 offen offset:0 lds\n"
"v_mfma_f32_16x16x32_bf16 v[28:31], v[200:203], a[124:127], v[28:31]\n"
"v_add_f32 v139, v139, v35\n"
"v_add_f32 v138, v138, v132\n"
"v_mfma_f32_16x16x32_bf16 v[24:27], v[200:203], a[120:123], v[24:27]\n"
"v_add_f32 v139, v139, v36\n"
"v_add_f32 v138, v138, v133\n"
"v_mfma_f32_16x16x32_bf16 v[24:27], v[204:207], a[104:107], v[24:27]\n"
"v_add_f32 v139, v139, v37\n"
"v_add_f32 v138, v138, v134\n"
"v_mfma_f32_16x16x32_bf16 v[20:23], v[204:207], a[100:103], v[20:23]\n"
"v_add_f32 v139, v139, v38\n"
"v_add_f32 v138, v138, v135\n"
"v_mfma_f32_16x16x32_bf16 v[20:23], v[200:203], a[116:119], v[20:23]\n"
"v_add_f32 v139, v139, v39\n"
"v_add_f32 v138, v138, v136\n"
"v_mfma_f32_16x16x32_bf16 v[16:19], v[200:203], a[112:115], v[16:19]\n"
"buffer_load_dwordx4 v213, s[44:47], s71 offen offset:1024 lds\n"
"v_mfma_f32_16x16x32_bf16 v[16:19], v[204:207], a[96:99], v[16:19]\n"
"s_waitcnt lgkmcnt(0)\n"
"ds_read_b128 v[200:203], v222 offset:44032\n"
"ds_read_b128 v[204:207], v222 offset:46080\n"
"v_mfma_f32_16x16x32_bf16 v[0:3], v[192:195], a[128:131], v[0:3]\n"
"s_add_u32 m0, s70, 26624\n"
"s_add_u32 s71, s72, 2064384\n"
"v_add_f32 v139, v139, v40\n"
"v_mfma_f32_16x16x32_bf16 v[0:3], v[196:199], a[144:147], v[0:3]\n"
"s_nop 0\n"
"v_permlane16_swap_b32 v138, v139\n"
"v_mfma_f32_16x16x32_bf16 v[4:7], v[196:199], a[148:151], v[4:7]\n"
"v_add_f32 v138, v138, v139\n"
"v_mfma_f32_16x16x32_bf16 v[4:7], v[192:195], a[132:135], v[4:7]\n"
"v_rcp_f32 v138, v138\n"
"v_mfma_f32_16x16x32_bf16 v[8:11], v[192:195], a[136:139], v[8:11]\n"
"v_mov_b32 v139, v138\n"
"v_mfma_f32_16x16x32_bf16 v[8:11], v[196:199], a[152:155], v[8:11]\n"
"buffer_load_dwordx4 v213, s[40:43], s71 offen offset:0 lds\n"
"v_mfma_f32_16x16x32_bf16 v[12:15], v[196:199], a[156:159], v[12:15]\n"
"v_permlane16_swap_b32 v138, v139\n"
"v_mfma_f32_16x16x32_bf16 v[12:15], v[192:195], a[140:143], v[12:15]\n"
"s_waitcnt lgkmcnt(0)\n"
"ds_read_b128 v[196:199], v222 offset:47104\n"
"ds_read_b128 v[192:195], v222 offset:49152\n"
"v_mfma_f32_16x16x32_bf16 v[28:31], v[200:203], a[140:143], v[28:31]\n"
"v_mul_f32 v128, v128, v138\n"
"v_mul_f32 v32, v32, v139\n"
"v_mfma_f32_16x16x32_bf16 v[28:31], v[204:207], a[156:159], v[28:31]\n"
"buffer_store_dwordx4 v[84:87], v44, s[36:39], s97 offen offset:0 sc1\n"
"v_mfma_f32_16x16x32_bf16 v[24:27], v[204:207], a[152:155], v[24:27]\n"
"s_add_u32 s97, s97, s94\n"
"v_mul_f32 v129, v129, v138\n"
"v_mul_f32 v33, v33, v139\n"
"v_mfma_f32_16x16x32_bf16 v[24:27], v[200:203], a[136:139], v[24:27]\n"
"v_mul_f32 v130, v130, v138\n"
"v_mul_f32 v34, v34, v139\n"
"v_mfma_f32_16x16x32_bf16 v[20:23], v[200:203], a[132:135], v[20:23]\n"
"v_mul_f32 v131, v131, v138\n"
"v_mul_f32 v35, v35, v139\n"
"v_mfma_f32_16x16x32_bf16 v[20:23], v[204:207], a[148:151], v[20:23]\n"
"v_mul_f32 v132, v132, v138\n"
"v_mul_f32 v36, v36, v139\n"
"v_mfma_f32_16x16x32_bf16 v[16:19], v[204:207], a[144:147], v[16:19]\n"
"v_mul_f32 v133, v133, v138\n"
"v_mul_f32 v37, v37, v139\n"
"v_mfma_f32_16x16x32_bf16 v[16:19], v[200:203], a[128:131], v[16:19]\n"
"s_waitcnt lgkmcnt(0)\n"
"ds_read_b128 v[204:207], v222 offset:48128\n"
"ds_read_b128 v[200:203], v222 offset:50176\n"
"v_mfma_f32_16x16x32_bf16 v[0:3], v[196:199], a[160:163], v[0:3]\n"
"s_waitcnt vmcnt(13)\n"
"buffer_store_dword v219, off, s[36:39], s23 offset:0 sc1\n"
"v_mfma_f32_16x16x32_bf16 v[0:3], v[192:195], a[176:179], v[0:3]\n"
"v_mul_f32 v134, v134, v138\n"
"v_mul_f32 v38, v38, v139\n"
"v_mfma_f32_16x16x32_bf16 v[4:7], v[192:195], a[180:183], v[4:7]\n"
"v_mul_f32 v135, v135, v138\n"
"v_mul_f32 v39, v39, v139\n"
"v_mfma_f32_16x16x32_bf16 v[4:7], v[196:199], a[164:167], v[4:7]\n"
"v_mul_f32 v136, v136, v138\n"
"v_mul_f32 v40, v40, v139\n"
"v_mfma_f32_16x16x32_bf16 v[8:11], v[196:199], a[168:171], v[8:11]\n"
"buffer_load_dwordx4 v213, s[44:47], s71 offen offset:1024 lds\n"
"v_mfma_f32_16x16x32_bf16 v[8:11], v[192:195], a[184:187], v[8:11]\n"
"v_cvt_pk_bf16_f32 v128, v128, v129\n"
"v_cvt_pk_bf16_f32 v129, v130, v131\n"
"v_mfma_f32_16x16x32_bf16 v[12:15], v[192:195], a[188:191], v[12:15]\n"
"v_add_u32 v222, 0xc000, v222\n"
"v_cvt_pk_bf16_f32 v130, v132, v133\n"
"v_mfma_f32_16x16x32_bf16 v[12:15], v[196:199], a[172:175], v[12:15]\n"
"s_waitcnt lgkmcnt(0)\n"
"ds_read_b128 v[192:195], v222 offset:2048\n"
"ds_read_b128 v[196:199], v222 offset:4096\n"
"v_mfma_f32_16x16x32_bf16 v[28:31], v[204:207], a[172:175], v[28:31]\n"
"v_cvt_pk_bf16_f32 v131, v134, v135\n"
"v_mfma_f32_16x16x32_bf16 v[28:31], v[200:203], a[188:191], v[28:31]\n"
"ds_write_b128 v215, v[128:131] offset:0\n"
"v_mfma_f32_16x16x32_bf16 v[24:27], v[200:203], a[184:187], v[24:27]\n"
"v_cvt_pk_bf16_f32 v32, v32, v33\n"
"v_cvt_pk_bf16_f32 v33, v34, v35\n"
"v_mfma_f32_16x16x32_bf16 v[24:27], v[204:207], a[168:171], v[24:27]\n"
"v_cvt_pk_bf16_f32 v34, v36, v37\n"
"v_cvt_pk_bf16_f32 v35, v38, v39\n"
"v_mfma_f32_16x16x32_bf16 v[20:23], v[204:207], a[164:167], v[20:23]\n"
"ds_write_b128 v215, v[32:35] offset:16\n"
"v_mfma_f32_16x16x32_bf16 v[20:23], v[200:203], a[180:183], v[20:23]\n"
"v_cvt_pk_bf16_f32 v136, v136, v40\n"
"v_mfma_f32_16x16x32_bf16 v[16:19], v[200:203], a[176:179], v[16:19]\n"
"ds_write_b32 v216, v136 offset:64\n"
"v_mfma_f32_16x16x32_bf16 v[16:19], v[204:207], a[160:163], v[16:19]\n"
"s_waitcnt lgkmcnt(3)\n"
"ds_read_b128 v[200:203], v222 offset:3072\n"
"ds_read_b128 v[204:207], v222 offset:5120\n"
"v_mfma_f32_16x16x32_bf16 v[0:3], v[192:195], a[192:195], v[0:3]\n"
"v_lshrrev_b32 v128, 1, v212\n"
"v_mfma_f32_16x16x32_bf16 v[0:3], v[196:199], a[208:211], v[0:3]\n"
"v_max3_f32 v154, v144, v145, v146\n"
"v_max3_f32 v155, -v144, -v145, -v146\n"
"v_mfma_f32_16x16x32_bf16 v[4:7], v[196:199], a[212:215], v[4:7]\n"
"v_max3_f32 v154, v154, v147, v148\n"
"v_max3_f32 v155, v155, -v147, -v148\n"
"v_mfma_f32_16x16x32_bf16 v[4:7], v[192:195], a[196:199], v[4:7]\n"
"v_max3_f32 v154, v154, v149, v150\n"
"v_max3_f32 v155, v155, -v149, -v150\n"
"v_mfma_f32_16x16x32_bf16 v[8:11], v[192:195], a[200:203], v[8:11]\n"
"v_max3_f32 v154, v154, v151, v152\n"
"v_max3_f32 v155, v155, -v151, -v152\n"
"v_mfma_f32_16x16x32_bf16 v[8:11], v[196:199], a[216:219], v[8:11]\n"
"v_cvt_pk_bf16_f32 v156, v156, v157\n"
"v_cvt_pk_bf16_f32 v157, v158, v159\n"
"v_mfma_f32_16x16x32_bf16 v[12:15], v[196:199], a[220:223], v[12:15]\n"
"buffer_store_dwordx4 v[100:103], v44, s[36:39], s98 offen offset:0 sc1\n"
"v_mfma_f32_16x16x32_bf16 v[12:15], v[192:195], a[204:207], v[12:15]\n"
"s_waitcnt lgkmcnt(0)\n"
"ds_read_b128 v[196:199], v222 offset:6144\n"
"ds_read_b128 v[192:195], v222 offset:8192\n"
"v_mfma_f32_16x16x32_bf16 v[28:31], v[200:203], a[204:207], v[28:31]\n"
"s_add_u32 s98, s98, s94\n"
"v_permlane16_swap_b32 v154, v155\n"
"v_mfma_f32_16x16x32_bf16 v[28:31], v[204:207], a[220:223], v[28:31]\n"
"v_max_f32 v154, v154, v155\n"
"v_mov_b32 v155, v154\n"
"v_mfma_f32_16x16x32_bf16 v[24:27], v[204:207], a[216:219], v[24:27]\n"
"v_cvt_pk_bf16_f32 v153, v153, 1.0\n"
"v_mfma_f32_16x16x32_bf16 v[24:27], v[200:203], a[200:203], v[24:27]\n"
"ds_write_b64 v217, v[156:157] offset:768\n"
"v_mfma_f32_16x16x32_bf16 v[20:23], v[200:203], a[196:199], v[20:23]\n"
"v_permlane16_swap_b32 v154, v155\n"
"v_mfma_f32_16x16x32_bf16 v[20:23], v[204:207], a[212:215], v[20:23]\n"
"ds_write_b32 v218, v153 offset:800\n"
"v_mfma_f32_16x16x32_bf16 v[16:19], v[204:207], a[208:211], v[16:19]\n"
"v_sub_f32 v132, -v144, v155\n"
"v_sub_f32 v144, v144, v154\n"
"v_mfma_f32_16x16x32_bf16 v[16:19], v[200:203], a[192:195], v[16:19]\n"
"s_waitcnt lgkmcnt(2)\n"
"ds_read_b128 v[204:207], v222 offset:7168\n"
"ds_read_b128 v[200:203], v222 offset:9216\n"
"v_mfma_f32_16x16x32_bf16 v[0:3], v[196:199], a[224:227], v[0:3]\n"
"v_exp_f32 v132, v132\n"
"v_mfma_f32_16x16x32_bf16 v[0:3], v[192:195], a[240:243], v[0:3]\n"
"v_exp_f32 v144, v144\n"
"v_mfma_f32_16x16x32_bf16 v[4:7], v[192:195], a[244:247], v[4:7]\n"
"v_sub_f32 v133, -v145, v155\n"
"v_sub_f32 v145, v145, v154\n"
"v_mfma_f32_16x16x32_bf16 v[4:7], v[196:199], a[228:231], v[4:7]\n"
"v_exp_f32 v133, v133\n"
"v_mfma_f32_16x16x32_bf16 v[8:11], v[196:199], a[232:235], v[8:11]\n"
"v_exp_f32 v145, v145\n"
"v_mfma_f32_16x16x32_bf16 v[8:11], v[192:195], a[248:251], v[8:11]\n"
"v_sub_f32 v134, -v146, v155\n"
"v_sub_f32 v146, v146, v154\n"
"v_mfma_f32_16x16x32_bf16 v[12:15], v[192:195], a[252:255], v[12:15]\n"
"s_waitcnt vmcnt(14)\n"
"ds_write_b32 v214, v219 offset:384\n"
"v_mfma_f32_16x16x32_bf16 v[12:15], v[196:199], a[236:239], v[12:15]\n"
"s_waitcnt lgkmcnt(1)\n"
"ds_read_b128 v[192:195], v222 offset:10240\n"
"ds_read_b128 v[196:199], v222 offset:12288\n"
"v_mfma_f32_16x16x32_bf16 v[28:31], v[204:207], a[236:239], v[28:31]\n"
"v_exp_f32 v134, v134\n"
"v_mfma_f32_16x16x32_bf16 v[28:31], v[200:203], a[252:255], v[28:31]\n"
"v_exp_f32 v146, v146\n"
"v_mfma_f32_16x16x32_bf16 v[24:27], v[200:203], a[248:251], v[24:27]\n"
"v_sub_f32 v135, -v147, v155\n"
"v_sub_f32 v147, v147, v154\n"
"v_mfma_f32_16x16x32_bf16 v[24:27], v[204:207], a[232:235], v[24:27]\n"
"v_exp_f32 v135, v135\n"
"v_mfma_f32_16x16x32_bf16 v[20:23], v[204:207], a[228:231], v[20:23]\n"
"v_exp_f32 v147, v147\n"
"v_mfma_f32_16x16x32_bf16 v[20:23], v[200:203], a[244:247], v[20:23]\n"
"ds_read_b32 v221, v220 offset:384\n"
"v_sub_f32 v136, -v148, v155\n"
"v_mfma_f32_16x16x32_bf16 v[16:19], v[200:203], a[240:243], v[16:19]\n"
"v_sub_f32 v148, v148, v154\n"
"v_mfma_f32_16x16x32_bf16 v[16:19], v[204:207], a[224:227], v[16:19]\n"
"s_waitcnt lgkmcnt(1)\n"
"ds_read_b128 v[200:203], v222 offset:11264\n"
"ds_read_b128 v[204:207], v222 offset:13312\n"
"v_mfma_f32_16x16x32_bf16 v[0:3], v[192:195], v[224:227], v[0:3]\n"
"v_exp_f32 v136, v136\n"
"v_mfma_f32_16x16x32_bf16 v[0:3], v[196:199], v[240:243], v[0:3]\n"
"v_exp_f32 v148, v148\n"
"v_mfma_f32_16x16x32_bf16 v[4:7], v[196:199], v[244:247], v[4:7]\n"
"s_waitcnt lgkmcnt(2)\n"
"v_cmp_ne_u32 vcc, s75, v221\n"
"v_sub_f32 v137, -v149, v155\n"
"v_mfma_f32_16x16x32_bf16 v[4:7], v[192:195], v[228:231], v[4:7]\n"
"v_sub_f32 v149, v149, v154\n"
"v_mfma_f32_16x16x32_bf16 v[8:11], v[192:195], v[232:235], v[8:11]\n"
"s_cbranch_vccnz Lsb12_%=\n"
"Lsr12_%=:\n"
"s_add_u32 m0, s70, 34816\n"
"v_mfma_f32_16x16x32_bf16 v[8:11], v[196:199], v[248:251], v[8:11]\n"
"s_add_u32 s71, s72, 2359296\n"
"v_add_u32 v210, 0xc000, v210\n"
"v_mfma_f32_16x16x32_bf16 v[12:15], v[196:199], v[252:255], v[12:15]\n"
"ds_read_b64_tr_b16 v[196:197], v210 offset:18432\n"
"ds_read_b64_tr_b16 v[198:199], v210 offset:18944\n"
"v_mfma_f32_16x16x32_bf16 v[12:15], v[192:195], v[236:239], v[12:15]\n"
"s_waitcnt lgkmcnt(2)\n"
"ds_read_b64_tr_b16 v[192:193], v210 offset:26624\n"
"ds_read_b64_tr_b16 v[194:195], v210 offset:27136\n"
"v_mfma_f32_16x16x32_bf16 v[28:31], v[200:203], v[236:239], v[28:31]\n"
"buffer_load_dwordx4 v213, s[40:43], s71 offen offset:0 lds\n"
"v_mfma_f32_16x16x32_bf16 v[28:31], v[204:207], v[252:255], v[28:31]\n"
"v_exp_f32 v137, v137\n"
"v_mfma_f32_16x16x32_bf16 v[24:27], v[204:207], v[248:251], v[24:27]\n"
"v_exp_f32 v149, v149\n"
"v_mfma_f32_16x16x32_bf16 v[24:27], v[200:203], v[232:235], v[24:27]\n"
"buffer_store_dwordx4 v[48:51], v44, s[36:39], s98 offen offset:0 sc1\n"
"v_mfma_f32_16x16x32_bf16 v[20:23], v[200:203], v[228:231], v[20:23]\n"
"s_add_u32 s98, s98, s94\n"
"v_sub_f32 v138, -v150, v155\n"
"v_sub_f32 v150, v150, v154\n"
"v_mfma_f32_16x16x32_bf16 v[20:23], v[204:207], v[244:247], v[20:23]\n"
"v_exp_f32 v138, v138\n"
"v_mfma_f32_16x16x32_bf16 v[16:19], v[204:207], v[240:243], v[16:19]\n"
"ds_read_b64_tr_b16 v[204:205], v210 offset:19456\n"
"ds_read_b64_tr_b16 v[206:207], v210 offset:19968\n"
"v_mfma_f32_16x16x32_bf16 v[16:19], v[200:203], v[224:227], v[16:19]\n"
"s_waitcnt lgkmcnt(2)\n"
"ds_read_b64_tr_b16 v[200:201], v210 offset:27648\n"
"ds_read_b64_tr_b16 v[202:203], v210 offset:28160\n"
"v_mfma_f32_16x16x32_bf16 v[64:67], v[196:199], a[0:3], 0\n"
"v_exp_f32 v150, v150\n"
"v_mfma_f32_16x16x32_bf16 v[64:67], v[192:195], a[16:19], v[64:67]\n"
"v_max_f32 v0, 0, v0\n"
"v_max_f32 v1, 0, v1\n"
"v_mfma_f32_16x16x32_bf16 v[68:71], v[192:195], a[20:23], 0\n"
"v_max_f32 v2, 0, v2\n"
"v_max_f32 v3, 0, v3\n"
"v_mfma_f32_16x16x32_bf16 v[68:71], v[196:199], a[4:7], v[68:71]\n"
"v_mul_f32 v16, v0, v16\n"
"v_mul_f32 v17, v1, v17\n"
"v_mfma_f32_16x16x32_bf16 v[72:75], v[196:199], a[8:11], 0\n"
"v_mul_f32 v18, v2, v18\n"
"v_mul_f32 v19, v3, v19\n"
"v_mfma_f32_16x16x32_bf16 v[72:75], v[192:195], a[24:27], v[72:75]\n"
"v_add_u32 v211, 0xc000, v211\n"
"v_cvt_pk_bf16_f32 v0, v16, v17\n"
"v_mfma_f32_16x16x32_bf16 v[76:79], v[192:195], a[28:31], 0\n"
"ds_read_b64_tr_b16 v[192:193], v211 offset:18432\n"
"ds_read_b64_tr_b16 v[194:195], v211 offset:18944\n"
"v_mfma_f32_16x16x32_bf16 v[76:79], v[196:199], a[12:15], v[76:79]\n"
"s_waitcnt lgkmcnt(2)\n"
"ds_read_b64_tr_b16 v[196:197], v211 offset:26624\n"
"ds_read_b64_tr_b16 v[198:199], v211 offset:27136\n"
"v_mfma_f32_16x16x32_bf16 v[92:95], v[204:207], a[12:15], 0\n"
"buffer_load_dwordx4 v213, s[44:47], s71 offen offset:1024 lds\n"
"v_mfma_f32_16x16x32_bf16 v[92:95], v[200:203], a[28:31], v[92:95]\n"
"s_add_u32 m0, s70, 43008\n"
"s_add_u32 s71, s72, 2654208\n"
"v_cvt_pk_bf16_f32 v1, v18, v19\n"
"v_mfma_f32_16x16x32_bf16 v[88:91], v[200:203], a[24:27], 0\n"
"v_sub_f32 v139, -v151, v155\n"
"v_sub_f32 v151, v151, v154\n"
"v_mfma_f32_16x16x32_bf16 v[88:91], v[204:207], a[8:11], v[88:91]\n"
"buffer_store_dwordx2 v[0:1], v128, s[36:39], s14 offen offset:0 sc1\n"
"v_mfma_f32_16x16x32_bf16 v[84:87], v[204:207], a[4:7], 0\n"
"v_max_f32 v4, 0, v4\n"
"v_max_f32 v5, 0, v5\n"
"v_mfma_f32_16x16x32_bf16 v[84:87], v[200:203], a[20:23], v[84:87]\n"
"v_max_f32 v6, 0, v6\n"
"v_max_f32 v7, 0, v7\n"
"v_mfma_f32_16x16x32_bf16 v[80:83], v[200:203], a[16:19], 0\n"
"ds_read_b64_tr_b16 v[200:201], v211 offset:19456\n"
"ds_read_b64_tr_b16 v[202:203], v211 offset:19968\n"
"v_mfma_f32_16x16x32_bf16 v[80:83], v[204:207], a[0:3], v[80:83]\n"
"s_waitcnt lgkmcnt(2)\n"
"ds_read_b64_tr_b16 v[204:205], v211 offset:27648\n"
"ds_read_b64_tr_b16 v[206:207], v211 offset:28160\n"
"v_mfma_f32_16x16x32_bf16 v[96:99], v[192:195], a[0:3], 0\n"
"v_mul_f32 v20, v4, v20\n"
"v_mul_f32 v21, v5, v21\n"
"v_mfma_f32_16x16x32_bf16 v[96:99], v[196:199], a[16:19], v[96:99]\n"
"v_mul_f32 v22, v6, v22\n"
"v_mul_f32 v23, v7, v23\n"
"v_mfma_f32_16x16x32_bf16 v[100:103], v[196:199], a[20:23], 0\n"
"buffer_load_dwordx4 v213, s[40:43], s71 offen offset:0 lds\n"
"v_mfma_f32_16x16x32_bf16 v[100:103], v[192:195], a[4:7], v[100:103]\n"
"v_cvt_pk_bf16_f32 v4, v20, v21\n"
"v_cvt_pk_bf16_f32 v5, v22, v23\n"
"v_mfma_f32_16x16x32_bf16 v[104:107], v[192:195], a[8:11], 0\n"
"v_exp_f32 v139, v139\n"
"v_mfma_f32_16x16x32_bf16 v[104:107], v[196:199], a[24:27], v[104:107]\n"
"buffer_store_dwordx2 v[4:5], v128, s[36:39], s14 offen offset:1024 sc1\n"
"v_mfma_f32_16x16x32_bf16 v[108:111], v[196:199], a[28:31], 0\n"
"ds_read_b64_tr_b16 v[196:197], v210 offset:34816\n"
"ds_read_b64_tr_b16 v[198:199], v210 offset:35328\n"
"v_mfma_f32_16x16x32_bf16 v[108:111], v[192:195], a[12:15], v[108:111]\n"
"s_waitcnt lgkmcnt(2)\n"
"ds_read_b64_tr_b16 v[192:193], v210 offset:43008\n"
"ds_read_b64_tr_b16 v[194:195], v210 offset:43520\n"
"v_mfma_f32_16x16x32_bf16 v[124:127], v[200:203], a[12:15], 0\n"
"v_max_f32 v8, 0, v8\n"
"v_max_f32 v9, 0, v9\n"
"v_mfma_f32_16x16x32_bf16 v[124:127], v[204:207], a[28:31], v[124:127]\n"
"buffer_store_dwordx4 v[52:55], v44, s[36:39], s98 offen offset:0 sc1\n"
"v_mfma_f32_16x16x32_bf16 v[120:123], v[204:207], a[24:27], 0\n"
"s_add_u32 s98, s98, s94\n"
"v_max_f32 v10, 0, v10\n"
"v_max_f32 v11, 0, v11\n"
"v_mfma_f32_16x16x32_bf16 v[120:123], v[200:203], a[8:11], v[120:123]\n"
"v_mul_f32 v24, v8, v24\n"
"v_mul_f32 v25, v9, v25\n"
"v_mfma_f32_16x16x32_bf16 v[116:119], v[200:203], a[4:7], 0\n"
"v_mul_f32 v26, v10, v26\n"
"v_mul_f32 v27, v11, v27\n"
"v_mfma_f32_16x16x32_bf16 v[116:119], v[204:207], a[20:23], v[116:119]\n"
"v_cvt_pk_bf16_f32 v8, v24, v25\n"
"v_cvt_pk_bf16_f32 v9, v26, v27\n"
"v_mfma_f32_16x16x32_bf16 v[112:115], v[204:207], a[16:19], 0\n"
"ds_read_b64_tr_b16 v[204:205], v210 offset:35840\n"
"ds_read_b64_tr_b16 v[206:207], v210 offset:36352\n"
"v_mfma_f32_16x16x32_bf16 v[112:115], v[200:203], a[0:3], v[112:115]\n"
"s_waitcnt lgkmcnt(2)\n"
"ds_read_b64_tr_b16 v[200:201], v210 offset:44032\n"
"ds_read_b64_tr_b16 v[202:203], v210 offset:44544\n"
"v_mfma_f32_16x16x32_bf16 v[64:67], v[196:199], a[32:35], v[64:67]\n"
"buffer_store_dwordx2 v[8:9], v128, s[36:39], s15 offen offset:0 sc1\n"
"v_mfma_f32_16x16x32_bf16 v[64:67], v[192:195], a[48:51], v[64:67]\n"
"v_max_f32 v12, 0, v12\n"
"v_max_f32 v13, 0, v13\n"
"v_mfma_f32_16x16x32_bf16 v[68:71], v[192:195], a[52:55], v[68:71]\n"
"v_max_f32 v14, 0, v14\n"
"v_max_f32 v15, 0, v15\n"
"v_mfma_f32_16x16x32_bf16 v[68:71], v[196:199], a[36:39], v[68:71]\n"
"v_mul_f32 v28, v12, v28\n"
"v_mul_f32 v29, v13, v29\n"
"v_mfma_f32_16x16x32_bf16 v[72:75], v[196:199], a[40:43], v[72:75]\n"
"v_and_b32 v129, 896, v212\n"
"v_lshlrev_b32 v129, 5, v129\n"
"v_mfma_f32_16x16x32_bf16 v[72:75], v[192:195], a[56:59], v[72:75]\n"
"s_waitcnt vmcnt(16)\n"
"ds_write_b32 v214, v219 offset:416\n"
"v_mfma_f32_16x16x32_bf16 v[76:79], v[192:195], a[60:63], v[76:79]\n"
"ds_read_b64_tr_b16 v[192:193], v211 offset:34816\n"
"ds_read_b64_tr_b16 v[194:195], v211 offset:35328\n"
"v_mfma_f32_16x16x32_bf16 v[76:79], v[196:199], a[44:47], v[76:79]\n"
"s_waitcnt lgkmcnt(3)\n"
"ds_read_b64_tr_b16 v[196:197], v211 offset:43008\n"
"ds_read_b64_tr_b16 v[198:199], v211 offset:43520\n"
"v_mfma_f32_16x16x32_bf16 v[92:95], v[204:207], a[44:47], v[92:95]\n"
"buffer_load_dwordx4 v213, s[44:47], s71 offen offset:1024 lds\n"
"v_mfma_f32_16x16x32_bf16 v[92:95], v[200:203], a[60:63], v[92:95]\n"
"v_mul_f32 v30, v14, v30\n"
"v_mul_f32 v31, v15, v31\n"
"v_mfma_f32_16x16x32_bf16 v[88:91], v[200:203], a[56:59], v[88:91]\n"
"v_cvt_pk_bf16_f32 v12, v28, v29\n"
"v_cvt_pk_bf16_f32 v13, v30, v31\n"
"v_mfma_f32_16x16x32_bf16 v[88:91], v[204:207], a[40:43], v[88:91]\n"
"buffer_load_dword v129, v129, s[36:39], s28 offen offset:0 sc1\n"
"v_mfma_f32_16x16x32_bf16 v[84:87], v[204:207], a[36:39], v[84:87]\n"
"v_exp_f32 v151, v151\n"
"v_mfma_f32_16x16x32_bf16 v[84:87], v[200:203], a[52:55], v[84:87]\n"
"ds_read_b32 v221, v220 offset:416\n"
"v_sub_f32 v140, -v152, v155\n"
"v_mfma_f32_16x16x32_bf16 v[80:83], v[200:203], a[48:51], v[80:83]\n"
"ds_read_b64_tr_b16 v[200:201], v211 offset:35840\n"
"ds_read_b64_tr_b16 v[202:203], v211 offset:36352\n"
"v_mfma_f32_16x16x32_bf16 v[80:83], v[204:207], a[32:35], v[80:83]\n"
"s_waitcnt lgkmcnt(3)\n"
"ds_read_b64_tr_b16 v[204:205], v211 offset:44032\n"
"ds_read_b64_tr_b16 v[206:207], v211 offset:44544\n"
"v_mfma_f32_16x16x32_bf16 v[96:99], v[192:195], a[32:35], v[96:99]\n"
"v_sub_f32 v152, v152, v154\n"
"v_mfma_f32_16x16x32_bf16 v[96:99], v[196:199], a[48:51], v[96:99]\n"
"v_exp_f32 v140, v140\n"
"v_mfma_f32_16x16x32_bf16 v[100:103], v[196:199], a[52:55], v[100:103]\n"
"s_waitcnt lgkmcnt(4)\n"
"v_cmp_ne_u32 vcc, s75, v221\n"
"v_mfma_f32_16x16x32_bf16 v[100:103], v[192:195], a[36:39], v[100:103]\n"
"buffer_store_dwordx2 v[12:13], v128, s[36:39], s15 offen offset:1024 sc1\n"
"v_mfma_f32_16x16x32_bf16 v[104:107], v[192:195], a[40:43], v[104:107]\n"
"s_cbranch_vccnz Lsb13_%=\n"
"Lsr13_%=:\n"
"s_add_u32 m0, s70, 51200\n"
"v_mfma_f32_16x16x32_bf16 v[104:107], v[196:199], a[56:59], v[104:107]\n"
"s_add_u32 s71, s72, 2949120\n"
"v_add_u32 v210, 0xffff4000, v210\n"
"v_mfma_f32_16x16x32_bf16 v[108:111], v[196:199], a[60:63], v[108:111]\n"
"ds_read_b64_tr_b16 v[196:197], v210 offset:2048\n"
"ds_read_b64_tr_b16 v[198:199], v210 offset:2560\n"
"v_mfma_f32_16x16x32_bf16 v[108:111], v[192:195], a[44:47], v[108:111]\n"
"s_waitcnt lgkmcnt(2)\n"
"ds_read_b64_tr_b16 v[192:193], v210 offset:10240\n"
"ds_read_b64_tr_b16 v[194:195], v210 offset:10752\n"
"v_mfma_f32_16x16x32_bf16 v[124:127], v[200:203], a[44:47], v[124:127]\n"
"buffer_load_dwordx4 v213, s[40:43], s71 offen offset:0 lds\n"
"v_mfma_f32_16x16x32_bf16 v[124:127], v[204:207], a[60:63], v[124:127]\n"
"v_exp_f32 v152, v152\n"
"v_mfma_f32_16x16x32_bf16 v[120:123], v[204:207], a[56:59], v[120:123]\n"
"v_add_f32 v154, v144, v145\n"
"v_add_f32 v155, v132, v133\n"
"v_mfma_f32_16x16x32_bf16 v[120:123], v[200:203], a[40:43], v[120:123]\n"
"v_add_f32 v154, v154, v146\n"
"v_add_f32 v155, v155, v134\n"
"v_mfma_f32_16x16x32_bf16 v[116:119], v[200:203], a[36:39], v[116:119]\n"
"buffer_store_dwordx4 v[56:59], v44, s[36:39], s98 offen offset:0 sc1\n"
"v_mfma_f32_16x16x32_bf16 v[116:119], v[204:207], a[52:55], v[116:119]\n"
"s_add_u32 s98, s98, s94\n"
"v_add_f32 v154, v154, v147\n"
"v_add_f32 v155, v155, v135\n"
"v_mfma_f32_16x16x32_bf16 v[112:115], v[204:207], a[48:51], v[112:115]\n"
"ds_read_b64_tr_b16 v[204:205], v210 offset:3072\n"
"ds_read_b64_tr_b16 v[206:207], v210 offset:3584\n"
"v_mfma_f32_16x16x32_bf16 v[112:115], v[200:203], a[32:35], v[112:115]\n"
"s_waitcnt lgkmcnt(2)\n"
"ds_read_b64_tr_b16 v[200:201], v210 offset:11264\n"
"ds_read_b64_tr_b16 v[202:203], v210 offset:11776\n"
"v_mfma_f32_16x16x32_bf16 v[64:67], v[196:199], a[64:67], v[64:67]\n"
"v_add_f32 v154, v154, v148\n"
"v_add_f32 v155, v155, v136\n"
"v_mfma_f32_16x16x32_bf16 v[64:67], v[192:195], a[80:83], v[64:67]\n"
"v_add_f32 v154, v154, v149\n"
"v_add_f32 v155, v155, v137\n"
"v_mfma_f32_16x16x32_bf16 v[68:71], v[192:195], a[84:87], v[68:71]\n"
"v_add_f32 v154, v154, v150\n"
"v_add_f32 v155, v155, v138\n"
"v_mfma_f32_16x16x32_bf16 v[68:71], v[196:199], a[68:71], v[68:71]\n"
"v_add_f32 v154, v154, v151\n"
"v_add_f32 v155, v155, v139\n"
"v_mfma_f32_16x16x32_bf16 v[72:75], v[196:199], a[72:75], v[72:75]\n"
"v_add_f32 v154, v154, v152\n"
"v_add_f32 v155, v155, v140\n"
"v_mfma_f32_16x16x32_bf16 v[72:75], v[192:195], a[88:91], v[72:75]\n"
"v_add_u32 v211, 0xffff4000, v211\n"
"v_mfma_f32_16x16x32_bf16 v[76:79], v[192:195], a[92:95], v[76:79]\n"
"ds_read_b64_tr_b16 v[192:193], v211 offset:2048\n"
"ds_read_b64_tr_b16 v[194:195], v211 offset:2560\n"
"v_mfma_f32_16x16x32_bf16 v[76:79], v[196:199], a[76:79], v[76:79]\n"
"s_waitcnt lgkmcnt(2)\n"
"ds_read_b64_tr_b16 v[196:197], v211 offset:10240\n"
"ds_read_b64_tr_b16 v[198:199], v211 offset:10752\n"
"v_mfma_f32_16x16x32_bf16 v[92:95], v[204:207], a[76:79], v[92:95]\n"
"buffer_load_dwordx4 v213, s[44:47], s71 offen offset:1024 lds\n"
"v_mfma_f32_16x16x32_bf16 v[92:95], v[200:203], a[92:95], v[92:95]\n"
"s_add_u32 m0, s70, 59392\n"
"s_add_u32 s71, s72, 3244032\n"
"v_mfma_f32_16x16x32_bf16 v[88:91], v[200:203], a[88:91], v[88:91]\n"
"v_permlane16_swap_b32 v154, v155\n"
"v_mfma_f32_16x16x32_bf16 v[88:91], v[204:207], a[72:75], v[88:91]\n"
"v_add_f32 v154, v154, v155\n"
"v_mfma_f32_16x16x32_bf16 v[84:87], v[204:207], a[68:71], v[84:87]\n"
"v_rcp_f32 v154, v154\n"
"v_mfma_f32_16x16x32_bf16 v[84:87], v[200:203], a[84:87], v[84:87]\n"
"v_mov_b32 v155, v154\n"
"v_mfma_f32_16x16x32_bf16 v[80:83], v[200:203], a[80:83], v[80:83]\n"
"ds_read_b64_tr_b16 v[200:201], v211 offset:3072\n"
"ds_read_b64_tr_b16 v[202:203], v211 offset:3584\n"
"v_mfma_f32_16x16x32_bf16 v[80:83], v[204:207], a[64:67], v[80:83]\n"
"s_waitcnt lgkmcnt(2)\n"
"ds_read_b64_tr_b16 v[204:205], v211 offset:11264\n"
"ds_read_b64_tr_b16 v[206:207], v211 offset:11776\n"
"v_mfma_f32_16x16x32_bf16 v[96:99], v[192:195], a[64:67], v[96:99]\n"
"buffer_load_dwordx4 v213, s[40:43], s71 offen offset:0 lds\n"
"v_mfma_f32_16x16x32_bf16 v[96:99], v[196:199], a[80:83], v[96:99]\n"
"v_permlane16_swap_b32 v154, v155\n"
"v_mfma_f32_16x16x32_bf16 v[100:103], v[196:199], a[84:87], v[100:103]\n"
"v_mul_f32 v144, v144, v154\n"
"v_mul_f32 v132, v132, v155\n"
"v_mfma_f32_16x16x32_bf16 v[100:103], v[192:195], a[68:71], v[100:103]\n"
"v_mul_f32 v145, v145, v154\n"
"v_mul_f32 v133, v133, v155\n"
"v_mfma_f32_16x16x32_bf16 v[104:107], v[192:195], a[72:75], v[104:107]\n"
"v_mul_f32 v146, v146, v154\n"
"v_mul_f32 v134, v134, v155\n"
"v_mfma_f32_16x16x32_bf16 v[104:107], v[196:199], a[88:91], v[104:107]\n"
"v_add_u32 v209, 0xc000, v209\n"
"v_mul_f32 v147, v147, v154\n"
"v_mfma_f32_16x16x32_bf16 v[108:111], v[196:199], a[92:95], v[108:111]\n"
"ds_read_b64_tr_b16 v[196:197], v209 offset:18432\n"
"ds_read_b64_tr_b16 v[198:199], v209 offset:18944\n"
"v_mfma_f32_16x16x32_bf16 v[108:111], v[192:195], a[76:79], v[108:111]\n"
"s_waitcnt lgkmcnt(2)\n"
"ds_read_b64_tr_b16 v[192:193], v209 offset:26624\n"
"ds_read_b64_tr_b16 v[194:195], v209 offset:27136\n"
"v_mfma_f32_16x16x32_bf16 v[124:127], v[200:203], a[76:79], v[124:127]\n"
"buffer_load_dwordx4 v213, s[44:47], s71 offen offset:1024 lds\n"
"v_mfma_f32_16x16x32_bf16 v[124:127], v[204:207], a[92:95], v[124:127]\n"
"v_mul_f32 v135, v135, v155\n"
"v_mul_f32 v148, v148, v154\n"
"v_mfma_f32_16x16x32_bf16 v[120:123], v[204:207], a[88:91], v[120:123]\n"
"v_mul_f32 v136, v136, v155\n"
"v_mul_f32 v149, v149, v154\n"
"v_mfma_f32_16x16x32_bf16 v[120:123], v[200:203], a[72:75], v[120:123]\n"
"buffer_store_dwordx4 v[60:63], v44, s[36:39], s96 offen offset:0 sc1\n"
"v_mfma_f32_16x16x32_bf16 v[116:119], v[200:203], a[68:71], v[116:119]\n"
"s_add_u32 s96, s96, s94\n"
"v_mul_f32 v137, v137, v155\n"
"v_mul_f32 v150, v150, v154\n"
"v_mfma_f32_16x16x32_bf16 v[116:119], v[204:207], a[84:87], v[116:119]\n"
"v_bfe_u32 v130, v212, 2, 7\n"
"v_add_u32 v130, s83, v130\n"
"v_mfma_f32_16x16x32_bf16 v[112:115], v[204:207], a[80:83], v[112:115]\n"
"Lpad5_%=:\n"
"s_cbranch_execz Lpad9_%=\n"
"ds_read_b64_tr_b16 v[204:205], v209 offset:19456\n"
"ds_read_b64_tr_b16 v[206:207], v209 offset:19968\n"
"v_mfma_f32_16x16x32_bf16 v[112:115], v[200:203], a[64:67], v[112:115]\n"
"s_waitcnt lgkmcnt(2)\n"
"ds_read_b64_tr_b16 v[200:201], v209 offset:27648\n"
"ds_read_b64_tr_b16 v[202:203], v209 offset:28160\n"
"v_mfma_f32_16x16x32_bf16 v[32:35], v[196:199], a[0:3], 0\n"
"v_mul_f32 v138, v138, v155\n"
"v_mul_f32 v151, v151, v154\n"
"v_mfma_f32_16x16x32_bf16 v[32:35], v[192:195], a[16:19], v[32:35]\n"
"v_mul_f32 v139, v139, v155\n"
"v_mul_f32 v152, v152, v154\n"
"v_mfma_f32_16x16x32_bf16 v[36:39], v[192:195], a[20:23], 0\n"
"v_mul_f32 v140, v140, v155\n"
"v_cvt_pk_bf16_f32 v144, v144, v145\n"
"v_mfma_f32_16x16x32_bf16 v[36:39], v[196:199], a[4:7], v[36:39]\n"
"v_cvt_pk_bf16_f32 v145, v146, v147\n"
"v_cvt_pk_bf16_f32 v146, v148, v149\n"
"v_mfma_f32_16x16x32_bf16 v[40:43], v[196:199], a[8:11], 0\n"
"s_waitcnt vmcnt(18)\n"
"ds_write_b32 v214, v219 offset:448\n"
"v_mfma_f32_16x16x32_bf16 v[40:43], v[192:195], a[24:27], v[40:43]\n"
"v_add_u32 v208, 0xc000, v208\n"
"v_cvt_pk_bf16_f32 v147, v150, v151\n"
"v_mfma_f32_16x16x32_bf16 v[44:47], v[192:195], a[28:31], 0\n"
"ds_read_b64_tr_b16 v[192:193], v208 offset:18432\n"
"ds_read_b64_tr_b16 v[194:195], v208 offset:18944\n"
"v_mfma_f32_16x16x32_bf16 v[44:47], v[196:199], a[12:15], v[44:47]\n"
"s_waitcnt lgkmcnt(3)\n"
"ds_read_b64_tr_b16 v[196:197], v208 offset:26624\n"
"ds_read_b64_tr_b16 v[198:199], v208 offset:27136\n"
"v_mfma_f32_16x16x32_bf16 v[60:63], v[204:207], a[12:15], 0\n"
"ds_write_b128 v215, v[144:147] offset:1280\n"
"v_mfma_f32_16x16x32_bf16 v[60:63], v[200:203], a[28:31], v[60:63]\n"
"v_cvt_pk_bf16_f32 v132, v132, v133\n"
"v_cvt_pk_bf16_f32 v133, v134, v135\n"
"v_mfma_f32_16x16x32_bf16 v[56:59], v[200:203], a[24:27], 0\n"
"v_cvt_pk_bf16_f32 v134, v136, v137\n"
"v_cvt_pk_bf16_f32 v135, v138, v139\n"
"v_mfma_f32_16x16x32_bf16 v[56:59], v[204:207], a[8:11], v[56:59]\n"
"ds_write_b128 v215, v[132:135] offset:1296\n"
"v_mfma_f32_16x16x32_bf16 v[52:55], v[204:207], a[4:7], 0\n"
"v_cvt_pk_bf16_f32 v152, v152, v140\n"
"v_mfma_f32_16x16x32_bf16 v[52:55], v[200:203], a[20:23], v[52:55]\n"
"ds_read_b32 v221, v220 offset:448\n"
"v_mfma_f32_16x16x32_bf16 v[48:51], v[200:203], a[16:19], 0\n"
"ds_read_b64_tr_b16 v[200:201], v208 offset:19456\n"
"ds_read_b64_tr_b16 v[202:203], v208 offset:19968\n"
"v_mfma_f32_16x16x32_bf16 v[48:51], v[204:207], a[0:3], v[48:51]\n"
"s_waitcnt lgkmcnt(5)\n"
"ds_read_b64_tr_b16 v[204:205], v208 offset:27648\n"
"ds_read_b64_tr_b16 v[206:207], v208 offset:28160\n"
"v_mfma_f32_16x16x32_bf16 v[0:3], v[192:195], a[0:3], 0\n"
"ds_write_b32 v216, v152 offset:1344\n"
"v_mfma_f32_16x16x32_bf16 v[0:3], v[196:199], a[16:19], v[0:3]\n"
"v_max3_f32 v170, v160, v161, v162\n"
"v_max3_f32 v171, -v160, -v161, -v162\n"
"v_mfma_f32_16x16x32_bf16 v[4:7], v[196:199], a[20:23], 0\n"
"s_waitcnt lgkmcnt(5)\n"
"v_cmp_ne_u32 vcc, s75, v221\n"
"v_max3_f32 v170, v170, v163, v164\n"
"v_mfma_f32_16x16x32_bf16 v[4:7], v[192:195], a[4:7], v[4:7]\n"
"v_max3_f32 v171, v171, -v163, -v164\n"
"v_max3_f32 v170, v170, v165, v166\n"
"v_mfma_f32_16x16x32_bf16 v[8:11], v[192:195], a[8:11], 0\n"
"s_cbranch_vccnz Lsb14_%=\n"
"Lsr14_%=:\n"
"v_max3_f32 v171, v171, -v165, -v166\n"
"v_mfma_f32_16x16x32_bf16 v[8:11], v[196:199], a[24:27], v[8:11]\n"
"v_max3_f32 v170, v170, v167, v168\n"
"v_max3_f32 v171, v171, -v167, -v168\n"
"v_mfma_f32_16x16x32_bf16 v[12:15], v[196:199], a[28:31], 0\n"
"ds_read_b64_tr_b16 v[196:197], v210 offset:18432\n"
"ds_read_b64_tr_b16 v[198:199], v210 offset:18944\n"
"v_mfma_f32_16x16x32_bf16 v[12:15], v[192:195], a[12:15], v[12:15]\n"
"s_waitcnt lgkmcnt(3)\n"
"ds_read_b64_tr_b16 v[192:193], v210 offset:26624\n"
"ds_read_b64_tr_b16 v[194:195], v210 offset:27136\n"
"v_mfma_f32_16x16x32_bf16 v[28:31], v[200:203], a[12:15], 0\n"
"v_cvt_pk_bf16_f32 v172, v172, v173\n"
"v_cvt_pk_bf16_f32 v173, v174, v175\n"
"v_mfma_f32_16x16x32_bf16 v[28:31], v[204:207], a[28:31], v[28:31]\n"
"v_permlane16_swap_b32 v170, v171\n"
"v_mfma_f32_16x16x32_bf16 v[24:27], v[204:207], a[24:27], 0\n"
"v_max_f32 v170, v170, v171\n"
"v_mov_b32 v171, v170\n"
"v_mfma_f32_16x16x32_bf16 v[24:27], v[200:203], a[8:11], v[24:27]\n"
"v_cvt_pk_bf16_f32 v169, v169, 1.0\n"
"v_mfma_f32_16x16x32_bf16 v[20:23], v[200:203], a[4:7], 0\n"
"ds_write_b64 v217, v[172:173] offset:1536\n"
"v_mfma_f32_16x16x32_bf16 v[20:23], v[204:207], a[20:23], v[20:23]\n"
"v_permlane16_swap_b32 v170, v171\n"
"v_mfma_f32_16x16x32_bf16 v[16:19], v[204:207], a[16:19], 0\n"
"ds_read_b64_tr_b16 v[204:205], v210 offset:19456\n"
"ds_read_b64_tr_b16 v[206:207], v210 offset:19968\n"
"v_mfma_f32_16x16x32_bf16 v[16:19], v[200:203], a[0:3], v[16:19]\n"
"s_waitcnt lgkmcnt(3)\n"
"ds_read_b64_tr_b16 v[200:201], v210 offset:27648\n"
"ds_read_b64_tr_b16 v[202:203], v210 offset:28160\n"
"v_mfma_f32_16x16x32_bf16 v[64:67], v[196:199], a[96:99], v[64:67]\n"
"ds_write_b32 v218, v169 offset:1568\n"
"v_mfma_f32_16x16x32_bf16 v[64:67], v[192:195], a[112:115], v[64:67]\n"
"v_sub_f32 v132, -v160, v171\n"
"v_sub_f32 v160, v160, v170\n"
"v_mfma_f32_16x16x32_bf16 v[68:71], v[192:195], a[116:119], v[68:71]\n"
"v_exp_f32 v132, v132\n"
"v_mfma_f32_16x16x32_bf16 v[68:71], v[196:199], a[100:103], v[68:71]\n"
"v_exp_f32 v160, v160\n"
"v_mfma_f32_16x16x32_bf16 v[72:75], v[196:199], a[104:107], v[72:75]\n"
"v_sub_f32 v133, -v161, v171\n"
"v_sub_f32 v161, v161, v170\n"
"v_mfma_f32_16x16x32_bf16 v[72:75], v[192:195], a[120:123], v[72:75]\n"
"v_exp_f32 v133, v133\n"
"v_mfma_f32_16x16x32_bf16 v[76:79], v[192:195], a[124:127], v[76:79]\n"
"ds_read_b64_tr_b16 v[192:193], v211 offset:18432\n"
"ds_read_b64_tr_b16 v[194:195], v211 offset:18944\n"
"v_mfma_f32_16x16x32_bf16 v[76:79], v[196:199], a[108:111], v[76:79]\n"
"s_waitcnt lgkmcnt(3)\n"
"ds_read_b64_tr_b16 v[196:197], v211 offset:26624\n"
"ds_read_b64_tr_b16 v[198:199], v211 offset:27136\n"
"v_mfma_f32_16x16x32_bf16 v[92:95], v[204:207], a[108:111], v[92:95]\n"
"v_exp_f32 v161, v161\n"
"v_mfma_f32_16x16x32_bf16 v[92:95], v[200:203], a[124:127], v[92:95]\n"
"v_sub_f32 v134, -v162, v171\n"
"v_sub_f32 v162, v162, v170\n"
"v_mfma_f32_16x16x32_bf16 v[88:91], v[200:203], a[120:123], v[88:91]\n"
"v_exp_f32 v134, v134\n"
"v_mfma_f32_16x16x32_bf16 v[88:91], v[204:207], a[104:107], v[88:91]\n"
"v_exp_f32 v162, v162\n"
"v_mfma_f32_16x16x32_bf16 v[84:87], v[204:207], a[100:103], v[84:87]\n"
"v_sub_f32 v135, -v163, v171\n"
"v_sub_f32 v163, v163, v170\n"
"v_mfma_f32_16x16x32_bf16 v[84:87], v[200:203], a[116:119], v[84:87]\n"
"v_exp_f32 v135, v135\n"
"v_mfma_f32_16x16x32_bf16 v[80:83], v[200:203], a[112:115], v[80:83]\n"
"ds_read_b64_tr_b16 v[200:201], v211 offset:19456\n"
"ds_read_b64_tr_b16 v[202:203], v211 offset:19968\n"
"v_mfma_f32_16x16x32_bf16 v[80:83], v[204:207], a[96:99], v[80:83]\n"
"s_waitcnt lgkmcnt(2)\n"
"ds_read_b64_tr_b16 v[204:205], v211 offset:27648\n"
"ds_read_b64_tr_b16 v[206:207], v211 offset:28160\n"
"v_mfma_f32_16x16x32_bf16 v[96:99], v[192:195], a[96:99], v[96:99]\n"
"s_waitcnt vmcnt(7)\n"
"v_cmp_ne_u32 s[34:35], s75, v129\n"
"v_mfma_f32_16x16x32_bf16 v[96:99], v[196:199], a[112:115], v[96:99]\n"
"v_exp_f32 v163, v163\n"
"v_mfma_f32_16x16x32_bf16 v[100:103], v[196:199], a[116:119], v[100:103]\n"
"s_cmp_lg_u64 s[34:35], 0\n"
"s_cbranch_scc1 Lslow3_%=\n"
"Lback3_%=:\n"
"v_mfma_f32_16x16x32_bf16 v[100:103], v[192:195], a[100:103], v[100:103]\n"
"buffer_load_dwordx4 a[0:3], v212, s[36:39], s20 offen offset:0 sc1\n"
"v_mfma_f32_16x16x32_bf16 v[104:107], v[192:195], a[104:107], v[104:107]\n"
"v_sub_f32 v136, -v164, v171\n"
"v_sub_f32 v164, v164, v170\n"
"v_mfma_f32_16x16x32_bf16 v[104:107], v[196:199], a[120:123], v[104:107]\n"
"v_exp_f32 v136, v136\n"
"v_mfma_f32_16x16x32_bf16 v[108:111], v[196:199], a[124:127], v[108:111]\n"
"ds_read_b64_tr_b16 v[196:197], v209 offset:34816\n"
"ds_read_b64_tr_b16 v[198:199], v209 offset:35328\n"
"v_mfma_f32_16x16x32_bf16 v[108:111], v[192:195], a[108:111], v[108:111]\n"
"s_waitcnt lgkmcnt(2)\n"
"ds_read_b64_tr_b16 v[192:193], v209 offset:43008\n"
"ds_read_b64_tr_b16 v[194:195], v209 offset:43520\n"
"v_mfma_f32_16x16x32_bf16 v[124:127], v[200:203], a[108:111], v[124:127]\n"
"buffer_load_dwordx4 a[4:7], v212, s[36:39], s20 offen offset:1024 sc1\n"
"v_mfma_f32_16x16x32_bf16 v[124:127], v[204:207], a[124:127], v[124:127]\n"
"v_exp_f32 v164, v164\n"
"v_mfma_f32_16x16x32_bf16 v[120:123], v[204:207], a[120:123], v[120:123]\n"
"v_sub_f32 v137, -v165, v171\n"
"v_sub_f32 v165, v165, v170\n"
"v_mfma_f32_16x16x32_bf16 v[120:123], v[200:203], a[104:107], v[120:123]\n"
"v_exp_f32 v137, v137\n"
"v_mfma_f32_16x16x32_bf16 v[116:119], v[200:203], a[100:103], v[116:119]\n"
"buffer_load_dwordx4 a[8:11], v212, s[36:39], s20 offen offset:2048 sc1\n"
"v_mfma_f32_16x16x32_bf16 v[116:119], v[204:207], a[116:119], v[116:119]\n"
"v_exp_f32 v165, v165\n"
"v_mfma_f32_16x16x32_bf16 v[112:115], v[204:207], a[112:115], v[112:115]\n"
"ds_read_b64_tr_b16 v[204:205], v209 offset:35840\n"
"ds_read_b64_tr_b16 v[206:207], v209 offset:36352\n"
"v_mfma_f32_16x16x32_bf16 v[112:115], v[200:203], a[96:99], v[112:115]\n"
"s_waitcnt lgkmcnt(2)\n"
"ds_read_b64_tr_b16 v[200:201], v209 offset:44032\n"
"ds_read_b64_tr_b16 v[202:203], v209 offset:44544\n"
"v_mfma_f32_16x16x32_bf16 v[32:35], v[196:199], a[32:35], v[32:35]\n"
"v_sub_f32 v138, -v166, v171\n"
"v_sub_f32 v166, v166, v170\n"
"v_mfma_f32_16x16x32_bf16 v[32:35], v[192:195], a[48:51], v[32:35]\n"
"buffer_load_dwordx4 a[12:15], v212, s[36:39], s20 offen offset:3072 sc1\n"
"v_mfma_f32_16x16x32_bf16 v[36:39], v[192:195], a[52:55], v[36:39]\n"
"s_add_u32 s20, s20, 4096\n"
"v_exp_f32 v138, v138\n"
"v_mfma_f32_16x16x32_bf16 v[36:39], v[196:199], a[36:39], v[36:39]\n"
"v_exp_f32 v166, v166\n"
"v_mfma_f32_16x16x32_bf16 v[40:43], v[196:199], a[40:43], v[40:43]\n"
"v_sub_f32 v139, -v167, v171\n"
"v_sub_f32 v167, v167, v170\n"
"v_mfma_f32_16x16x32_bf16 v[40:43], v[192:195], a[56:59], v[40:43]\n"
"ds_write_b32 v214, v219 offset:480\n"
"v_mfma_f32_16x16x32_bf16 v[44:47], v[192:195], a[60:63], v[44:47]\n"
"ds_read_b64_tr_b16 v[192:193], v208 offset:34816\n"
"ds_read_b64_tr_b16 v[194:195], v208 offset:35328\n"
"v_mfma_f32_16x16x32_bf16 v[44:47], v[196:199], a[44:47], v[44:47]\n"
"s_waitcnt lgkmcnt(3)\n"
"ds_read_b64_tr_b16 v[196:197], v208 offset:43008\n"
"ds_read_b64_tr_b16 v[198:199], v208 offset:43520\n"
"v_mfma_f32_16x16x32_bf16 v[60:63], v[204:207], a[44:47], v[60:63]\n"
"v_exp_f32 v139, v139\n"
"v_mfma_f32_16x16x32_bf16 v[60:63], v[200:203], a[60:63], v[60:63]\n"
"v_exp_f32 v167, v167\n"
"v_mfma_f32_16x16x32_bf16 v[56:59], v[200:203], a[56:59], v[56:59]\n"
"buffer_load_dwordx4 a[16:19], v212, s[36:39], s20 offen offset:0 sc1\n"
"v_mfma_f32_16x16x32_bf16 v[56:59], v[204:207], a[40:43], v[56:59]\n"
"v_sub_f32 v140, -v168, v171\n"
"v_sub_f32 v168, v168, v170\n"
"v_mfma_f32_16x16x32_bf16 v[52:55], v[204:207], a[36:39], v[52:55]\n"
"s_waitcnt vmcnt(11)\n"
"buffer_store_dword v219, off, s[36:39], s23 offset:128 sc1\n"
"v_mfma_f32_16x16x32_bf16 v[52:55], v[200:203], a[52:55], v[52:55]\n"
"ds_read_b32 v221, v220 offset:480\n"
"v_mfma_f32_16x16x32_bf16 v[48:51], v[200:203], a[48:51], v[48:51]\n"
"ds_read_b64_tr_b16 v[200:201], v208 offset:35840\n"
"ds_read_b64_tr_b16 v[202:203], v208 offset:36352\n"
"v_mfma_f32_16x16x32_bf16 v[48:51], v[204:207], a[32:35], v[48:51]\n"
"s_waitcnt lgkmcnt(3)\n"
"ds_read_b64_tr_b16 v[204:205], v208 offset:44032\n"
"ds_read_b64_tr_b16 v[206:207], v208 offset:44544\n"
"v_mfma_f32_16x16x32_bf16 v[0:3], v[192:195], a[32:35], v[0:3]\n"
"buffer_load_dwordx4 a[20:23], v212, s[36:39], s20 offen offset:1024 sc1\n"
"v_mfma_f32_16x16x32_bf16 v[0:3], v[196:199], a[48:51], v[0:3]\n"
"v_exp_f32 v140, v140\n"
"v_mfma_f32_16x16x32_bf16 v[4:7], v[196:199], a[52:55], v[4:7]\n"
"s_waitcnt lgkmcnt(4)\n"
"v_cmp_ne_u32 vcc, s75, v221\n"
"v_mfma_f32_16x16x32_bf16 v[4:7], v[192:195], a[36:39], v[4:7]\n"
"v_exp_f32 v168, v168\n"
"v_mfma_f32_16x16x32_bf16 v[8:11], v[192:195], a[40:43], v[8:11]\n"
"s_cbranch_vccnz Lsb15_%=\n"
"Lsr15_%=:\n"
"s_add_u32 m0, s70, 67584\n"
"v_mfma_f32_16x16x32_bf16 v[8:11], v[196:199], a[56:59], v[8:11]\n"
"s_add_u32 s71, s72, 3538944\n"
"v_add_f32 v170, v160, v161\n"
"v_add_f32 v171, v132, v133\n"
"v_mfma_f32_16x16x32_bf16 v[12:15], v[196:199], a[60:63], v[12:15]\n"
"ds_read_b64_tr_b16 v[196:197], v210 offset:34816\n"
"ds_read_b64_tr_b16 v[198:199], v210 offset:35328\n"
"v_mfma_f32_16x16x32_bf16 v[12:15], v[192:195], a[44:47], v[12:15]\n"
"s_waitcnt lgkmcnt(2)\n"
"ds_read_b64_tr_b16 v[192:193], v210 offset:43008\n"
"ds_read_b64_tr_b16 v[194:195], v210 offset:43520\n"
"v_mfma_f32_16x16x32_bf16 v[28:31], v[200:203], a[44:47], v[28:31]\n"
"buffer_load_dwordx4 v213, s[40:43], s71 offen offset:0 lds\n"
"v_mfma_f32_16x16x32_bf16 v[28:31], v[204:207], a[60:63], v[28:31]\n"
"v_add_f32 v170, v170, v162\n"
"v_add_f32 v171, v171, v134\n"
"v_mfma_f32_16x16x32_bf16 v[24:27], v[204:207], a[56:59], v[24:27]\n"
"v_add_f32 v170, v170, v163\n"
"v_add_f32 v171, v171, v135\n"
"v_mfma_f32_16x16x32_bf16 v[24:27], v[200:203], a[40:43], v[24:27]\n"
"v_add_f32 v170, v170, v164\n"
"v_add_f32 v171, v171, v136\n"
"v_mfma_f32_16x16x32_bf16 v[20:23], v[200:203], a[36:39], v[20:23]\n"
"buffer_load_dwordx4 v213, s[44:47], s71 offen offset:1024 lds\n"
"v_mfma_f32_16x16x32_bf16 v[20:23], v[204:207], a[52:55], v[20:23]\n"
"s_add_u32 m0, s70, 75776\n"
"s_add_u32 s71, s72, 3833856\n"
"v_add_f32 v170, v170, v165\n"
"v_mfma_f32_16x16x32_bf16 v[16:19], v[204:207], a[48:51], v[16:19]\n"
"ds_read_b64_tr_b16 v[204:205], v210 offset:35840\n"
"ds_read_b64_tr_b16 v[206:207], v210 offset:36352\n"
"v_mfma_f32_16x16x32_bf16 v[16:19], v[200:203], a[32:35], v[16:19]\n"
"s_waitcnt lgkmcnt(2)\n"
"ds_read_b64_tr_b16 v[200:201], v210 offset:44032\n"
"ds_read_b64_tr_b16 v[202:203], v210 offset:44544\n"
"v_mfma_f32_16x16x32_bf16 v[64:67], v[196:199], a[128:131], v[64:67]\n"
"v_add_f32 v171, v171, v137\n"
"v_add_f32 v170, v170, v166\n"
"v_mfma_f32_16x16x32_bf16 v[64:67], v[192:195], a[144:147], v[64:67]\n"
"buffer_load_dwordx4 v213, s[40:43], s71 offen offset:0 lds\n"
"v_mfma_f32_16x16x32_bf16 v[68:71], v[192:195], a[148:151], v[68:71]\n"
"s_waitcnt vmcnt(10)\n"
"buffer_store_dword v219, v130, s[36:39], s69 offen offset:0 sc1\n"
"v_mfma_f32_16x16x32_bf16 v[68:71], v[196:199], a[132:135], v[68:71]\n"
"v_add_f32 v171, v171, v138\n"
"v_add_f32 v170, v170, v167\n"
"v_mfma_f32_16x16x32_bf16 v[72:75], v[196:199], a[136:139], v[72:75]\n"
"v_add_f32 v171, v171, v139\n"
"v_add_f32 v170, v170, v168\n"
"v_mfma_f32_16x16x32_bf16 v[72:75], v[192:195], a[152:155], v[72:75]\n"
"v_add_f32 v171, v171, v140\n"
"v_mfma_f32_16x16x32_bf16 v[76:79], v[192:195], a[156:159], v[76:79]\n"
"ds_read_b64_tr_b16 v[192:193], v211 offset:34816\n"
"ds_read_b64_tr_b16 v[194:195], v211 offset:35328\n"
"v_mfma_f32_16x16x32_bf16 v[76:79], v[196:199], a[140:143], v[76:79]\n"
"s_waitcnt lgkmcnt(2)\n"
"ds_read_b64_tr_b16 v[196:197], v211 offset:43008\n"
"ds_read_b64_tr_b16 v[198:199], v211 offset:43520\n"
"v_mfma_f32_16x16x32_bf16 v[92:95], v[204:207], a[140:143], v[92:95]\n"
"buffer_load_dwordx4 v213, s[44:47], s71 offen offset:1024 lds\n"
"v_mfma_f32_16x16x32_bf16 v[92:95], v[200:203], a[156:159], v[92:95]\n"
"v_permlane16_swap_b32 v170, v171\n"
"v_mfma_f32_16x16x32_bf16 v[88:91], v[200:203], a[152:155], v[88:91]\n"
"v_add_f32 v170, v170, v171\n"
"v_mfma_f32_16x16x32_bf16 v[88:91], v[204:207], a[136:139], v[88:91]\n"
"v_rcp_f32 v170, v170\n"
"v_mfma_f32_16x16x32_bf16 v[84:87], v[204:207], a[132:135], v[84:87]\n"
"buffer_load_dwordx4 a[24:27], v212, s[36:39], s20 offen offset:2048 sc1\n"
"v_mfma_f32_16x16x32_bf16 v[84:87], v[200:203], a[148:151], v[84:87]\n"
"v_mov_b32 v171, v170\n"
"v_mfma_f32_16x16x32_bf16 v[80:83], v[200:203], a[144:147], v[80:83]\n"
"ds_read_b64_tr_b16 v[200:201], v211 offset:35840\n"
"ds_read_b64_tr_b16 v[202:203], v211 offset:36352\n"
"v_mfma_f32_16x16x32_bf16 v[80:83], v[204:207], a[128:131], v[80:83]\n"
"s_waitcnt lgkmcnt(2)\n"
"ds_read_b64_tr_b16 v[204:205], v211 offset:44032\n"
"ds_read_b64_tr_b16 v[206:207], v211 offset:44544\n"
"v_mfma_f32_16x16x32_bf16 v[96:99], v[192:195], a[128:131], v[96:99]\n"
"v_permlane16_swap_b32 v170, v171\n"
"v_mfma_f32_16x16x32_bf16 v[96:99], v[196:199], a[144:147], v[96:99]\n"
"buffer_load_dwordx4 a[28:31], v212, s[36:39], s20 offen offset:3072 sc1\n"
"v_mfma_f32_16x16x32_bf16 v[100:103], v[196:199], a[148:151], v[100:103]\n"
"s_add_u32 s20, s20, 4096\n"
"v_mul_f32 v160, v160, v170\n"
"v_mul_f32 v132, v132, v171\n"
"v_mfma_f32_16x16x32_bf16 v[100:103], v[192:195], a[132:135], v[100:103]\n"
"v_mul_f32 v161, v161, v170\n"
"v_mul_f32 v133, v133, v171\n"
"v_mfma_f32_16x16x32_bf16 v[104:107], v[192:195], a[136:139], v[104:107]\n"
"v_mul_f32 v162, v162, v170\n"
"v_mul_f32 v134, v134, v171\n"
"v_mfma_f32_16x16x32_bf16 v[104:107], v[196:199], a[152:155], v[104:107]\n"
"v_add_u32 v209, 0xffff4000, v209\n"
"v_mul_f32 v163, v163, v170\n"
"v_mfma_f32_16x16x32_bf16 v[108:111], v[196:199], a[156:159], v[108:111]\n"
"ds_read_b64_tr_b16 v[196:197], v209 offset:2048\n"
"ds_read_b64_tr_b16 v[198:199], v209 offset:2560\n"
"v_mfma_f32_16x16x32_bf16 v[108:111], v[192:195], a[140:143], v[108:111]\n"
"s_waitcnt lgkmcnt(2)\n"
"ds_read_b64_tr_b16 v[192:193], v209 offset:10240\n"
"ds_read_b64_tr_b16 v[194:195], v209 offset:10752\n"
"v_mfma_f32_16x16x32_bf16 v[124:127], v[200:203], a[140:143], v[124:127]\n"
"buffer_load_dwordx4 a[32:35], v212, s[36:39], s20 offen offset:0 sc1\n"
"v_mfma_f32_16x16x32_bf16 v[124:127], v[204:207], a[156:159], v[124:127]\n"
"v_mul_f32 v135, v135, v171\n"
"v_mul_f32 v164, v164, v170\n"
"v_mfma_f32_16x16x32_bf16 v[120:123], v[204:207], a[152:155], v[120:123]\n"
"v_mul_f32 v136, v136, v171\n"
"v_mul_f32 v165, v165, v170\n"
"v_mfma_f32_16x16x32_bf16 v[120:123], v[200:203], a[136:139], v[120:123]\n"
"v_mul_f32 v137, v137, v171\n"
"v_mul_f32 v166, v166, v170\n"
"v_mfma_f32_16x16x32_bf16 v[116:119], v[200:203], a[132:135], v[116:119]\n"
"buffer_load_dwordx4 a[36:39], v212, s[36:39], s20 offen offset:1024 sc1\n"
"v_mfma_f32_16x16x32_bf16 v[116:119], v[204:207], a[148:151], v[116:119]\n"
"v_mul_f32 v138, v138, v171\n"
"v_mul_f32 v167, v167, v170\n"
"v_mfma_f32_16x16x32_bf16 v[112:115], v[204:207], a[144:147], v[112:115]\n"
"ds_read_b64_tr_b16 v[204:205], v209 offset:3072\n"
"ds_read_b64_tr_b16 v[206:207], v209 offset:3584\n"
"v_mfma_f32_16x16x32_bf16 v[112:115], v[200:203], a[128:131], v[112:115]\n"
"s_waitcnt lgkmcnt(2)\n"
"ds_read_b64_tr_b16 v[200:201], v209 offset:11264\n"
"ds_read_b64_tr_b16 v[202:203], v209 offset:11776\n"
"v_mfma_f32_16x16x32_bf16 v[32:35], v[196:199], a[64:67], v[32:35]\n"
"v_mul_f32 v139, v139, v171\n"
"v_mul_f32 v168, v168, v170\n"
"v_mfma_f32_16x16x32_bf16 v[32:35], v[192:195], a[80:83], v[32:35]\n"
"buffer_load_dwordx4 a[40:43], v212, s[36:39], s20 offen offset:2048 sc1\n"
"v_mfma_f32_16x16x32_bf16 v[36:39], v[192:195], a[84:87], v[36:39]\n"
"v_mul_f32 v140, v140, v171\n"
"v_cvt_pk_bf16_f32 v160, v160, v161\n"
"v_mfma_f32_16x16x32_bf16 v[36:39], v[196:199], a[68:71], v[36:39]\n"
"v_cvt_pk_bf16_f32 v161, v162, v163\n"
"v_cvt_pk_bf16_f32 v162, v164, v165\n"
"v_mfma_f32_16x16x32_bf16 v[40:43], v[196:199], a[72:75], v[40:43]\n"
"ds_write_b32 v214, v219 offset:512\n"
"v_mfma_f32_16x16x32_bf16 v[40:43], v[192:195], a[88:91], v[40:43]\n"
"v_add_u32 v208, 0xffff4000, v208\n"
"v_cvt_pk_bf16_f32 v163, v166, v167\n"
"v_mfma_f32_16x16x32_bf16 v[44:47], v[192:195], a[92:95], v[44:47]\n"
"ds_read_b64_tr_b16 v[192:193], v208 offset:2048\n"
"ds_read_b64_tr_b16 v[194:195], v208 offset:2560\n"
"v_mfma_f32_16x16x32_bf16 v[44:47], v[196:199], a[76:79], v[44:47]\n"
"s_waitcnt lgkmcnt(3)\n"
"ds_read_b64_tr_b16 v[196:197], v208 offset:10240\n"
"ds_read_b64_tr_b16 v[198:199], v208 offset:10752\n"
"v_mfma_f32_16x16x32_bf16 v[60:63], v[204:207], a[76:79], v[60:63]\n"
"buffer_load_dwordx4 a[44:47], v212, s[36:39], s20 offen offset:3072 sc1\n"
"v_mfma_f32_16x16x32_bf16 v[60:63], v[200:203], a[92:95], v[60:63]\n"
"s_add_u32 s20, s20, 4096\n"
"ds_write_b128 v215, v[160:163] offset:2560\n"
"v_mfma_f32_16x16x32_bf16 v[56:59], v[200:203], a[88:91], v[56:59]\n"
"v_cvt_pk_bf16_f32 v132, v132, v133\n"
"v_cvt_pk_bf16_f32 v133, v134, v135\n"
"v_mfma_f32_16x16x32_bf16 v[56:59], v[204:207], a[72:75], v[56:59]\n"
"v_cvt_pk_bf16_f32 v134, v136, v137\n"
"v_cvt_pk_bf16_f32 v135, v138, v139\n"
"v_mfma_f32_16x16x32_bf16 v[52:55], v[204:207], a[68:71], v[52:55]\n"
"buffer_load_dwordx4 a[48:51], v212, s[36:39], s20 offen offset:0 sc1\n"
"v_mfma_f32_16x16x32_bf16 v[52:55], v[200:203], a[84:87], v[52:55]\n"
"ds_read_b32 v221, v220 offset:512\n"
"v_mfma_f32_16x16x32_bf16 v[48:51], v[200:203], a[80:83], v[48:51]\n"
"ds_read_b64_tr_b16 v[200:201], v208 offset:3072\n"
"ds_read_b64_tr_b16 v[202:203], v208 offset:3584\n"
"v_mfma_f32_16x16x32_bf16 v[48:51], v[204:207], a[64:67], v[48:51]\n"
"s_waitcnt lgkmcnt(4)\n"
"ds_read_b64_tr_b16 v[204:205], v208 offset:11264\n"
"ds_read_b64_tr_b16 v[206:207], v208 offset:11776\n"
"v_mfma_f32_16x16x32_bf16 v[0:3], v[192:195], a[64:67], v[0:3]\n"
"ds_write_b128 v215, v[132:135] offset:2576\n"
"v_mfma_f32_16x16x32_bf16 v[0:3], v[196:199], a[80:83], v[0:3]\n"
"buffer_load_dwordx4 a[52:55], v212, s[36:39], s20 offen offset:1024 sc1\n"
"v_mfma_f32_16x16x32_bf16 v[4:7], v[196:199], a[84:87], v[4:7]\n"
"s_waitcnt lgkmcnt(5)\n"
"v_cmp_ne_u32 vcc, s75, v221\n"
"v_cvt_pk_bf16_f32 v168, v168, v140\n"
"v_mfma_f32_16x16x32_bf16 v[4:7], v[192:195], a[68:71], v[4:7]\n"
"ds_write_b32 v216, v168 offset:2624\n"
"v_mfma_f32_16x16x32_bf16 v[8:11], v[192:195], a[72:75], v[8:11]\n"
"s_cbranch_vccnz Lsb16_%=\n"
"Lsr16_%=:\n"
"s_add_u32 m0, s70, 83968\n"
"v_mfma_f32_16x16x32_bf16 v[8:11], v[196:199], a[88:91], v[8:11]\n"
"s_add_u32 s71, s72, 4128768\n"
"v_add_u32 v210, 0xc000, v210\n"
"v_max3_f32 v186, v176, v177, v178\n"
"v_mfma_f32_16x16x32_bf16 v[12:15], v[196:199], a[92:95], v[12:15]\n"
"ds_read_b64_tr_b16 v[196:197], v210 offset:2048\n"
"ds_read_b64_tr_b16 v[198:199], v210 offset:2560\n"
"v_mfma_f32_16x16x32_bf16 v[12:15], v[192:195], a[76:79], v[12:15]\n"
"s_waitcnt lgkmcnt(4)\n"
"ds_read_b64_tr_b16 v[192:193], v210 offset:10240\n"
"ds_read_b64_tr_b16 v[194:195], v210 offset:10752\n"
"v_mfma_f32_16x16x32_bf16 v[28:31], v[200:203], a[76:79], v[28:31]\n"
"buffer_load_dwordx4 v213, s[40:43], s71 offen offset:0 lds\n"
"v_mfma_f32_16x16x32_bf16 v[28:31], v[204:207], a[92:95], v[28:31]\n"
"v_max3_f32 v187, -v176, -v177, -v178\n"
"v_max3_f32 v186, v186, v179, v180\n"
"v_mfma_f32_16x16x32_bf16 v[24:27], v[204:207], a[88:91], v[24:27]\n"
"v_max3_f32 v187, v187, -v179, -v180\n"
"v_max3_f32 v186, v186, v181, v182\n"
"v_mfma_f32_16x16x32_bf16 v[24:27], v[200:203], a[72:75], v[24:27]\n"
"v_max3_f32 v187, v187, -v181, -v182\n"
"v_max3_f32 v186, v186, v183, v184\n"
"v_mfma_f32_16x16x32_bf16 v[20:23], v[200:203], a[68:71], v[20:23]\n"
"buffer_load_dwordx4 v213, s[44:47], s71 offen offset:1024 lds\n"
"v_mfma_f32_16x16x32_bf16 v[20:23], v[204:207], a[84:87], v[20:23]\n"
"s_add_u32 m0, s70, 92160\n"
"s_add_u32 s71, s72, 4423680\n"
"v_max3_f32 v187, v187, -v183, -v184\n"
"v_mfma_f32_16x16x32_bf16 v[16:19], v[204:207], a[80:83], v[16:19]\n"
"ds_read_b64_tr_b16 v[204:205], v210 offset:3072\n"
"ds_read_b64_tr_b16 v[206:207], v210 offset:3584\n"
"v_mfma_f32_16x16x32_bf16 v[16:19], v[200:203], a[64:67], v[16:19]\n"
"s_waitcnt lgkmcnt(2)\n"
"ds_read_b64_tr_b16 v[200:201], v210 offset:11264\n"
"ds_read_b64_tr_b16 v[202:203], v210 offset:11776\n"
"v_mfma_f32_16x16x32_bf16 v[64:67], v[196:199], a[160:163], v[64:67]\n"
"v_cvt_pk_bf16_f32 v188, v188, v189\n"
"v_cvt_pk_bf16_f32 v189, v190, v191\n"
"v_mfma_f32_16x16x32_bf16 v[64:67], v[192:195], a[176:179], v[64:67]\n"
"buffer_load_dwordx4 v213, s[40:43], s71 offen offset:0 lds\n"
"v_mfma_f32_16x16x32_bf16 v[68:71], v[192:195], a[180:183], v[68:71]\n"
"v_permlane16_swap_b32 v186, v187\n"
"v_mfma_f32_16x16x32_bf16 v[68:71], v[196:199], a[164:167], v[68:71]\n"
"v_max_f32 v186, v186, v187\n"
"v_mov_b32 v187, v186\n"
"v_mfma_f32_16x16x32_bf16 v[72:75], v[196:199], a[168:171], v[72:75]\n"
"v_cvt_pk_bf16_f32 v185, v185, 1.0\n"
"v_mfma_f32_16x16x32_bf16 v[72:75], v[192:195], a[184:187], v[72:75]\n"
"v_add_u32 v211, 0xc000, v211\n"
"v_mfma_f32_16x16x32_bf16 v[76:79], v[192:195], a[188:191], v[76:79]\n"
"ds_read_b64_tr_b16 v[192:193], v211 offset:2048\n"
"ds_read_b64_tr_b16 v[194:195], v211 offset:2560\n"
"v_mfma_f32_16x16x32_bf16 v[76:79], v[196:199], a[172:175], v[76:79]\n"
"s_waitcnt lgkmcnt(2)\n"
"ds_read_b64_tr_b16 v[196:197], v211 offset:10240\n"
"ds_read_b64_tr_b16 v[198:199], v211 offset:10752\n"
"v_mfma_f32_16x16x32_bf16 v[92:95], v[204:207], a[172:175], v[92:95]\n"
"buffer_load_dwordx4 v213, s[44:47], s71 offen offset:1024 lds\n"
"v_mfma_f32_16x16x32_bf16 v[92:95], v[200:203], a[188:191], v[92:95]\n"
"ds_write_b64 v217, v[188:189] offset:2304\n"
"v_mfma_f32_16x16x32_bf16 v[88:91], v[200:203], a[184:187], v[88:91]\n"
"v_permlane16_swap_b32 v186, v187\n"
"v_mfma_f32_16x16x32_bf16 v[88:91], v[204:207], a[168:171], v[88:91]\n"
"ds_write_b32 v218, v185 offset:2336\n"
"v_mfma_f32_16x16x32_bf16 v[84:87], v[204:207], a[164:167], v[84:87]\n"
"buffer_load_dwordx4 a[56:59], v212, s[36:39], s20 offen offset:2048 sc1\n"
"v_mfma_f32_16x16x32_bf16 v[84:87], v[200:203], a[180:183], v[84:87]\n"
"v_sub_f32 v132, -v176, v187\n"
"v_sub_f32 v176, v176, v186\n"
"v_mfma_f32_16x16x32_bf16 v[80:83], v[200:203], a[176:179], v[80:83]\n"
"ds_read_b64_tr_b16 v[200:201], v211 offset:3072\n"
"ds_read_b64_tr_b16 v[202:203], v211 offset:3584\n"
"v_mfma_f32_16x16x32_bf16 v[80:83], v[204:207], a[160:163], v[80:83]\n"
"s_waitcnt lgkmcnt(4)\n"
"ds_read_b64_tr_b16 v[204:205], v211 offset:11264\n"
"ds_read_b64_tr_b16 v[206:207], v211 offset:11776\n"
"v_mfma_f32_16x16x32_bf16 v[96:99], v[192:195], a[160:163], v[96:99]\n"
"v_exp_f32 v132, v132\n"
"v_mfma_f32_16x16x32_bf16 v[96:99], v[196:199], a[176:179], v[96:99]\n"
"buffer_load_dwordx4 a[60:63], v212, s[36:39], s20 offen offset:3072 sc1\n"
"v_mfma_f32_16x16x32_bf16 v[100:103], v[196:199], a[180:183], v[100:103]\n"
"s_add_u32 s20, s20, 4096\n"
"v_exp_f32 v176, v176\n"
"v_mfma_f32_16x16x32_bf16 v[100:103], v[192:195], a[164:167], v[100:103]\n"
"v_sub_f32 v133, -v177, v187\n"
"v_sub_f32 v177, v177, v186\n"
"v_mfma_f32_16x16x32_bf16 v[104:107], v[192:195], a[168:171], v[104:107]\n"
"v_exp_f32 v133, v133\n"
"v_mfma_f32_16x16x32_bf16 v[104:107], v[196:199], a[184:187], v[104:107]\n"
"buffer_load_dwordx4 a[64:67], v212, s[36:39], s20 offen offset:0 sc1\n"
"v_mfma_f32_16x16x32_bf16 v[108:111], v[196:199], a[188:191], v[108:111]\n"
"ds_read_b64_tr_b16 v[196:197], v209 offset:18432\n"
"ds_read_b64_tr_b16 v[198:199], v209 offset:18944\n"
"v_mfma_f32_16x16x32_bf16 v[108:111], v[192:195], a[172:175], v[108:111]\n"
"s_waitcnt lgkmcnt(2)\n"
"ds_read_b64_tr_b16 v[192:193], v209 offset:26624\n"
"ds_read_b64_tr_b16 v[194:195], v209 offset:27136\n"
"v_mfma_f32_16x16x32_bf16 v[124:127], v[200:203], a[172:175], v[124:127]\n"
"v_exp_f32 v177, v177\n"
"v_mfma_f32_16x16x32_bf16 v[124:127], v[204:207], a[188:191], v[124:127]\n"
"v_sub_f32 v134, -v178, v187\n"
"v_sub_f32 v178, v178, v186\n"
"v_mfma_f32_16x16x32_bf16 v[120:123], v[204:207], a[184:187], v[120:123]\n"
"buffer_load_dwordx4 a[68:71], v212, s[36:39], s20 offen offset:1024 sc1\n"
"v_mfma_f32_16x16x32_bf16 v[120:123], v[200:203], a[168:171], v[120:123]\n"
"v_exp_f32 v134, v134\n"
"v_mfma_f32_16x16x32_bf16 v[116:119], v[200:203], a[164:167], v[116:119]\n"
"v_exp_f32 v178, v178\n"
"v_mfma_f32_16x16x32_bf16 v[116:119], v[204:207], a[180:183], v[116:119]\n"
"v_sub_f32 v135, -v179, v187\n"
"v_sub_f32 v179, v179, v186\n"
"v_mfma_f32_16x16x32_bf16 v[112:115], v[204:207], a[176:179], v[112:115]\n"
"ds_read_b64_tr_b16 v[204:205], v209 offset:19456\n"
"ds_read_b64_tr_b16 v[206:207], v209 offset:19968\n"
"v_mfma_f32_16x16x32_bf16 v[112:115], v[200:203], a[160:163], v[112:115]\n"
"s_waitcnt lgkmcnt(2)\n"
"ds_read_b64_tr_b16 v[200:201], v209 offset:27648\n"
"ds_read_b64_tr_b16 v[202:203], v209 offset:28160\n"
"v_mfma_f32_16x16x32_bf16 v[32:35], v[196:199], a[96:99], v[32:35]\n"
"buffer_load_dwordx4 a[72:75], v212, s[36:39], s20 offen offset:2048 sc1\n"
"v_mfma_f32_16x16x32_bf16 v[32:35], v[192:195], a[112:115], v[32:35]\n"
"v_exp_f32 v135, v135\n"
"v_mfma_f32_16x16x32_bf16 v[36:39], v[192:195], a[116:119], v[36:39]\n"
"v_exp_f32 v179, v179\n"
"v_mfma_f32_16x16x32_bf16 v[36:39], v[196:199], a[100:103], v[36:39]\n"
"v_sub_f32 v136, -v180, v187\n"
"v_sub_f32 v180, v180, v186\n"
"v_mfma_f32_16x16x32_bf16 v[40:43], v[196:199], a[104:107], v[40:43]\n"
"buffer_load_dwordx4 a[76:79], v212, s[36:39], s20 offen offset:3072 sc1\n"
"v_mfma_f32_16x16x32_bf16 v[40:43], v[192:195], a[120:123], v[40:43]\n"
"s_waitcnt vmcnt(18)\n"
"ds_write_b32 v214, v219 offset:544\n"
"v_mfma_f32_16x16x32_bf16 v[44:47], v[192:195], a[124:127], v[44:47]\n"
"s_add_u32 s20, s20, 4096\n"
"ds_read_b64_tr_b16 v[192:193], v208 offset:18432\n"
"ds_read_b64_tr_b16 v[194:195], v208 offset:18944\n"
"v_mfma_f32_16x16x32_bf16 v[44:47], v[196:199], a[108:111], v[44:47]\n"
"s_waitcnt lgkmcnt(3)\n"
"ds_read_b64_tr_b16 v[196:197], v208 offset:26624\n"
"ds_read_b64_tr_b16 v[198:199], v208 offset:27136\n"
"v_mfma_f32_16x16x32_bf16 v[60:63], v[204:207], a[108:111], v[60:63]\n"
"v_exp_f32 v136, v136\n"
"v_mfma_f32_16x16x32_bf16 v[60:63], v[200:203], a[124:127], v[60:63]\n"
"buffer_load_dwordx4 a[80:83], v212, s[36:39], s20 offen offset:0 sc1\n"
"v_mfma_f32_16x16x32_bf16 v[56:59], v[200:203], a[120:123], v[56:59]\n"
"v_exp_f32 v180, v180\n"
"v_mfma_f32_16x16x32_bf16 v[56:59], v[204:207], a[104:107], v[56:59]\n"
"v_sub_f32 v137, -v181, v187\n"
"v_sub_f32 v181, v181, v186\n"
"v_mfma_f32_16x16x32_bf16 v[52:55], v[204:207], a[100:103], v[52:55]\n"
"v_exp_f32 v137, v137\n"
"v_mfma_f32_16x16x32_bf16 v[52:55], v[200:203], a[116:119], v[52:55]\n"
"ds_read_b32 v221, v220 offset:544\n"
"v_mfma_f32_16x16x32_bf16 v[48:51], v[200:203], a[112:115], v[48:51]\n"
"ds_read_b64_tr_b16 v[200:201], v208 offset:19456\n"
"ds_read_b64_tr_b16 v[202:203], v208 offset:19968\n"
"v_mfma_f32_16x16x32_bf16 v[48:51], v[204:207], a[96:99], v[48:51]\n"
"s_waitcnt lgkmcnt(3)\n"
"ds_read_b64_tr_b16 v[204:205], v208 offset:27648\n"
"ds_read_b64_tr_b16 v[206:207], v208 offset:28160\n"
"v_mfma_f32_16x16x32_bf16 v[0:3], v[192:195], a[96:99], v[0:3]\n"
"Lpad6_%=:\n"
"s_cbranch_execz Lpad10_%=\n"
"buffer_load_dwordx4 a[84:87], v212, s[36:39], s20 offen offset:1024 sc1\n"
"v_mfma_f32_16x16x32_bf16 v[0:3], v[196:199], a[112:115], v[0:3]\n"
"v_exp_f32 v181, v181\n"
"v_mfma_f32_16x16x32_bf16 v[4:7], v[196:199], a[116:119], v[4:7]\n"
"s_waitcnt lgkmcnt(4)\n"
"v_cmp_ne_u32 vcc, s75, v221\n"
"v_sub_f32 v138, -v182, v187\n"
"v_mfma_f32_16x16x32_bf16 v[4:7], v[192:195], a[100:103], v[4:7]\n"
"v_sub_f32 v182, v182, v186\n"
"v_mfma_f32_16x16x32_bf16 v[8:11], v[192:195], a[104:107], v[8:11]\n"
"s_cbranch_vccnz Lsb17_%=\n"
"Lsr17_%=:\n"
"s_add_u32 m0, s70, 2048\n"
"v_mfma_f32_16x16x32_bf16 v[8:11], v[196:199], a[120:123], v[8:11]\n"
"s_add_u32 s71, s72, 4718592\n"
"v_exp_f32 v138, v138\n"
"v_mfma_f32_16x16x32_bf16 v[12:15], v[196:199], a[124:127], v[12:15]\n"
"ds_read_b64_tr_b16 v[196:197], v210 offset:18432\n"
"ds_read_b64_tr_b16 v[198:199], v210 offset:18944\n"
"v_mfma_f32_16x16x32_bf16 v[12:15], v[192:195], a[108:111], v[12:15]\n"
"s_waitcnt lgkmcnt(2)\n"
"ds_read_b64_tr_b16 v[192:193], v210 offset:26624\n"
"ds_read_b64_tr_b16 v[194:195], v210 offset:27136\n"
"v_mfma_f32_16x16x32_bf16 v[28:31], v[200:203], a[108:111], v[28:31]\n"
"buffer_load_dwordx4 v213, s[40:43], s71 offen offset:0 lds\n"
"v_mfma_f32_16x16x32_bf16 v[28:31], v[204:207], a[124:127], v[28:31]\n"
"v_exp_f32 v182, v182\n"
"v_mfma_f32_16x16x32_bf16 v[24:27], v[204:207], a[120:123], v[24:27]\n"
"v_sub_f32 v139, -v183, v187\n"
"v_sub_f32 v183, v183, v186\n"
"v_mfma_f32_16x16x32_bf16 v[24:27], v[200:203], a[104:107], v[24:27]\n"
"v_exp_f32 v139, v139\n"
"v_mfma_f32_16x16x32_bf16 v[20:23], v[200:203], a[100:103], v[20:23]\n"
"buffer_load_dwordx4 v213, s[44:47], s71 offen offset:1024 lds\n"
"v_mfma_f32_16x16x32_bf16 v[20:23], v[204:207], a[116:119], v[20:23]\n"
"s_add_u32 m0, s70, 10240\n"
"s_add_u32 s71, s72, 5013504\n"
"v_mfma_f32_16x16x32_bf16 v[16:19], v[204:207], a[112:115], v[16:19]\n"
"ds_read_b64_tr_b16 v[204:205], v210 offset:19456\n"
"ds_read_b64_tr_b16 v[206:207], v210 offset:19968\n"
"v_mfma_f32_16x16x32_bf16 v[16:19], v[200:203], a[96:99], v[16:19]\n"
"s_waitcnt lgkmcnt(2)\n"
"ds_read_b64_tr_b16 v[200:201], v210 offset:27648\n"
"ds_read_b64_tr_b16 v[202:203], v210 offset:28160\n"
"v_mfma_f32_16x16x32_bf16 v[64:67], v[196:199], a[192:195], v[64:67]\n"
"v_exp_f32 v183, v183\n"
"v_mfma_f32_16x16x32_bf16 v[64:67], v[192:195], a[208:211], v[64:67]\n"
"buffer_load_dwordx4 v213, s[40:43], s71 offen offset:0 lds\n"
"v_mfma_f32_16x16x32_bf16 v[68:71], v[192:195], a[212:215], v[68:71]\n"
"v_min_u32 v130, 160, v212\n"
"s_mul_i32 s96, s61, 160\n"
"v_mfma_f32_16x16x32_bf16 v[68:71], v[196:199], a[196:199], v[68:71]\n"
"v_add_u32 v130, s96, v130\n"
"v_lshlrev_b32 v130, 3, v130\n"
"v_mfma_f32_16x16x32_bf16 v[72:75], v[196:199], a[200:203], v[72:75]\n"
"buffer_load_dword v131, v130, s[36:39], s99 offen offset:0 sc1\n"
"v_mfma_f32_16x16x32_bf16 v[72:75], v[192:195], a[216:219], v[72:75]\n"
"v_sub_f32 v140, -v184, v187\n"
"v_sub_f32 v184, v184, v186\n"
"v_mfma_f32_16x16x32_bf16 v[76:79], v[192:195], a[220:223], v[76:79]\n"
"ds_read_b64_tr_b16 v[192:193], v211 offset:18432\n"
"ds_read_b64_tr_b16 v[194:195], v211 offset:18944\n"
"v_mfma_f32_16x16x32_bf16 v[76:79], v[196:199], a[204:207], v[76:79]\n"
"s_waitcnt lgkmcnt(2)\n"
"ds_read_b64_tr_b16 v[196:197], v211 offset:26624\n"
"ds_read_b64_tr_b16 v[198:199], v211 offset:27136\n"
"v_mfma_f32_16x16x32_bf16 v[92:95], v[204:207], a[204:207], v[92:95]\n"
"v_and_b32 v129, 896, v212\n"
"v_lshlrev_b32 v129, 5, v129\n"
"v_mfma_f32_16x16x32_bf16 v[92:95], v[200:203], a[220:223], v[92:95]\n"
"buffer_load_dword v129, v129, s[36:39], s28 offen offset:128 sc1\n"
"v_mfma_f32_16x16x32_bf16 v[88:91], v[200:203], a[216:219], v[88:91]\n"
"v_exp_f32 v140, v140\n"
"v_mfma_f32_16x16x32_bf16 v[88:91], v[204:207], a[200:203], v[88:91]\n"
"v_exp_f32 v184, v184\n"
"v_mfma_f32_16x16x32_bf16 v[84:87], v[204:207], a[196:199], v[84:87]\n"
"v_add_f32 v186, v176, v177\n"
"v_add_f32 v187, v132, v133\n"
"v_mfma_f32_16x16x32_bf16 v[84:87], v[200:203], a[212:215], v[84:87]\n"
"v_add_f32 v186, v186, v178\n"
"v_add_f32 v187, v187, v134\n"
"v_mfma_f32_16x16x32_bf16 v[80:83], v[200:203], a[208:211], v[80:83]\n"
"ds_read_b64_tr_b16 v[200:201], v211 offset:19456\n"
"ds_read_b64_tr_b16 v[202:203], v211 offset:19968\n"
"v_mfma_f32_16x16x32_bf16 v[80:83], v[204:207], a[192:195], v[80:83]\n"
"s_waitcnt lgkmcnt(2)\n"
"ds_read_b64_tr_b16 v[204:205], v211 offset:27648\n"
"ds_read_b64_tr_b16 v[206:207], v211 offset:28160\n"
"v_mfma_f32_16x16x32_bf16 v[96:99], v[192:195], a[192:195], v[96:99]\n"
"buffer_load_dwordx4 v213, s[44:47], s71 offen offset:1024 lds\n"
"v_mfma_f32_16x16x32_bf16 v[96:99], v[196:199], a[208:211], v[96:99]\n"
"v_add_f32 v186, v186, v179\n"
"v_add_f32 v187, v187, v135\n"
"v_mfma_f32_16x16x32_bf16 v[100:103], v[196:199], a[212:215], v[100:103]\n"
"v_add_f32 v186, v186, v180\n"
"v_add_f32 v187, v187, v136\n"
"v_mfma_f32_16x16x32_bf16 v[100:103], v[192:195], a[196:199], v[100:103]\n"
"v_add_f32 v186, v186, v181\n"
"v_add_f32 v187, v187, v137\n"
"v_mfma_f32_16x16x32_bf16 v[104:107], v[192:195], a[200:203], v[104:107]\n"
"v_add_f32 v186, v186, v182\n"
"v_add_f32 v187, v187, v138\n"
"v_mfma_f32_16x16x32_bf16 v[104:107], v[196:199], a[216:219], v[104:107]\n"
"buffer_load_dwordx4 a[88:91], v212, s[36:39], s20 offen offset:2048 sc1\n"
"v_mfma_f32_16x16x32_bf16 v[108:111], v[196:199], a[220:223], v[108:111]\n"
"ds_read_b64_tr_b16 v[196:197], v209 offset:34816\n"
"ds_read_b64_tr_b16 v[198:199], v209 offset:35328\n"
"v_mfma_f32_16x16x32_bf16 v[108:111], v[192:195], a[204:207], v[108:111]\n"
"s_waitcnt lgkmcnt(2)\n"
"ds_read_b64_tr_b16 v[192:193], v209 offset:43008\n"
"ds_read_b64_tr_b16 v[194:195], v209 offset:43520\n"
"v_mfma_f32_16x16x32_bf16 v[124:127], v[200:203], a[204:207], v[124:127]\n"
"v_add_f32 v186, v186, v183\n"
"v_add_f32 v187, v187, v139\n"
"v_mfma_f32_16x16x32_bf16 v[124:127], v[204:207], a[220:223], v[124:127]\n"
"v_add_f32 v186, v186, v184\n"
"v_add_f32 v187, v187, v140\n"
"v_mfma_f32_16x16x32_bf16 v[120:123], v[204:207], a[216:219], v[120:123]\n"
"buffer_load_dwordx4 a[92:95], v212, s[36:39], s20 offen offset:3072 sc1\n"
"v_mfma_f32_16x16x32_bf16 v[120:123], v[200:203], a[200:203], v[120:123]\n"
"s_add_u32 s20, s20, 4096\n"
"v_permlane16_swap_b32 v186, v187\n"
"v_mfma_f32_16x16x32_bf16 v[116:119], v[200:203], a[196:199], v[116:119]\n"
"v_add_f32 v186, v186, v187\n"
"v_mfma_f32_16x16x32_bf16 v[116:119], v[204:207], a[212:215], v[116:119]\n"
"v_rcp_f32 v186, v186\n"
"v_mfma_f32_16x16x32_bf16 v[112:115], v[204:207], a[208:211], v[112:115]\n"
"ds_read_b64_tr_b16 v[204:205], v209 offset:35840\n"
"ds_read_b64_tr_b16 v[206:207], v209 offset:36352\n"
"v_mfma_f32_16x16x32_bf16 v[112:115], v[200:203], a[192:195], v[112:115]\n"
"s_waitcnt lgkmcnt(2)\n"
"ds_read_b64_tr_b16 v[200:201], v209 offset:44032\n"
"ds_read_b64_tr_b16 v[202:203], v209 offset:44544\n"
"v_mfma_f32_16x16x32_bf16 v[32:35], v[196:199], a[128:131], v[32:35]\n"
"buffer_load_dwordx4 a[96:99], v212, s[36:39], s20 offen offset:0 sc1\n"
"v_mfma_f32_16x16x32_bf16 v[32:35], v[192:195], a[144:147], v[32:35]\n"
"v_mov_b32 v187, v186\n"
"v_mfma_f32_16x16x32_bf16 v[36:39], v[192:195], a[148:151], v[36:39]\n"
"v_mfma_f32_16x16x32_bf16 v[36:39], v[196:199], a[132:135], v[36:39]\n"
"v_permlane16_swap_b32 v186, v187\n"
"v_mfma_f32_16x16x32_bf16 v[40:43], v[196:199], a[136:139], v[40:43]\n"
"buffer_load_dwordx4 a[100:103], v212, s[36:39], s20 offen offset:1024 sc1\n"
"v_mfma_f32_16x16x32_bf16 v[40:43], v[192:195], a[152:155], v[40:43]\n"
"s_waitcnt vmcnt(18)\n"
"ds_write_b32 v214, v219 offset:576\n"
"v_mfma_f32_16x16x32_bf16 v[44:47], v[192:195], a[156:159], v[44:47]\n"
"ds_read_b64_tr_b16 v[192:193], v208 offset:34816\n"
"ds_read_b64_tr_b16 v[194:195], v208 offset:35328\n"
"v_mfma_f32_16x16x32_bf16 v[44:47], v[196:199], a[140:143], v[44:47]\n"
"s_waitcnt lgkmcnt(3)\n"
"ds_read_b64_tr_b16 v[196:197], v208 offset:43008\n"
"ds_read_b64_tr_b16 v[198:199], v208 offset:43520\n"
"v_mfma_f32_16x16x32_bf16 v[60:63], v[204:207], a[140:143], v[60:63]\n"
"v_mul_f32 v176, v176, v186\n"
"v_mul_f32 v132, v132, v187\n"
"v_mfma_f32_16x16x32_bf16 v[60:63], v[200:203], a[156:159], v[60:63]\n"
"buffer_load_dwordx4 a[104:107], v212, s[36:39], s20 offen offset:2048 sc1\n"
"v_mfma_f32_16x16x32_bf16 v[56:59], v[200:203], a[152:155], v[56:59]\n"
"v_mul_f32 v177, v177, v186\n"
"v_mul_f32 v133, v133, v187\n"
"v_mfma_f32_16x16x32_bf16 v[56:59], v[204:207], a[136:139], v[56:59]\n"
"v_mul_f32 v178, v178, v186\n"
"v_mul_f32 v134, v134, v187\n"
"v_mfma_f32_16x16x32_bf16 v[52:55], v[204:207], a[132:135], v[52:55]\n"
"v_mul_f32 v179, v179, v186\n"
"v_mul_f32 v135, v135, v187\n"
"v_mfma_f32_16x16x32_bf16 v[52:55], v[200:203], a[148:151], v[52:55]\n"
"ds_read_b32 v221, v220 offset:576\n"
"v_mul_f32 v180, v180, v186\n"
"v_mfma_f32_16x16x32_bf16 v[48:51], v[200:203], a[144:147], v[48:51]\n"
"ds_read_b64_tr_b16 v[200:201], v208 offset:35840\n"
"ds_read_b64_tr_b16 v[202:203], v208 offset:36352\n"
"v_mfma_f32_16x16x32_bf16 v[48:51], v[204:207], a[128:131], v[48:51]\n"
"s_waitcnt lgkmcnt(3)\n"
"ds_read_b64_tr_b16 v[204:205], v208 offset:44032\n"
"ds_read_b64_tr_b16 v[206:207], v208 offset:44544\n"
"v_mfma_f32_16x16x32_bf16 v[0:3], v[192:195], a[128:131], v[0:3]\n"
"buffer_load_dwordx4 a[108:111], v212, s[36:39], s20 offen offset:3072 sc1\n"
"v_mfma_f32_16x16x32_bf16 v[0:3], v[196:199], a[144:147], v[0:3]\n"
"s_add_u32 s20, s20, 4096\n"
"v_mul_f32 v136, v136, v187\n"
"v_mul_f32 v181, v181, v186\n"
"v_mfma_f32_16x16x32_bf16 v[4:7], v[196:199], a[148:151], v[4:7]\n"
"s_waitcnt lgkmcnt(4)\n"
"v_cmp_ne_u32 vcc, s75, v221\n"
"v_mul_f32 v137, v137, v187\n"
"v_mfma_f32_16x16x32_bf16 v[4:7], v[192:195], a[132:135], v[4:7]\n"
"v_mul_f32 v182, v182, v186\n"
"v_mul_f32 v138, v138, v187\n"
"v_mfma_f32_16x16x32_bf16 v[8:11], v[192:195], a[136:139], v[8:11]\n"
"s_cbranch_vccnz Lsb18_%=\n"
"Lsr18_%=:\n"
"v_mul_f32 v183, v183, v186\n"
"v_mfma_f32_16x16x32_bf16 v[8:11], v[196:199], a[152:155], v[8:11]\n"
"buffer_load_dwordx4 a[112:115], v212, s[36:39], s20 offen offset:0 sc1\n"
"v_mfma_f32_16x16x32_bf16 v[12:15], v[196:199], a[156:159], v[12:15]\n"
"ds_read_b64_tr_b16 v[196:197], v210 offset:34816\n"
"ds_read_b64_tr_b16 v[198:199], v210 offset:35328\n"
"v_mfma_f32_16x16x32_bf16 v[12:15], v[192:195], a[140:143], v[12:15]\n"
"s_waitcnt lgkmcnt(2)\n"
"ds_read_b64_tr_b16 v[192:193], v210 offset:43008\n"
"ds_read_b64_tr_b16 v[194:195], v210 offset:43520\n"
"v_mfma_f32_16x16x32_bf16 v[28:31], v[200:203], a[140:143], v[28:31]\n"
"s_waitcnt vmcnt(9)\n"
"v_cmp_ne_u32 s[92:93], s75, v131\n"
"v_mul_f32 v139, v139, v187\n"
"v_mfma_f32_16x16x32_bf16 v[28:31], v[204:207], a[156:159], v[28:31]\n"
"buffer_load_dwordx4 a[116:119], v212, s[36:39], s20 offen offset:1024 sc1\n"
"v_mfma_f32_16x16x32_bf16 v[24:27], v[204:207], a[152:155], v[24:27]\n"
"s_cmp_lg_u64 s[92:93], 0\n"
"s_cbranch_scc1 Lslow1_%=\n"
"Lback1_%=:\n"
"v_mfma_f32_16x16x32_bf16 v[24:27], v[200:203], a[136:139], v[24:27]\n"
"s_waitcnt vmcnt(9)\n"
"v_cmp_ne_u32 s[34:35], s75, v129\n"
"v_mul_f32 v184, v184, v186\n"
"v_mfma_f32_16x16x32_bf16 v[20:23], v[200:203], a[132:135], v[20:23]\n"
"v_mul_f32 v140, v140, v187\n"
"v_cvt_pk_bf16_f32 v176, v176, v177\n"
"v_mfma_f32_16x16x32_bf16 v[20:23], v[204:207], a[148:151], v[20:23]\n"
"s_cmp_lg_u64 s[34:35], 0\n"
"s_cbranch_scc1 Lslow4_%=\n"
"Lback4_%=:\n"
"v_mfma_f32_16x16x32_bf16 v[16:19], v[204:207], a[144:147], v[16:19]\n"
"ds_read_b64_tr_b16 v[204:205], v210 offset:35840\n"
"ds_read_b64_tr_b16 v[206:207], v210 offset:36352\n"
"v_mfma_f32_16x16x32_bf16 v[16:19], v[200:203], a[128:131], v[16:19]\n"
"s_waitcnt lgkmcnt(2)\n"
"ds_read_b64_tr_b16 v[200:201], v210 offset:44032\n"
"ds_read_b64_tr_b16 v[202:203], v210 offset:44544\n"
"v_mfma_f32_16x16x32_bf16 v[64:67], v[196:199], a[224:227], v[64:67]\n"
"buffer_load_dwordx4 a[120:123], v212, s[36:39], s20 offen offset:2048 sc1\n"
"v_mfma_f32_16x16x32_bf16 v[64:67], v[192:195], a[240:243], v[64:67]\n"
"v_cvt_pk_bf16_f32 v177, v178, v179\n"
"v_cvt_pk_bf16_f32 v178, v180, v181\n"
"v_mfma_f32_16x16x32_bf16 v[68:71], v[192:195], a[244:247], v[68:71]\n"
"v_cvt_pk_bf16_f32 v179, v182, v183\n"
"v_mfma_f32_16x16x32_bf16 v[68:71], v[196:199], a[228:231], v[68:71]\n"
"ds_write_b128 v215, v[176:179] offset:3840\n"
"v_mfma_f32_16x16x32_bf16 v[72:75], v[196:199], a[232:235], v[72:75]\n"
"buffer_load_dwordx4 a[124:127], v212, s[36:39], s20 offen offset:3072 sc1\n"
"v_mfma_f32_16x16x32_bf16 v[72:75], v[192:195], a[248:251], v[72:75]\n"
"s_add_u32 s20, s20, 4096\n"
"ds_write_b32 v214, v219 offset:608\n"
"v_mfma_f32_16x16x32_bf16 v[76:79], v[192:195], a[252:255], v[76:79]\n"
"ds_read_b64_tr_b16 v[192:193], v211 offset:34816\n"
"ds_read_b64_tr_b16 v[194:195], v211 offset:35328\n"
"v_mfma_f32_16x16x32_bf16 v[76:79], v[196:199], a[236:239], v[76:79]\n"
"s_waitcnt lgkmcnt(4)\n"
"ds_read_b64_tr_b16 v[196:197], v211 offset:43008\n"
"ds_read_b64_tr_b16 v[198:199], v211 offset:43520\n"
"v_mfma_f32_16x16x32_bf16 v[92:95], v[204:207], a[236:239], v[92:95]\n"
"v_cvt_pk_bf16_f32 v132, v132, v133\n"
"v_cvt_pk_bf16_f32 v133, v134, v135\n"
"v_mfma_f32_16x16x32_bf16 v[92:95], v[200:203], a[252:255], v[92:95]\n"
"buffer_load_dwordx2 a[128:129], v128, s[36:39], s20 offen offset:0 sc1\n"
"v_mfma_f32_16x16x32_bf16 v[88:91], v[200:203], a[248:251], v[88:91]\n"
"v_cvt_pk_bf16_f32 v134, v136, v137\n"
"v_cvt_pk_bf16_f32 v135, v138, v139\n"
"v_mfma_f32_16x16x32_bf16 v[88:91], v[204:207], a[232:235], v[88:91]\n"
"buffer_load_dwordx2 a[130:131], v128, s[36:39], s20 offen offset:512 sc1\n"
"v_mfma_f32_16x16x32_bf16 v[84:87], v[204:207], a[228:231], v[84:87]\n"
"ds_write_b128 v215, v[132:135] offset:3856\n"
"v_mfma_f32_16x16x32_bf16 v[84:87], v[200:203], a[244:247], v[84:87]\n"
"ds_read_b32 v221, v220 offset:608\n"
"v_cvt_pk_bf16_f32 v184, v184, v140\n"
"v_mfma_f32_16x16x32_bf16 v[80:83], v[200:203], a[240:243], v[80:83]\n"
"ds_read_b64_tr_b16 v[200:201], v211 offset:35840\n"
"ds_read_b64_tr_b16 v[202:203], v211 offset:36352\n"
"v_mfma_f32_16x16x32_bf16 v[80:83], v[204:207], a[224:227], v[80:83]\n"
"s_waitcnt lgkmcnt(4)\n"
"ds_read_b64_tr_b16 v[204:205], v211 offset:44032\n"
"ds_read_b64_tr_b16 v[206:207], v211 offset:44544\n"
"v_mfma_f32_16x16x32_bf16 v[96:99], v[192:195], a[224:227], v[96:99]\n"
"buffer_load_dwordx2 a[132:133], v128, s[36:39], s20 offen offset:1024 sc1\n"
"v_mfma_f32_16x16x32_bf16 v[96:99], v[196:199], a[240:243], v[96:99]\n"
"ds_write_b32 v216, v184 offset:3904\n"
"v_mfma_f32_16x16x32_bf16 v[100:103], v[196:199], a[244:247], v[100:103]\n"
"s_waitcnt lgkmcnt(5)\n"
"v_cmp_ne_u32 vcc, s75, v221\n"
"v_lshrrev_b32 v168, 4, v212\n"
"v_mfma_f32_16x16x32_bf16 v[100:103], v[192:195], a[228:231], v[100:103]\n"
"buffer_load_dwordx2 a[134:135], v128, s[36:39], s20 offen offset:1536 sc1\n"
"v_mfma_f32_16x16x32_bf16 v[104:107], v[192:195], a[232:235], v[104:107]\n"
"s_cbranch_vccnz Lsb19_%=\n"
"Lsr19_%=:\n"
"s_nop 0\n"
"v_mfma_f32_16x16x32_bf16 v[104:107], v[196:199], a[248:251], v[104:107]\n"
"s_add_u32 m0, s70, 100352\n"
"v_add_u32 v209, 0xc000, v209\n"
"s_add_u32 s71, s82, 0\n"
"v_mfma_f32_16x16x32_bf16 v[108:111], v[196:199], a[252:255], v[108:111]\n"
"ds_read_b64_tr_b16 v[196:197], v209 offset:2048\n"
"ds_read_b64_tr_b16 v[198:199], v209 offset:2560\n"
"v_mfma_f32_16x16x32_bf16 v[108:111], v[192:195], a[236:239], v[108:111]\n"
"s_waitcnt lgkmcnt(3)\n"
"ds_read_b64_tr_b16 v[192:193], v209 offset:10240\n"
"ds_read_b64_tr_b16 v[194:195], v209 offset:10752\n"
"v_mfma_f32_16x16x32_bf16 v[124:127], v[200:203], a[236:239], v[124:127]\n"
"buffer_load_dwordx2 a[136:137], v128, s[36:39], s20 offen offset:2048 sc1\n"
"v_mfma_f32_16x16x32_bf16 v[124:127], v[204:207], a[252:255], v[124:127]\n"
"s_waitcnt lgkmcnt(4)\n"
"ds_write_b32 v214, v219 offset:1984\n"
"v_mfma_f32_16x16x32_bf16 v[120:123], v[204:207], a[248:251], v[120:123]\n"
"buffer_load_dwordx2 a[138:139], v128, s[36:39], s20 offen offset:2560 sc1\n"
"v_mfma_f32_16x16x32_bf16 v[120:123], v[200:203], a[232:235], v[120:123]\n"
"v_lshrrev_b32 v169, 2, v168\n"
"v_mul_u32_u24 v215, 80, v169\n"
"v_mfma_f32_16x16x32_bf16 v[116:119], v[200:203], a[228:231], v[116:119]\n"
"buffer_load_dwordx2 a[140:141], v128, s[36:39], s20 offen offset:3072 sc1\n"
"v_mfma_f32_16x16x32_bf16 v[116:119], v[204:207], a[244:247], v[116:119]\n"
"v_mul_u32_u24 v217, 48, v169\n"
"v_add_u32 v217, 129024, v217\n"
"v_mfma_f32_16x16x32_bf16 v[112:115], v[204:207], a[240:243], v[112:115]\n"
"ds_read_b64_tr_b16 v[204:205], v209 offset:3072\n"
"ds_read_b64_tr_b16 v[206:207], v209 offset:3584\n"
"v_mfma_f32_16x16x32_bf16 v[112:115], v[200:203], a[224:227], v[112:115]\n"
"s_waitcnt lgkmcnt(3)\n"
"ds_read_b64_tr_b16 v[200:201], v209 offset:11264\n"
"ds_read_b64_tr_b16 v[202:203], v209 offset:11776\n"
"v_mfma_f32_16x16x32_bf16 v[32:35], v[196:199], a[160:163], v[32:35]\n"
"buffer_load_dwordx2 a[142:143], v128, s[36:39], s20 offen offset:3584 sc1\n"
"v_mfma_f32_16x16x32_bf16 v[32:35], v[192:195], a[176:179], v[32:35]\n"
"s_add_u32 s20, s20, 4096\n"
"ds_read_b32 v171, v220 offset:1984\n"
"v_and_b32 v218, s87, v212\n"
"v_mfma_f32_16x16x32_bf16 v[36:39], v[192:195], a[180:183], v[36:39]\n"
"buffer_load_dwordx2 a[144:145], v128, s[36:39], s20 offen offset:0 sc1\n"
"v_mfma_f32_16x16x32_bf16 v[36:39], v[196:199], a[164:167], v[36:39]\n"
"v_add3_u32 v215, v215, v218, s86\n"
"v_and_b32 v169, 3, v168\n"
"v_mfma_f32_16x16x32_bf16 v[40:43], v[196:199], a[168:171], v[40:43]\n"
"s_waitcnt vmcnt(19)\n"
"ds_write_b32 v214, v219 offset:640\n"
"v_mfma_f32_16x16x32_bf16 v[40:43], v[192:195], a[184:187], v[40:43]\n"
"v_add_u32 v208, 0xc000, v208\n"
"v_lshl_add_u32 v216, v169, 3, v217\n"
"v_mfma_f32_16x16x32_bf16 v[44:47], v[192:195], a[188:191], v[44:47]\n"
"ds_read_b64_tr_b16 v[192:193], v208 offset:2048\n"
"ds_read_b64_tr_b16 v[194:195], v208 offset:2560\n"
"v_mfma_f32_16x16x32_bf16 v[44:47], v[196:199], a[172:175], v[44:47]\n"
"s_waitcnt lgkmcnt(4)\n"
"ds_read_b64_tr_b16 v[196:197], v208 offset:10240\n"
"ds_read_b64_tr_b16 v[198:199], v208 offset:10752\n"
"v_mfma_f32_16x16x32_bf16 v[60:63], v[204:207], a[172:175], v[60:63]\n"
"buffer_load_dwordx2 a[146:147], v128, s[36:39], s20 offen offset:512 sc1\n"
"v_mfma_f32_16x16x32_bf16 v[60:63], v[200:203], a[188:191], v[60:63]\n"
"s_waitcnt lgkmcnt(5)\n"
"v_cmp_ne_u32 s[92:93], s75, v171\n"
"v_and_b32 v168, s85, v168\n"
"v_mfma_f32_16x16x32_bf16 v[56:59], v[200:203], a[184:187], v[56:59]\n"
"buffer_load_dwordx2 a[148:149], v128, s[36:39], s20 offen offset:1024 sc1\n"
"v_mfma_f32_16x16x32_bf16 v[56:59], v[204:207], a[168:171], v[56:59]\n"
"s_cmp_lg_u64 s[92:93], 0\n"
"s_cbranch_scc1 Lsb62_%=\n"
"Lsr62_%=:\n"
"v_mfma_f32_16x16x32_bf16 v[52:55], v[204:207], a[164:167], v[52:55]\n"
"buffer_load_dwordx2 a[150:151], v128, s[36:39], s20 offen offset:1536 sc1\n"
"v_mfma_f32_16x16x32_bf16 v[52:55], v[200:203], a[180:183], v[52:55]\n"
"ds_read_b32 v221, v220 offset:640\n"
"ds_read_b64_tr_b16 v[136:137], v215 offset:0\n"
"v_mfma_f32_16x16x32_bf16 v[48:51], v[200:203], a[176:179], v[48:51]\n"
"ds_read_b64_tr_b16 v[200:201], v208 offset:3072\n"
"ds_read_b64_tr_b16 v[202:203], v208 offset:3584\n"
"v_mfma_f32_16x16x32_bf16 v[48:51], v[204:207], a[160:163], v[48:51]\n"
"s_waitcnt lgkmcnt(4)\n"
"ds_read_b64_tr_b16 v[204:205], v208 offset:11264\n"
"ds_read_b64_tr_b16 v[206:207], v208 offset:11776\n"
"v_mfma_f32_16x16x32_bf16 v[0:3], v[192:195], a[160:163], v[0:3]\n"
"buffer_load_dwordx2 a[152:153], v128, s[36:39], s20 offen offset:2048 sc1\n"
"v_mfma_f32_16x16x32_bf16 v[0:3], v[196:199], a[176:179], v[0:3]\n"
"ds_read_b64_tr_b16 v[144:145], v216 offset:0\n"
"ds_read_b64_tr_b16 v[152:153], v217 offset:32\n"
"v_mfma_f32_16x16x32_bf16 v[4:7], v[196:199], a[180:183], v[4:7]\n"
"s_waitcnt lgkmcnt(7)\n"
"ds_read_b64_tr_b16 v[138:139], v215 offset:1280\n"
"v_cmp_ne_u32 vcc, s75, v221\n"
"v_mfma_f32_16x16x32_bf16 v[4:7], v[192:195], a[164:167], v[4:7]\n"
"buffer_load_dwordx2 a[154:155], v128, s[36:39], s20 offen offset:2560 sc1\n"
"v_mfma_f32_16x16x32_bf16 v[8:11], v[192:195], a[168:171], v[8:11]\n"
"s_cbranch_vccnz Lsb20_%=\n"
"Lsr20_%=:\n"
"ds_read_b64_tr_b16 v[146:147], v216 offset:768\n"
"v_mfma_f32_16x16x32_bf16 v[8:11], v[196:199], a[184:187], v[8:11]\n"
"ds_read_b64_tr_b16 v[154:155], v217 offset:800\n"
"v_add_u32 v210, 0xffff4000, v210\n"
"v_mfma_f32_16x16x32_bf16 v[12:15], v[196:199], a[188:191], v[12:15]\n"
"ds_read_b64_tr_b16 v[196:197], v210 offset:2048\n"
"ds_read_b64_tr_b16 v[198:199], v210 offset:2560\n"
"v_mfma_f32_16x16x32_bf16 v[12:15], v[192:195], a[172:175], v[12:15]\n"
"s_waitcnt lgkmcnt(7)\n"
"ds_read_b64_tr_b16 v[192:193], v210 offset:10240\n"
"ds_read_b64_tr_b16 v[194:195], v210 offset:10752\n"
"v_mfma_f32_16x16x32_bf16 v[28:31], v[200:203], a[172:175], v[28:31]\n"
"buffer_load_dwordx2 a[156:157], v128, s[36:39], s20 offen offset:3072 sc1\n"
"v_mfma_f32_16x16x32_bf16 v[28:31], v[204:207], a[188:191], v[28:31]\n"
"ds_read_b64_tr_b16 v[140:141], v215 offset:2560\n"
"ds_read_b64_tr_b16 v[148:149], v216 offset:1536\n"
"v_mfma_f32_16x16x32_bf16 v[24:27], v[204:207], a[184:187], v[24:27]\n"
"buffer_load_dwordx2 a[158:159], v128, s[36:39], s20 offen offset:3584 sc1\n"
"v_mfma_f32_16x16x32_bf16 v[24:27], v[200:203], a[168:171], v[24:27]\n"
"s_add_u32 s20, s20, 4096\n"
"ds_read_b64_tr_b16 v[156:157], v217 offset:1568\n"
"ds_read_b64_tr_b16 v[142:143], v215 offset:3840\n"
"v_mfma_f32_16x16x32_bf16 v[20:23], v[200:203], a[164:167], v[20:23]\n"
"buffer_load_dwordx4 v212, s[36:39], s71 offen offset:0 sc1 lds\n"
"v_mfma_f32_16x16x32_bf16 v[20:23], v[204:207], a[180:183], v[20:23]\n"
"ds_write_b32 v214, v219 offset:672\n"
"v_mfma_f32_16x16x32_bf16 v[16:19], v[204:207], a[176:179], v[16:19]\n"
"s_waitcnt lgkmcnt(7)\n"
"ds_read_b64_tr_b16 v[204:205], v210 offset:3072\n"
"ds_read_b64_tr_b16 v[206:207], v210 offset:3584\n"
"v_mfma_f32_16x16x32_bf16 v[16:19], v[200:203], a[160:163], v[16:19]\n"
"ds_read_b64_tr_b16 v[200:201], v210 offset:11264\n"
"ds_read_b64_tr_b16 v[202:203], v210 offset:11776\n"
"v_mfma_f32_16x16x32_bf16 v[64:67], v[196:199], v[224:227], v[64:67]\n"
"s_waitcnt lgkmcnt(9)\n"
"buffer_load_dwordx4 v212, s[36:39], s71 offen offset:1024 sc1 lds\n"
"v_mfma_f32_16x16x32_bf16 v[64:67], v[192:195], v[240:243], v[64:67]\n"
"s_add_u32 m0, s70, 18432\n"
"ds_read_b64_tr_b16 v[150:151], v216 offset:2304\n"
"s_add_u32 s71, s82, 8192\n"
"v_mfma_f32_16x16x32_bf16 v[68:71], v[192:195], v[244:247], v[68:71]\n"
"ds_read_b64_tr_b16 v[158:159], v217 offset:2336\n"
"v_lshl_add_u32 v170, v168, 4, s88\n"
"v_mfma_f32_16x16x32_bf16 v[68:71], v[196:199], v[228:231], v[68:71]\n"
"ds_read_b32 v221, v220 offset:672\n"
"v_and_b32 v169, s5, v168\n"
"v_mfma_f32_16x16x32_bf16 v[72:75], v[196:199], v[232:235], v[72:75]\n"
"v_mfma_f32_16x16x32_bf16 v[160:163], v[136:139], v[144:147], 0\n"
"ds_read_b64_tr_b16 v[172:173], v215 offset:5120\n"
"ds_read_b64_tr_b16 v[176:177], v216 offset:3072\n"
"v_mfma_f32_16x16x32_bf16 v[164:167], v[136:139], v[152:155], 0\n"
"s_waitcnt lgkmcnt(7)\n"
"ds_read_b64_tr_b16 v[180:181], v217 offset:3104\n"
"ds_read_b64_tr_b16 v[174:175], v215 offset:6400\n"
"v_mfma_f32_16x16x32_bf16 v[72:75], v[192:195], v[248:251], v[72:75]\n"
"ds_read_b64_tr_b16 v[178:179], v216 offset:3840\n"
"v_add_u32 v211, 0xffff4000, v211\n"
"v_mfma_f32_16x16x32_bf16 v[76:79], v[192:195], v[252:255], v[76:79]\n"
"ds_read_b64_tr_b16 v[192:193], v211 offset:2048\n"
"ds_read_b64_tr_b16 v[194:195], v211 offset:2560\n"
"v_mfma_f32_16x16x32_bf16 v[76:79], v[196:199], v[236:239], v[76:79]\n"
"ds_read_b64_tr_b16 v[196:197], v211 offset:10240\n"
"ds_read_b64_tr_b16 v[198:199], v211 offset:10752\n"
"v_mfma_f32_16x16x32_bf16 v[92:95], v[204:207], v[236:239], v[92:95]\n"
"s_waitcnt lgkmcnt(9)\n"
"ds_read_b64_tr_b16 v[182:183], v217 offset:3872\n"
"v_cmp_ne_u32 vcc, s75, v221\n"
"v_mfma_f32_16x16x32_bf16 v[160:163], v[140:143], v[148:151], v[160:163]\n"
"ds_read_b64_tr_b16 v[136:137], v215 offset:7680\n"
"ds_read_b64_tr_b16 v[144:145], v216 offset:4608\n"
"v_mfma_f32_16x16x32_bf16 v[164:167], v[140:143], v[156:159], v[164:167]\n"
"ds_read_b64_tr_b16 v[152:153], v217 offset:4640\n"
"ds_read_b64_tr_b16 v[138:139], v215 offset:8960\n"
"v_mfma_f32_16x16x32_bf16 v[92:95], v[200:203], v[252:255], v[92:95]\n"
"buffer_load_dwordx4 v212, s[36:39], s71 offen offset:0 sc1 lds\n"
"v_mfma_f32_16x16x32_bf16 v[88:91], v[200:203], v[248:251], v[88:91]\n"
"s_cbranch_vccnz Lsb21_%=\n"
"Lsr21_%=:\n"
"ds_read_b64_tr_b16 v[146:147], v216 offset:5376\n"
"v_mfma_f32_16x16x32_bf16 v[88:91], v[204:207], v[232:235], v[88:91]\n"
"s_waitcnt lgkmcnt(10)\n"
"ds_read_b64_tr_b16 v[154:155], v217 offset:5408\n"
"v_mfma_f32_16x16x32_bf16 v[160:163], v[172:175], v[176:179], v[160:163]\n"
"s_waitcnt lgkmcnt(6)\n"
"ds_read_b64_tr_b16 v[140:141], v215 offset:10240\n"
"ds_read_b64_tr_b16 v[148:149], v216 offset:6144\n"
"v_mfma_f32_16x16x32_bf16 v[164:167], v[172:175], v[180:183], v[164:167]\n"
"ds_read_b64_tr_b16 v[156:157], v217 offset:6176\n"
"ds_read_b64_tr_b16 v[142:143], v215 offset:11520\n"
"v_mfma_f32_16x16x32_bf16 v[84:87], v[204:207], v[228:231], v[84:87]\n"
"s_waitcnt lgkmcnt(5)\n"
"ds_read_b64_tr_b16 v[150:151], v216 offset:6912\n"
"ds_read_b64_tr_b16 v[158:159], v217 offset:6944\n"
"v_mfma_f32_16x16x32_bf16 v[160:163], v[136:139], v[144:147], v[160:163]\n"
"s_waitcnt lgkmcnt(6)\n"
"ds_read_b64_tr_b16 v[172:173], v215 offset:12800\n"
"ds_read_b64_tr_b16 v[176:177], v216 offset:7680\n"
"v_mfma_f32_16x16x32_bf16 v[164:167], v[136:139], v[152:155], v[164:167]\n"
"ds_read_b64_tr_b16 v[180:181], v217 offset:7712\n"
"ds_read_b64_tr_b16 v[174:175], v215 offset:14080\n"
"v_mfma_f32_16x16x32_bf16 v[84:87], v[200:203], v[244:247], v[84:87]\n"
"buffer_load_dwordx4 v212, s[36:39], s71 offen offset:1024 sc1 lds\n"
"v_mfma_f32_16x16x32_bf16 v[80:83], v[200:203], v[240:243], v[80:83]\n"
"s_add_u32 m0, s70, 26624\n"
"ds_read_b64_tr_b16 v[200:201], v211 offset:3072\n"
"ds_read_b64_tr_b16 v[202:203], v211 offset:3584\n"
"v_mfma_f32_16x16x32_bf16 v[80:83], v[204:207], v[224:227], v[80:83]\n"
"s_add_u32 s71, s82, 16384\n"
"ds_read_b64_tr_b16 v[204:205], v211 offset:11264\n"
"ds_read_b64_tr_b16 v[206:207], v211 offset:11776\n"
"v_mfma_f32_16x16x32_bf16 v[96:99], v[192:195], v[224:227], v[96:99]\n"
"s_waitcnt lgkmcnt(9)\n"
"ds_read_b64_tr_b16 v[178:179], v216 offset:8448\n"
"ds_read_b64_tr_b16 v[182:183], v217 offset:8480\n"
"v_mfma_f32_16x16x32_bf16 v[160:163], v[140:143], v[148:151], v[160:163]\n"
"s_waitcnt lgkmcnt(10)\n"
"ds_read_b64_tr_b16 v[136:137], v215 offset:15360\n"
"ds_read_b64_tr_b16 v[144:145], v216 offset:9216\n"
"v_mfma_f32_16x16x32_bf16 v[164:167], v[140:143], v[156:159], v[164:167]\n"
"ds_read_b64_tr_b16 v[152:153], v217 offset:9248\n"
"ds_read_b64_tr_b16 v[138:139], v215 offset:16640\n"
"v_mfma_f32_16x16x32_bf16 v[96:99], v[196:199], v[240:243], v[96:99]\n"
"buffer_load_dwordx4 v212, s[36:39], s71 offen offset:0 sc1 lds\n"
"v_mfma_f32_16x16x32_bf16 v[100:103], v[196:199], v[244:247], v[100:103]\n"
"s_waitcnt lgkmcnt(5)\n"
"ds_read_b64_tr_b16 v[146:147], v216 offset:9984\n"
"ds_read_b64_tr_b16 v[154:155], v217 offset:10016\n"
"v_mfma_f32_16x16x32_bf16 v[160:163], v[172:175], v[176:179], v[160:163]\n"
"s_waitcnt lgkmcnt(6)\n"
"ds_read_b64_tr_b16 v[140:141], v215 offset:17920\n"
"ds_read_b64_tr_b16 v[148:149], v216 offset:10752\n"
"v_mfma_f32_16x16x32_bf16 v[164:167], v[172:175], v[180:183], v[164:167]\n"
"ds_read_b64_tr_b16 v[156:157], v217 offset:10784\n"
"ds_read_b64_tr_b16 v[142:143], v215 offset:19200\n"
"v_mfma_f32_16x16x32_bf16 v[100:103], v[192:195], v[228:231], v[100:103]\n"
"ds_read_b64_tr_b16 v[150:151], v216 offset:11520\n"
"ds_read_b64_tr_b16 v[158:159], v217 offset:11552\n"
"v_mfma_f32_16x16x32_bf16 v[104:107], v[192:195], v[232:235], v[104:107]\n"
"v_lshrrev_b32 v168, 4, v168\n"
"v_lshl_add_u32 v168, v168, 2, v169\n"
"v_mfma_f32_16x16x32_bf16 v[104:107], v[196:199], v[248:251], v[104:107]\n"
"v_lshl_add_u32 v218, v168, 4, s89\n"
"v_mfma_f32_16x16x32_bf16 v[108:111], v[196:199], v[252:255], v[108:111]\n"
"s_waitcnt lgkmcnt(7)\n"
"ds_read_b64_tr_b16 v[196:197], v209 offset:18432\n"
"ds_read_b64_tr_b16 v[198:199], v209 offset:18944\n"
"v_mfma_f32_16x16x32_bf16 v[160:163], v[136:139], v[144:147], v[160:163]\n"
"s_waitcnt lgkmcnt(8)\n"
"v_mfma_f32_16x16x32_bf16 v[164:167], v[136:139], v[152:155], v[164:167]\n"
"v_mfma_f32_16x16x32_bf16 v[108:111], v[192:195], v[236:239], v[108:111]\n"
"ds_read_b64_tr_b16 v[192:193], v209 offset:26624\n"
"ds_read_b64_tr_b16 v[194:195], v209 offset:27136\n"
"v_mfma_f32_16x16x32_bf16 v[124:127], v[200:203], v[236:239], v[124:127]\n"
"buffer_load_dwordx4 v212, s[36:39], s71 offen offset:1024 sc1 lds\n"
"v_mfma_f32_16x16x32_bf16 v[124:127], v[204:207], v[252:255], v[124:127]\n"
"s_waitcnt lgkmcnt(4)\n"
"s_add_u32 m0, s70, 34816\n"
"s_add_u32 s71, s82, 24576\n"
"v_mfma_f32_16x16x32_bf16 v[160:163], v[140:143], v[148:151], v[160:163]\n"
"v_mfma_f32_16x16x32_bf16 v[164:167], v[140:143], v[156:159], v[164:167]\n"
"v_mfma_f32_16x16x32_bf16 v[120:123], v[204:207], v[248:251], v[120:123]\n"
"v_mfma_f32_16x16x32_bf16 v[120:123], v[200:203], v[232:235], v[120:123]\n"
"v_mfma_f32_16x16x32_bf16 v[116:119], v[200:203], v[228:231], v[116:119]\n"
"buffer_load_dwordx4 v212, s[36:39], s71 offen offset:0 sc1 lds\n"
"v_mfma_f32_16x16x32_bf16 v[116:119], v[204:207], v[244:247], v[116:119]\n"
"s_nop 1\n"
"buffer_store_dwordx4 v[160:163], v170, s[36:39], s21 offen offset:0 sc1\n"
"v_mfma_f32_16x16x32_bf16 v[112:115], v[204:207], v[240:243], v[112:115]\n"
"ds_read_b64_tr_b16 v[204:205], v209 offset:19456\n"
"ds_read_b64_tr_b16 v[206:207], v209 offset:19968\n"
"v_mfma_f32_16x16x32_bf16 v[112:115], v[200:203], v[224:227], v[112:115]\n"
"s_waitcnt lgkmcnt(2)\n"
"ds_read_b64_tr_b16 v[200:201], v209 offset:27648\n"
"ds_read_b64_tr_b16 v[202:203], v209 offset:28160\n"
"v_mfma_f32_16x16x32_bf16 v[32:35], v[196:199], a[192:195], v[32:35]\n"
"v_mfma_f32_16x16x32_bf16 v[32:35], v[192:195], a[208:211], v[32:35]\n"
"v_mfma_f32_16x16x32_bf16 v[36:39], v[192:195], a[212:215], v[36:39]\n"
"buffer_store_dwordx4 v[164:167], v218, s[36:39], s21 offen offset:0 sc1\n"
"v_mfma_f32_16x16x32_bf16 v[36:39], v[196:199], a[196:199], v[36:39]\n"
"v_max_f32 v64, 0, v64\n"
"v_max_f32 v65, 0, v65\n"
"v_mfma_f32_16x16x32_bf16 v[40:43], v[196:199], a[200:203], v[40:43]\n"
"v_max_f32 v66, 0, v66\n"
"v_max_f32 v67, 0, v67\n"
"v_mfma_f32_16x16x32_bf16 v[40:43], v[192:195], a[216:219], v[40:43]\n"
"v_mul_f32 v80, v64, v80\n"
"v_mul_f32 v81, v65, v81\n"
"v_mfma_f32_16x16x32_bf16 v[44:47], v[192:195], a[220:223], v[44:47]\n"
"ds_read_b64_tr_b16 v[192:193], v208 offset:18432\n"
"ds_read_b64_tr_b16 v[194:195], v208 offset:18944\n"
"v_mfma_f32_16x16x32_bf16 v[44:47], v[196:199], a[204:207], v[44:47]\n"
"s_waitcnt lgkmcnt(2)\n"
"ds_read_b64_tr_b16 v[196:197], v208 offset:26624\n"
"ds_read_b64_tr_b16 v[198:199], v208 offset:27136\n"
"v_mfma_f32_16x16x32_bf16 v[60:63], v[204:207], a[204:207], v[60:63]\n"
"v_mul_f32 v82, v66, v82\n"
"v_mul_f32 v83, v67, v83\n"
"v_mfma_f32_16x16x32_bf16 v[60:63], v[200:203], a[220:223], v[60:63]\n"
"buffer_load_dwordx4 v212, s[36:39], s71 offen offset:1024 sc1 lds\n"
"v_mfma_f32_16x16x32_bf16 v[56:59], v[200:203], a[216:219], v[56:59]\n"
"s_add_u32 m0, s70, 43008\n"
"s_add_u32 s71, s82, 32768\n"
"v_cvt_pk_bf16_f32 v64, v80, v81\n"
"v_mfma_f32_16x16x32_bf16 v[56:59], v[204:207], a[200:203], v[56:59]\n"
"Lpad7_%=:\n"
"s_cbranch_execz Lpad11_%=\n"
"v_cvt_pk_bf16_f32 v65, v82, v83\n"
"v_max_f32 v96, 0, v96\n"
"v_mfma_f32_16x16x32_bf16 v[52:55], v[204:207], a[196:199], v[52:55]\n"
"v_max_f32 v97, 0, v97\n"
"v_max_f32 v98, 0, v98\n"
"v_mfma_f32_16x16x32_bf16 v[52:55], v[200:203], a[212:215], v[52:55]\n"
"v_max_f32 v99, 0, v99\n"
"v_mul_f32 v112, v96, v112\n"
"v_mfma_f32_16x16x32_bf16 v[48:51], v[200:203], a[208:211], v[48:51]\n"
"ds_read_b64_tr_b16 v[200:201], v208 offset:19456\n"
"ds_read_b64_tr_b16 v[202:203], v208 offset:19968\n"
"v_mfma_f32_16x16x32_bf16 v[48:51], v[204:207], a[192:195], v[48:51]\n"
"s_waitcnt lgkmcnt(2)\n"
"ds_read_b64_tr_b16 v[204:205], v208 offset:27648\n"
"ds_read_b64_tr_b16 v[206:207], v208 offset:28160\n"
"v_mfma_f32_16x16x32_bf16 v[0:3], v[192:195], a[192:195], v[0:3]\n"
"buffer_load_dwordx4 v212, s[36:39], s71 offen offset:0 sc1 lds\n"
"v_mfma_f32_16x16x32_bf16 v[0:3], v[196:199], a[208:211], v[0:3]\n"
"v_mul_f32 v113, v97, v113\n"
"v_mul_f32 v114, v98, v114\n"
"v_mfma_f32_16x16x32_bf16 v[4:7], v[196:199], a[212:215], v[4:7]\n"
"v_mul_f32 v115, v99, v115\n"
"v_cvt_pk_bf16_f32 v66, v112, v113\n"
"v_mfma_f32_16x16x32_bf16 v[4:7], v[192:195], a[196:199], v[4:7]\n"
"v_cvt_pk_bf16_f32 v67, v114, v115\n"
"v_lshrrev_b32 v132, 4, v212\n"
"v_mfma_f32_16x16x32_bf16 v[8:11], v[192:195], a[200:203], v[8:11]\n"
"buffer_load_dwordx4 v212, s[36:39], s71 offen offset:1024 sc1 lds\n"
"v_mfma_f32_16x16x32_bf16 v[8:11], v[196:199], a[216:219], v[8:11]\n"
"s_add_u32 m0, s70, 51200\n"
"s_add_u32 s71, s82, 40960\n"
"v_bfe_u32 v139, v132, 1, 2\n"
"v_mfma_f32_16x16x32_bf16 v[12:15], v[196:199], a[220:223], v[12:15]\n"
"s_and_b32 s66, s61, 1\n"
"ds_read_b64_tr_b16 v[196:197], v209 offset:34816\n"
"ds_read_b64_tr_b16 v[198:199], v209 offset:35328\n"
"v_mfma_f32_16x16x32_bf16 v[12:15], v[192:195], a[204:207], v[12:15]\n"
"s_waitcnt lgkmcnt(2)\n"
"ds_read_b64_tr_b16 v[192:193], v209 offset:43008\n"
"ds_read_b64_tr_b16 v[194:195], v209 offset:43520\n"
"v_mfma_f32_16x16x32_bf16 v[28:31], v[200:203], a[204:207], v[28:31]\n"
"buffer_load_dwordx4 v212, s[36:39], s71 offen offset:0 sc1 lds\n"
"v_mfma_f32_16x16x32_bf16 v[28:31], v[204:207], a[220:223], v[28:31]\n"
"v_max_u32 v139, s66, v139\n"
"s_min_u32 s66, s61, 2\n"
"v_mfma_f32_16x16x32_bf16 v[24:27], v[204:207], a[216:219], v[24:27]\n"
"buffer_store_dwordx4 v[64:67], v212, s[36:39], s10 offen offset:0 sc1\n"
"v_mfma_f32_16x16x32_bf16 v[24:27], v[200:203], a[200:203], v[24:27]\n"
"v_max_f32 v68, 0, v68\n"
"v_max_f32 v69, 0, v69\n"
"v_mfma_f32_16x16x32_bf16 v[20:23], v[200:203], a[196:199], v[20:23]\n"
"v_max_f32 v70, 0, v70\n"
"v_max_f32 v71, 0, v71\n"
"v_mfma_f32_16x16x32_bf16 v[20:23], v[204:207], a[212:215], v[20:23]\n"
"ds_write_b32 v214, v219 offset:704\n"
"v_mfma_f32_16x16x32_bf16 v[16:19], v[204:207], a[208:211], v[16:19]\n"
"ds_read_b64_tr_b16 v[204:205], v209 offset:35840\n"
"ds_read_b64_tr_b16 v[206:207], v209 offset:36352\n"
"v_mfma_f32_16x16x32_bf16 v[16:19], v[200:203], a[192:195], v[16:19]\n"
"s_waitcnt lgkmcnt(3)\n"
"ds_read_b64_tr_b16 v[200:201], v209 offset:44032\n"
"ds_read_b64_tr_b16 v[202:203], v209 offset:44544\n"
"v_mfma_f32_16x16x32_bf16 v[32:35], v[196:199], a[224:227], v[32:35]\n"
"v_mul_f32 v84, v68, v84\n"
"v_mul_f32 v85, v69, v85\n"
"v_mfma_f32_16x16x32_bf16 v[32:35], v[192:195], a[240:243], v[32:35]\n"
"buffer_load_dwordx4 v212, s[36:39], s71 offen offset:1024 sc1 lds\n"
"v_mfma_f32_16x16x32_bf16 v[36:39], v[192:195], a[244:247], v[36:39]\n"
"s_add_u32 m0, s70, 59392\n"
"s_add_u32 s71, s82, 49152\n"
"v_mul_f32 v86, v70, v86\n"
"v_mfma_f32_16x16x32_bf16 v[36:39], v[196:199], a[228:231], v[36:39]\n"
"ds_read_b32 v221, v220 offset:704\n"
"v_mul_f32 v87, v71, v87\n"
"v_mfma_f32_16x16x32_bf16 v[40:43], v[196:199], a[232:235], v[40:43]\n"
"v_cvt_pk_bf16_f32 v68, v84, v85\n"
"v_cvt_pk_bf16_f32 v69, v86, v87\n"
"v_mfma_f32_16x16x32_bf16 v[40:43], v[192:195], a[248:251], v[40:43]\n"
"v_max_f32 v100, 0, v100\n"
"v_max_f32 v101, 0, v101\n"
"v_mfma_f32_16x16x32_bf16 v[44:47], v[192:195], a[252:255], v[44:47]\n"
"ds_read_b64_tr_b16 v[192:193], v208 offset:34816\n"
"ds_read_b64_tr_b16 v[194:195], v208 offset:35328\n"
"v_mfma_f32_16x16x32_bf16 v[44:47], v[196:199], a[236:239], v[44:47]\n"
"s_waitcnt lgkmcnt(3)\n"
"ds_read_b64_tr_b16 v[196:197], v208 offset:43008\n"
"ds_read_b64_tr_b16 v[198:199], v208 offset:43520\n"
"v_mfma_f32_16x16x32_bf16 v[60:63], v[204:207], a[236:239], v[60:63]\n"
"s_waitcnt lgkmcnt(4)\n"
"v_cmp_ne_u32 vcc, s75, v221\n"
"v_max_f32 v102, 0, v102\n"
"v_mfma_f32_16x16x32_bf16 v[60:63], v[200:203], a[252:255], v[60:63]\n"
"buffer_load_dwordx4 v212, s[36:39], s71 offen offset:0 sc1 lds\n"
"v_mfma_f32_16x16x32_bf16 v[56:59], v[200:203], a[248:251], v[56:59]\n"
"s_cbranch_vccnz Lsb22_%=\n"
"Lsr22_%=:\n"
"s_nop 0\n"
"v_mfma_f32_16x16x32_bf16 v[56:59], v[204:207], a[232:235], v[56:59]\n"
"v_max_f32 v103, 0, v103\n"
"v_mul_f32 v116, v100, v116\n"
"v_mfma_f32_16x16x32_bf16 v[52:55], v[204:207], a[228:231], v[52:55]\n"
"v_mul_f32 v117, v101, v117\n"
"v_mul_f32 v118, v102, v118\n"
"v_mfma_f32_16x16x32_bf16 v[52:55], v[200:203], a[244:247], v[52:55]\n"
"buffer_load_dwordx4 v212, s[36:39], s71 offen offset:1024 sc1 lds\n"
"v_mfma_f32_16x16x32_bf16 v[48:51], v[200:203], a[240:243], v[48:51]\n"
"s_add_u32 m0, s70, 67584\n"
"ds_read_b64_tr_b16 v[200:201], v208 offset:35840\n"
"ds_read_b64_tr_b16 v[202:203], v208 offset:36352\n"
"v_mfma_f32_16x16x32_bf16 v[48:51], v[204:207], a[224:227], v[48:51]\n"
"s_waitcnt lgkmcnt(2)\n"
"ds_read_b64_tr_b16 v[204:205], v208 offset:44032\n"
"ds_read_b64_tr_b16 v[206:207], v208 offset:44544\n"
"v_mfma_f32_16x16x32_bf16 v[0:3], v[192:195], a[224:227], v[0:3]\n"
"s_add_u32 s71, s82, 57344\n"
"v_mul_f32 v119, v103, v119\n"
"v_mfma_f32_16x16x32_bf16 v[0:3], v[196:199], a[240:243], v[0:3]\n"
"buffer_load_dwordx4 v212, s[36:39], s71 offen offset:0 sc1 lds\n"
"v_mfma_f32_16x16x32_bf16 v[4:7], v[196:199], a[244:247], v[4:7]\n"
"v_cvt_pk_bf16_f32 v70, v116, v117\n"
"v_cvt_pk_bf16_f32 v71, v118, v119\n"
"v_mfma_f32_16x16x32_bf16 v[4:7], v[192:195], a[228:231], v[4:7]\n"
"s_waitcnt vmcnt(9)\n"
"buffer_store_dword v219, off, s[36:39], s23 offset:640 sc1\n"
"v_mfma_f32_16x16x32_bf16 v[8:11], v[192:195], a[232:235], v[8:11]\n"
"buffer_store_dwordx4 v[68:71], v212, s[36:39], s10 offen offset:1024 sc1\n"
"v_mfma_f32_16x16x32_bf16 v[8:11], v[196:199], a[248:251], v[8:11]\n"
"v_add_u32 v209, 0xffff4000, v209\n"
"v_max_f32 v72, 0, v72\n"
"v_mfma_f32_16x16x32_bf16 v[12:15], v[196:199], a[252:255], v[12:15]\n"
"ds_read_b64_tr_b16 v[196:197], v209 offset:2048\n"
"ds_read_b64_tr_b16 v[198:199], v209 offset:2560\n"
"v_mfma_f32_16x16x32_bf16 v[12:15], v[192:195], a[236:239], v[12:15]\n"
"s_waitcnt lgkmcnt(2)\n"
"ds_read_b64_tr_b16 v[192:193], v209 offset:10240\n"
"ds_read_b64_tr_b16 v[194:195], v209 offset:10752\n"
"v_mfma_f32_16x16x32_bf16 v[28:31], v[200:203], a[236:239], v[28:31]\n"
"v_max_f32 v73, 0, v73\n"
"v_max_f32 v74, 0, v74\n"
"v_mfma_f32_16x16x32_bf16 v[28:31], v[204:207], a[252:255], v[28:31]\n"
"v_max_f32 v75, 0, v75\n"
"v_mul_f32 v88, v72, v88\n"
"v_mfma_f32_16x16x32_bf16 v[24:27], v[204:207], a[248:251], v[24:27]\n"
"v_mul_f32 v89, v73, v89\n"
"v_mul_f32 v90, v74, v90\n"
"v_mfma_f32_16x16x32_bf16 v[24:27], v[200:203], a[232:235], v[24:27]\n"
"buffer_load_dwordx4 v212, s[36:39], s71 offen offset:1024 sc1 lds\n"
"v_mfma_f32_16x16x32_bf16 v[20:23], v[200:203], a[228:231], v[20:23]\n"
"s_add_u32 m0, s70, 108544\n"
"s_add_u32 s71, s82, 65536\n"
"v_mul_f32 v91, v75, v91\n"
"v_mfma_f32_16x16x32_bf16 v[20:23], v[204:207], a[244:247], v[20:23]\n"
"v_cvt_pk_bf16_f32 v72, v88, v89\n"
"v_cvt_pk_bf16_f32 v73, v90, v91\n"
"v_mfma_f32_16x16x32_bf16 v[16:19], v[204:207], a[240:243], v[16:19]\n"
"ds_read_b64_tr_b16 v[204:205], v209 offset:3072\n"
"ds_read_b64_tr_b16 v[206:207], v209 offset:3584\n"
"v_mfma_f32_16x16x32_bf16 v[16:19], v[200:203], a[224:227], v[16:19]\n"
"s_waitcnt lgkmcnt(2)\n"
"ds_read_b64_tr_b16 v[200:201], v209 offset:11264\n"
"ds_read_b64_tr_b16 v[202:203], v209 offset:11776\n"
"v_mfma_f32_16x16x32_bf16 v[32:35], v[196:199], v[224:227], v[32:35]\n"
"buffer_load_dwordx4 v212, s[36:39], s71 offen offset:0 sc1 lds\n"
"v_mfma_f32_16x16x32_bf16 v[32:35], v[192:195], v[240:243], v[32:35]\n"
"v_max_f32 v104, 0, v104\n"
"v_max_f32 v105, 0, v105\n"
"v_mfma_f32_16x16x32_bf16 v[36:39], v[192:195], v[244:247], v[36:39]\n"
"v_max_f32 v106, 0, v106\n"
"v_max_f32 v107, 0, v107\n"
"v_mfma_f32_16x16x32_bf16 v[36:39], v[196:199], v[228:231], v[36:39]\n"
"v_mul_f32 v120, v104, v120\n"
"v_mul_f32 v121, v105, v121\n"
"v_mfma_f32_16x16x32_bf16 v[40:43], v[196:199], v[232:235], v[40:43]\n"
"ds_write_b32 v214, v219 offset:736\n"
"v_mfma_f32_16x16x32_bf16 v[40:43], v[192:195], v[248:251], v[40:43]\n"
"v_add_u32 v208, 0xffff4000, v208\n"
"v_mul_f32 v122, v106, v122\n"
"v_mfma_f32_16x16x32_bf16 v[44:47], v[192:195], v[252:255], v[44:47]\n"
"ds_read_b64_tr_b16 v[192:193], v208 offset:2048\n"
"ds_read_b64_tr_b16 v[194:195], v208 offset:2560\n"
"v_mfma_f32_16x16x32_bf16 v[44:47], v[196:199], v[236:239], v[44:47]\n"
"s_waitcnt lgkmcnt(3)\n"
"ds_read_b64_tr_b16 v[196:197], v208 offset:10240\n"
"ds_read_b64_tr_b16 v[198:199], v208 offset:10752\n"
"v_mfma_f32_16x16x32_bf16 v[60:63], v[204:207], v[236:239], v[60:63]\n"
"buffer_load_dwordx4 v212, s[36:39], s71 offen offset:1024 sc1 lds\n"
"v_mfma_f32_16x16x32_bf16 v[60:63], v[200:203], v[252:255], v[60:63]\n"
"s_add_u32 m0, s70, 116736\n"
"s_add_u32 s71, s82, 73728\n"
"v_mul_f32 v123, v107, v123\n"
"v_mfma_f32_16x16x32_bf16 v[56:59], v[200:203], v[248:251], v[56:59]\n"
"v_cvt_pk_bf16_f32 v74, v120, v121\n"
"v_cvt_pk_bf16_f32 v75, v122, v123\n"
"v_mfma_f32_16x16x32_bf16 v[56:59], v[204:207], v[232:235], v[56:59]\n"
"ds_read_b32 v221, v220 offset:736\n"
"v_min_u32 v139, s66, v139\n"
"v_mfma_f32_16x16x32_bf16 v[52:55], v[204:207], v[228:231], v[52:55]\n"
"buffer_load_dwordx4 v212, s[36:39], s71 offen offset:0 sc1 lds\n"
"v_mfma_f32_16x16x32_bf16 v[52:55], v[200:203], v[244:247], v[52:55]\n"
"buffer_store_dwordx4 v[72:75], v212, s[36:39], s11 offen offset:0 sc1\n"
"v_mfma_f32_16x16x32_bf16 v[48:51], v[200:203], v[240:243], v[48:51]\n"
"ds_read_b64_tr_b16 v[200:201], v208 offset:3072\n"
"ds_read_b64_tr_b16 v[202:203], v208 offset:3584\n"
"v_mfma_f32_16x16x32_bf16 v[48:51], v[204:207], v[224:227], v[48:51]\n"
"s_waitcnt lgkmcnt(3)\n"
"ds_read_b64_tr_b16 v[204:205], v208 offset:11264\n"
"ds_read_b64_tr_b16 v[206:207], v208 offset:11776\n"
"v_mfma_f32_16x16x32_bf16 v[0:3], v[192:195], v[224:227], v[0:3]\n"
"s_waitcnt lgkmcnt(4)\n"
"v_cmp_ne_u32 vcc, s75, v221\n"
"v_max_f32 v76, 0, v76\n"
"v_mfma_f32_16x16x32_bf16 v[0:3], v[196:199], v[240:243], v[0:3]\n"
"v_max_f32 v77, 0, v77\n"
"v_max_f32 v78, 0, v78\n"
"v_mfma_f32_16x16x32_bf16 v[4:7], v[196:199], v[244:247], v[4:7]\n"
"s_cbranch_vccnz Lsb23_%=\n"
"Lsr23_%=:\n"
"v_max_f32 v79, 0, v79\n"
"v_mfma_f32_16x16x32_bf16 v[4:7], v[192:195], v[228:231], v[4:7]\n"
"v_add_u32 v211, 98304, v212\n"
"v_mul_f32 v92, v76, v92\n"
"v_mfma_f32_16x16x32_bf16 v[8:11], v[192:195], v[232:235], v[8:11]\n"
"ds_read_b128 a[224:227], v211 offset:2048\n"
"ds_read_b128 a[228:231], v211 offset:3072\n"
"v_mfma_f32_16x16x32_bf16 v[8:11], v[196:199], v[248:251], v[8:11]\n"
"buffer_load_dwordx4 v212, s[36:39], s71 offen offset:1024 sc1 lds\n"
"v_mfma_f32_16x16x32_bf16 v[12:15], v[196:199], v[252:255], v[12:15]\n"
"s_add_u32 m0, s70, 124928\n"
"s_add_u32 s71, s82, 81920\n"
"v_mul_f32 v93, v77, v93\n"
"v_mfma_f32_16x16x32_bf16 v[12:15], v[192:195], v[236:239], v[12:15]\n"
"s_waitcnt lgkmcnt(2)\n"
"v_mul_f32 v94, v78, v94\n"
"v_mul_f32 v95, v79, v95\n"
"v_mfma_f32_16x16x32_bf16 v[28:31], v[200:203], v[236:239], v[28:31]\n"
"ds_read_b128 a[232:235], v211 offset:4096\n"
"ds_read_b128 a[236:239], v211 offset:5120\n"
"v_mfma_f32_16x16x32_bf16 v[28:31], v[204:207], v[252:255], v[28:31]\n"
"buffer_load_dwordx4 v212, s[36:39], s71 offen offset:0 sc1 lds\n"
"v_mfma_f32_16x16x32_bf16 v[24:27], v[204:207], v[248:251], v[24:27]\n"
"v_cvt_pk_bf16_f32 v76, v92, v93\n"
"v_cvt_pk_bf16_f32 v77, v94, v95\n"
"v_mfma_f32_16x16x32_bf16 v[24:27], v[200:203], v[232:235], v[24:27]\n"
"v_max_f32 v108, 0, v108\n"
"v_max_f32 v109, 0, v109\n"
"v_mfma_f32_16x16x32_bf16 v[20:23], v[200:203], v[228:231], v[20:23]\n"
"ds_read_b128 a[240:243], v211 offset:6144\n"
"ds_read_b128 a[244:247], v211 offset:7168\n"
"v_mfma_f32_16x16x32_bf16 v[20:23], v[204:207], v[244:247], v[20:23]\n"
"v_max_f32 v110, 0, v110\n"
"v_max_f32 v111, 0, v111\n"
"v_mfma_f32_16x16x32_bf16 v[16:19], v[204:207], v[240:243], v[16:19]\n"
"buffer_load_dwordx4 v212, s[36:39], s71 offen offset:1024 sc1 lds\n"
"v_mfma_f32_16x16x32_bf16 v[16:19], v[200:203], v[224:227], v[16:19]\n"
"s_waitcnt lgkmcnt(4)\n"
"s_add_u32 m0, s70, 133120\n"
"s_add_u32 s71, s82, 90112\n"
"v_mfma_f32_16x16x32_bf16 v[224:227], a[224:227], a[0:3], 0\n"
"ds_read_b128 a[248:251], v211 offset:8192\n"
"ds_read_b128 a[252:255], v211 offset:9216\n"
"v_mfma_f32_16x16x32_bf16 v[224:227], a[228:231], a[8:11], v[224:227]\n"
"v_mul_f32 v124, v108, v124\n"
"v_mul_f32 v125, v109, v125\n"
"v_mfma_f32_16x16x32_bf16 v[228:231], a[228:231], a[12:15], 0\n"
"buffer_load_dwordx4 v212, s[36:39], s71 offen offset:0 sc1 lds\n"
"v_mfma_f32_16x16x32_bf16 v[228:231], a[224:227], a[4:7], v[228:231]\n"
"s_waitcnt lgkmcnt(4)\n"
"v_mul_f32 v126, v110, v126\n"
"v_mul_f32 v127, v111, v127\n"
"v_mfma_f32_16x16x32_bf16 v[236:239], a[232:235], a[4:7], 0\n"
"ds_read_b128 a[228:231], v212 offset:18432\n"
"ds_read_b128 a[224:227], v212 offset:19456\n"
"v_mfma_f32_16x16x32_bf16 v[236:239], a[236:239], a[12:15], v[236:239]\n"
"v_cvt_pk_bf16_f32 v78, v124, v125\n"
"v_cvt_pk_bf16_f32 v79, v126, v127\n"
"v_mfma_f32_16x16x32_bf16 v[232:235], a[236:239], a[8:11], 0\n"
"buffer_load_dwordx4 v212, s[36:39], s71 offen offset:1024 sc1 lds\n"
"v_mfma_f32_16x16x32_bf16 v[232:235], a[232:235], a[0:3], v[232:235]\n"
"s_waitcnt lgkmcnt(4)\n"
"s_add_u32 m0, s70, 75776\n"
"s_add_u32 s71, s82, 98304\n"
"v_mfma_f32_16x16x32_bf16 v[240:243], a[240:243], a[0:3], 0\n"
"ds_read_b128 a[236:239], v212 offset:20480\n"
"ds_read_b128 a[232:235], v212 offset:21504\n"
"v_mfma_f32_16x16x32_bf16 v[240:243], a[244:247], a[8:11], v[240:243]\n"
"buffer_store_dwordx4 v[76:79], v212, s[36:39], s11 offen offset:1024 sc1\n"
"v_mfma_f32_16x16x32_bf16 v[244:247], a[244:247], a[12:15], 0\n"
"v_lshlrev_b32 v139, 10, v139\n"
"v_lshrrev_b32 v133, 3, v132\n"
"v_mfma_f32_16x16x32_bf16 v[244:247], a[240:243], a[4:7], v[244:247]\n"
"s_waitcnt lgkmcnt(4)\n"
"v_lshl_add_u32 v139, v133, 15, v139\n"
"s_lshl_b32 s66, s61, 10\n"
"v_mfma_f32_16x16x32_bf16 v[252:255], a[248:251], a[4:7], 0\n"
"ds_read_b128 a[244:247], v212 offset:22528\n"
"ds_read_b128 a[240:243], v212 offset:23552\n"
"v_mfma_f32_16x16x32_bf16 v[252:255], a[252:255], a[12:15], v[252:255]\n"
"v_add_u32 v134, s66, v212\n"
"v_mov_b32 v133, 0x40000000\n"
"v_mfma_f32_16x16x32_bf16 v[248:251], a[252:255], a[8:11], 0\n"
"v_mov_b32 v135, 2880\n"
"v_max_f32 v0, 0, v0\n"
"v_mfma_f32_16x16x32_bf16 v[248:251], a[248:251], a[0:3], v[248:251]\n"
"s_waitcnt lgkmcnt(4)\n"
"buffer_load_dwordx4 v212, s[36:39], s71 offen offset:0 sc1 lds\n"
"v_mfma_f32_16x16x32_bf16 v[80:83], a[228:231], a[0:3], 0\n"
"ds_read_b128 a[252:255], v212 offset:24576\n"
"ds_read_b128 a[248:251], v212 offset:25600\n"
"v_mfma_f32_16x16x32_bf16 v[80:83], a[224:227], a[8:11], v[80:83]\n"
"v_cmp_lt_u32 s[92:93], v134, v135\n"
"v_add_u32 v135, 161792, v128\n"
"v_and_b32 v138, 3, v132\n"
"v_cndmask_b32 v134, v133, v134, s[92:93]\n"
"v_mfma_f32_16x16x32_bf16 v[96:99], a[224:227], a[12:15], 0\n"
"s_lshl_b32 s66, s61, 9\n"
"v_add_u32 v215, s66, v135\n"
"v_lshrrev_b32 v133, 4, v132\n"
"v_mfma_f32_16x16x32_bf16 v[96:99], a[228:231], a[4:7], v[96:99]\n"
"s_waitcnt vmcnt(17) lgkmcnt(4)\n"
"ds_write_b32 v214, v219 offset:768\n"
"v_mfma_f32_16x16x32_bf16 v[64:67], a[236:239], a[4:7], 0\n"
"ds_read_b128 a[224:227], v212 offset:26624\n"
"ds_read_b128 a[228:231], v212 offset:27648\n"
"v_mfma_f32_16x16x32_bf16 v[64:67], a[232:235], a[12:15], v[64:67]\n"
"buffer_load_dwordx4 v212, s[36:39], s71 offen offset:1024 sc1 lds\n"
"v_mfma_f32_16x16x32_bf16 v[112:115], a[232:235], a[8:11], 0\n"
"s_add_u32 m0, s70, 83968\n"
"s_add_u32 s71, s82, 106496\n"
"v_lshl_add_u32 v136, v133, 2, v138\n"
"v_mfma_f32_16x16x32_bf16 v[112:115], a[236:239], a[0:3], v[112:115]\n"
"s_waitcnt lgkmcnt(5)\n"
"v_lshlrev_b32 v136, 3, v136\n"
"v_add_u32 v136, 162816, v136\n"
"v_mfma_f32_16x16x32_bf16 v[84:87], a[244:247], a[0:3], 0\n"
"ds_read_b128 a[232:235], v212 offset:28672\n"
"ds_read_b128 a[236:239], v212 offset:29696\n"
"v_mfma_f32_16x16x32_bf16 v[84:87], a[240:243], a[8:11], v[84:87]\n"
"buffer_load_dwordx4 v212, s[36:39], s71 offen offset:0 sc1 lds\n"
"v_mfma_f32_16x16x32_bf16 v[100:103], a[240:243], a[12:15], 0\n"
"ds_read_b32 v221, v220 offset:768\n"
"v_add_u32 v138, 48, v138\n"
"v_mfma_f32_16x16x32_bf16 v[100:103], a[244:247], a[4:7], v[100:103]\n"
"s_waitcnt lgkmcnt(5)\n"
"v_cmp_gt_u32 s[92:93], 16, v132\n"
"v_and_b32 v218, 15, v132\n"
"s_lshl_b32 s66, s61, 6\n"
"v_cndmask_b32 v137, 60, v132, s[92:93]\n"
"v_cndmask_b32 v138, 60, v138, s[92:93]\n"
"v_mfma_f32_16x16x32_bf16 v[68:71], a[252:255], a[4:7], 0\n"
"ds_read_b128 a[240:243], v212 offset:30720\n"
"ds_read_b128 a[244:247], v212 offset:31744\n"
"v_mfma_f32_16x16x32_bf16 v[68:71], a[248:251], a[12:15], v[68:71]\n"
"buffer_load_dwordx4 v212, s[36:39], s71 offen offset:1024 sc1 lds\n"
"v_mfma_f32_16x16x32_bf16 v[116:119], a[248:251], a[8:11], 0\n"
"s_add_u32 m0, s70, 92160\n"
"s_add_u32 s71, s82, 114688\n"
"v_lshlrev_b32 v137, 3, v137\n"
"v_mfma_f32_16x16x32_bf16 v[116:119], a[252:255], a[0:3], v[116:119]\n"
"s_waitcnt lgkmcnt(2)\n"
"v_cmp_ne_u32 vcc, s75, v221\n"
"v_add_u32 v137, 162816, v137\n"
"v_mfma_f32_16x16x32_bf16 v[88:91], a[224:227], a[0:3], 0\n"
"ds_read_b128 a[248:251], v212 offset:32768\n"
"ds_read_b128 a[252:255], v212 offset:33792\n"
"v_mfma_f32_16x16x32_bf16 v[88:91], a[228:231], a[8:11], v[88:91]\n"
"v_lshlrev_b32 v138, 3, v138\n"
"v_add_u32 v138, 162816, v138\n"
"v_mfma_f32_16x16x32_bf16 v[104:107], a[228:231], a[12:15], 0\n"
"s_cbranch_vccnz Lsb24_%=\n"
"Lsr24_%=:\n"
"v_add_u32 v217, s66, v218\n"
"v_mfma_f32_16x16x32_bf16 v[104:107], a[224:227], a[4:7], v[104:107]\n"
"buffer_load_dwordx4 v212, s[36:39], s71 offen offset:0 sc1 lds\n"
"v_mfma_f32_16x16x32_bf16 v[228:231], a[232:235], a[20:23], v[228:231]\n"
"ds_read_b128 a[228:231], v212 offset:34816\n"
"ds_read_b128 a[224:227], v212 offset:35840\n"
"v_mfma_f32_16x16x32_bf16 v[228:231], a[236:239], a[28:31], v[228:231]\n"
"v_bfe_u32 v218, v212, 2, 7\n"
"v_mul_u32_u24 v217, 80, v217\n"
"v_mfma_f32_16x16x32_bf16 v[224:227], a[236:239], a[24:27], v[224:227]\n"
"v_add_u32 v217, 141312, v217\n"
"v_lshl_add_u32 v216, v133, 4, v217\n"
"v_mfma_f32_16x16x32_bf16 v[224:227], a[232:235], a[16:19], v[224:227]\n"
"s_waitcnt lgkmcnt(4)\n"
"buffer_load_dwordx4 v212, s[36:39], s71 offen offset:1024 sc1 lds\n"
"v_mfma_f32_16x16x32_bf16 v[232:235], a[240:243], a[16:19], v[232:235]\n"
"s_add_u32 m0, s70, 2048\n"
"ds_read_b128 a[236:239], v212 offset:36864\n"
"ds_read_b128 a[232:235], v212 offset:37888\n"
"v_mfma_f32_16x16x32_bf16 v[232:235], a[244:247], a[24:27], v[232:235]\n"
"s_add_u32 s71, s82, 122880\n"
"v_max_f32 v1, 0, v1\n"
"v_max_f32 v2, 0, v2\n"
"v_mfma_f32_16x16x32_bf16 v[236:239], a[244:247], a[28:31], v[236:239]\n"
"buffer_load_dword v132, v139, s[36:39], s30 offen offset:640 sc1\n"
"v_mfma_f32_16x16x32_bf16 v[236:239], a[240:243], a[20:23], v[236:239]\n"
"s_waitcnt lgkmcnt(4)\n"
"v_max_f32 v3, 0, v3\n"
"v_mul_f32 v16, v0, v16\n"
"v_mfma_f32_16x16x32_bf16 v[244:247], a[248:251], a[20:23], v[244:247]\n"
"ds_read_b128 a[244:247], v212 offset:38912\n"
"ds_read_b128 a[240:243], v212 offset:39936\n"
"v_mfma_f32_16x16x32_bf16 v[244:247], a[252:255], a[28:31], v[244:247]\n"
"v_mul_f32 v17, v1, v17\n"
"v_mul_f32 v18, v2, v18\n"
"v_mfma_f32_16x16x32_bf16 v[240:243], a[252:255], a[24:27], v[240:243]\n"
"v_mul_f32 v19, v3, v19\n"
"v_cvt_pk_bf16_f32 v0, v16, v17\n"
"v_mfma_f32_16x16x32_bf16 v[240:243], a[248:251], a[16:19], v[240:243]\n"
"s_waitcnt lgkmcnt(4)\n"
"v_cvt_pk_bf16_f32 v1, v18, v19\n"
"v_max_f32 v32, 0, v32\n"
"v_mfma_f32_16x16x32_bf16 v[248:251], a[228:231], a[16:19], v[248:251]\n"
"ds_read_b128 a[252:255], v212 offset:40960\n"
"ds_read_b128 a[248:251], v212 offset:41984\n"
"v_mfma_f32_16x16x32_bf16 v[248:251], a[224:227], a[24:27], v[248:251]\n"
"v_max_f32 v33, 0, v33\n"
"v_max_f32 v34, 0, v34\n"
"v_mfma_f32_16x16x32_bf16 v[252:255], a[224:227], a[28:31], v[252:255]\n"
"v_max_f32 v35, 0, v35\n"
"v_mul_f32 v48, v32, v48\n"
"v_mfma_f32_16x16x32_bf16 v[252:255], a[228:231], a[20:23], v[252:255]\n"
"s_waitcnt lgkmcnt(4)\n"
"buffer_load_dwordx4 v212, s[36:39], s71 offen offset:0 sc1 lds\n"
"v_mfma_f32_16x16x32_bf16 v[96:99], a[236:239], a[20:23], v[96:99]\n"
"ds_read_b128 a[224:227], v212 offset:43008\n"
"ds_read_b128 a[228:231], v212 offset:44032\n"
"v_mfma_f32_16x16x32_bf16 v[96:99], a[232:235], a[28:31], v[96:99]\n"
"v_mul_f32 v49, v33, v49\n"
"v_mul_f32 v50, v34, v50\n"
"v_mfma_f32_16x16x32_bf16 v[80:83], a[232:235], a[24:27], v[80:83]\n"
"v_mul_f32 v51, v35, v51\n"
"v_cvt_pk_bf16_f32 v2, v48, v49\n"
"v_mfma_f32_16x16x32_bf16 v[80:83], a[236:239], a[16:19], v[80:83]\n"
"s_waitcnt lgkmcnt(4)\n"
"buffer_load_dwordx4 v212, s[36:39], s71 offen offset:1024 sc1 lds\n"
"v_mfma_f32_16x16x32_bf16 v[112:115], a[244:247], a[16:19], v[112:115]\n"
"s_add_u32 m0, s70, 10240\n"
"ds_read_b128 a[232:235], v212 offset:45056\n"
"ds_read_b128 a[236:239], v212 offset:46080\n"
"v_mfma_f32_16x16x32_bf16 v[112:115], a[240:243], a[24:27], v[112:115]\n"
"s_add_u32 s71, s82, 131072\n"
"v_cvt_pk_bf16_f32 v3, v50, v51\n"
"v_mfma_f32_16x16x32_bf16 v[64:67], a[240:243], a[28:31], v[64:67]\n"
"buffer_store_dwordx4 v[0:3], v212, s[36:39], s12 offen offset:0 sc1\n"
"v_mfma_f32_16x16x32_bf16 v[64:67], a[244:247], a[20:23], v[64:67]\n"
"s_waitcnt lgkmcnt(4)\n"
"v_max_f32 v4, 0, v4\n"
"v_max_f32 v5, 0, v5\n"
"v_mfma_f32_16x16x32_bf16 v[100:103], a[252:255], a[20:23], v[100:103]\n"
"ds_read_b128 a[240:243], v212 offset:47104\n"
"ds_read_b128 a[244:247], v212 offset:48128\n"
"v_mfma_f32_16x16x32_bf16 v[100:103], a[248:251], a[28:31], v[100:103]\n"
"v_max_f32 v6, 0, v6\n"
"v_max_f32 v7, 0, v7\n"
"v_mfma_f32_16x16x32_bf16 v[84:87], a[248:251], a[24:27], v[84:87]\n"
"v_mul_f32 v20, v4, v20\n"
"v_mul_f32 v21, v5, v21\n"
"v_mfma_f32_16x16x32_bf16 v[84:87], a[252:255], a[16:19], v[84:87]\n"
"s_waitcnt lgkmcnt(4)\n"
"v_mul_f32 v22, v6, v22\n"
"v_mul_f32 v23, v7, v23\n"
"v_mfma_f32_16x16x32_bf16 v[116:119], a[224:227], a[16:19], v[116:119]\n"
"ds_read_b128 a[248:251], v212 offset:49152\n"
"ds_read_b128 a[252:255], v212 offset:50176\n"
"v_mfma_f32_16x16x32_bf16 v[116:119], a[228:231], a[24:27], v[116:119]\n"
"buffer_load_dwordx4 v212, s[36:39], s71 offen offset:0 sc1 lds\n"
"v_mfma_f32_16x16x32_bf16 v[68:71], a[228:231], a[28:31], v[68:71]\n"
"s_waitcnt vmcnt(19)\n"
"ds_write_b32 v214, v219 offset:800\n"
"v_mfma_f32_16x16x32_bf16 v[68:71], a[224:227], a[20:23], v[68:71]\n"
"s_waitcnt lgkmcnt(5)\n"
"v_add_u32 v210, 49152, v212\n"
"v_cvt_pk_bf16_f32 v4, v20, v21\n"
"v_mfma_f32_16x16x32_bf16 v[104:107], a[232:235], a[20:23], v[104:107]\n"
"ds_read_b128 a[228:231], v210 offset:2048\n"
"ds_read_b128 a[224:227], v210 offset:3072\n"
"v_mfma_f32_16x16x32_bf16 v[104:107], a[236:239], a[28:31], v[104:107]\n"
"buffer_load_dwordx4 v212, s[36:39], s71 offen offset:1024 sc1 lds\n"
"v_mfma_f32_16x16x32_bf16 v[88:91], a[236:239], a[24:27], v[88:91]\n"
"s_add_u32 m0, s70, 100352\n"
"s_add_u32 s71, s82, 139264\n"
"v_cvt_pk_bf16_f32 v5, v22, v23\n"
"v_mfma_f32_16x16x32_bf16 v[88:91], a[232:235], a[16:19], v[88:91]\n"
"s_waitcnt vmcnt(12) lgkmcnt(5)\n"
"buffer_store_dword v219, off, s[36:39], s23 offset:256 sc1\n"
"v_mfma_f32_16x16x32_bf16 v[224:227], a[240:243], a[32:35], v[224:227]\n"
"ds_read_b128 a[236:239], v210 offset:4096\n"
"ds_read_b128 a[232:235], v210 offset:5120\n"
"v_mfma_f32_16x16x32_bf16 v[224:227], a[244:247], a[40:43], v[224:227]\n"
"v_max_f32 v36, 0, v36\n"
"v_max_f32 v37, 0, v37\n"
"v_mfma_f32_16x16x32_bf16 v[228:231], a[244:247], a[44:47], v[228:231]\n"
"ds_read_b32 v221, v220 offset:800\n"
"v_max_f32 v38, 0, v38\n"
"v_mfma_f32_16x16x32_bf16 v[228:231], a[240:243], a[36:39], v[228:231]\n"
"s_waitcnt lgkmcnt(5)\n"
"buffer_load_dwordx4 v212, s[36:39], s71 offen offset:0 sc1 lds\n"
"v_mfma_f32_16x16x32_bf16 v[236:239], a[248:251], a[36:39], v[236:239]\n"
"ds_read_b128 a[244:247], v210 offset:6144\n"
"ds_read_b128 a[240:243], v210 offset:7168\n"
"v_mfma_f32_16x16x32_bf16 v[236:239], a[252:255], a[44:47], v[236:239]\n"
"v_max_f32 v39, 0, v39\n"
"v_mul_f32 v52, v36, v52\n"
"v_mfma_f32_16x16x32_bf16 v[232:235], a[252:255], a[40:43], v[232:235]\n"
"v_mul_f32 v53, v37, v53\n"
"v_mul_f32 v54, v38, v54\n"
"v_mfma_f32_16x16x32_bf16 v[232:235], a[248:251], a[32:35], v[232:235]\n"
"s_waitcnt lgkmcnt(2)\n"
"v_cmp_ne_u32 vcc, s75, v221\n"
"v_mul_f32 v55, v39, v55\n"
"v_mfma_f32_16x16x32_bf16 v[240:243], a[228:231], a[32:35], v[240:243]\n"
"ds_read_b128 a[252:255], v210 offset:8192\n"
"ds_read_b128 a[248:251], v210 offset:9216\n"
"v_mfma_f32_16x16x32_bf16 v[240:243], a[224:227], a[40:43], v[240:243]\n"
"buffer_load_dwordx4 v212, s[36:39], s71 offen offset:1024 sc1 lds\n"
"v_mfma_f32_16x16x32_bf16 v[244:247], a[224:227], a[44:47], v[244:247]\n"
"s_cbranch_vccnz Lsb25_%=\n"
"Lsr25_%=:\n"
"s_add_u32 m0, s70, 18432\n"
"v_mfma_f32_16x16x32_bf16 v[244:247], a[228:231], a[36:39], v[244:247]\n"
"s_add_u32 s71, s82, 147456\n"
"v_cvt_pk_bf16_f32 v6, v52, v53\n"
"v_cvt_pk_bf16_f32 v7, v54, v55\n"
"v_mfma_f32_16x16x32_bf16 v[252:255], a[236:239], a[36:39], v[252:255]\n"
"ds_read_b128 a[224:227], v210 offset:10240\n"
"ds_read_b128 a[228:231], v210 offset:11264\n"
"v_mfma_f32_16x16x32_bf16 v[252:255], a[232:235], a[44:47], v[252:255]\n"
"buffer_load_dwordx4 v212, s[36:39], s71 offen offset:0 sc1 lds\n"
"v_mfma_f32_16x16x32_bf16 v[248:251], a[232:235], a[40:43], v[248:251]\n"
"buffer_store_dwordx4 v[4:7], v212, s[36:39], s12 offen offset:1024 sc1\n"
"v_mfma_f32_16x16x32_bf16 v[248:251], a[236:239], a[32:35], v[248:251]\n"
"s_waitcnt lgkmcnt(4)\n"
"v_max_f32 v8, 0, v8\n"
"v_max_f32 v9, 0, v9\n"
"v_mfma_f32_16x16x32_bf16 v[80:83], a[244:247], a[32:35], v[80:83]\n"
"ds_read_b128 a[232:235], v210 offset:12288\n"
"ds_read_b128 a[236:239], v210 offset:13312\n"
"v_mfma_f32_16x16x32_bf16 v[80:83], a[240:243], a[40:43], v[80:83]\n"
"v_max_f32 v10, 0, v10\n"
"v_max_f32 v11, 0, v11\n"
"v_mfma_f32_16x16x32_bf16 v[96:99], a[240:243], a[44:47], v[96:99]\n"
"v_mul_f32 v24, v8, v24\n"
"v_mul_f32 v25, v9, v25\n"
"v_mfma_f32_16x16x32_bf16 v[96:99], a[244:247], a[36:39], v[96:99]\n"
"s_waitcnt lgkmcnt(4)\n"
"v_mul_f32 v26, v10, v26\n"
"v_mul_f32 v27, v11, v27\n"
"v_mfma_f32_16x16x32_bf16 v[64:67], a[252:255], a[36:39], v[64:67]\n"
"ds_read_b128 a[240:243], v210 offset:14336\n"
"ds_read_b128 a[244:247], v210 offset:15360\n"
"v_mfma_f32_16x16x32_bf16 v[64:67], a[248:251], a[44:47], v[64:67]\n"
"s_waitcnt vmcnt(10)\n"
"v_cmp_ne_u32 s[92:93], s75, v132\n"
"v_cvt_pk_bf16_f32 v8, v24, v25\n"
"v_mfma_f32_16x16x32_bf16 v[112:115], a[248:251], a[40:43], v[112:115]\n"
"buffer_load_dwordx4 v212, s[36:39], s71 offen offset:1024 sc1 lds\n"
"v_mfma_f32_16x16x32_bf16 v[112:115], a[252:255], a[32:35], v[112:115]\n"
"s_waitcnt lgkmcnt(4)\n"
"s_add_u32 m0, s70, 26624\n"
"s_add_u32 s71, s82, 155648\n"
"v_mfma_f32_16x16x32_bf16 v[84:87], a[224:227], a[32:35], v[84:87]\n"
"ds_read_b128 a[248:251], v210 offset:16384\n"
"ds_read_b128 a[252:255], v210 offset:17408\n"
"v_mfma_f32_16x16x32_bf16 v[84:87], a[228:231], a[40:43], v[84:87]\n"
"s_cmp_lg_u64 s[92:93], 0\n"
"s_cbranch_scc1 Lslow2_%=\n"
"Lback2_%=:\n"
"v_mfma_f32_16x16x32_bf16 v[100:103], a[228:231], a[44:47], v[100:103]\n"
"buffer_load_dwordx4 v212, s[36:39], s71 offen offset:0 sc1 lds\n"
"v_mfma_f32_16x16x32_bf16 v[100:103], a[224:227], a[36:39], v[100:103]\n"
"s_waitcnt lgkmcnt(4)\n"
"v_cvt_pk_bf16_f32 v9, v26, v27\n"
"v_max_f32 v40, 0, v40\n"
"v_mfma_f32_16x16x32_bf16 v[68:71], a[232:235], a[36:39], v[68:71]\n"
"ds_read_b128 a[228:231], v210 offset:18432\n"
"ds_read_b128 a[224:227], v210 offset:19456\n"
"v_mfma_f32_16x16x32_bf16 v[68:71], a[236:239], a[44:47], v[68:71]\n"
"v_max_f32 v41, 0, v41\n"
"v_max_f32 v42, 0, v42\n"
"v_mfma_f32_16x16x32_bf16 v[116:119], a[236:239], a[40:43], v[116:119]\n"
"v_max_f32 v43, 0, v43\n"
"v_mul_f32 v56, v40, v56\n"
"v_mfma_f32_16x16x32_bf16 v[116:119], a[232:235], a[32:35], v[116:119]\n"
"s_waitcnt lgkmcnt(4)\n"
"buffer_load_dwordx4 v212, s[36:39], s71 offen offset:1024 sc1 lds\n"
"v_mfma_f32_16x16x32_bf16 v[88:91], a[240:243], a[32:35], v[88:91]\n"
"s_add_u32 m0, s70, 34816\n"
"ds_read_b128 a[236:239], v210 offset:20480\n"
"ds_read_b128 a[232:235], v210 offset:21504\n"
"v_mfma_f32_16x16x32_bf16 v[88:91], a[244:247], a[40:43], v[88:91]\n"
"s_add_u32 s71, s82, 163840\n"
"v_mul_f32 v57, v41, v57\n"
"v_mul_f32 v58, v42, v58\n"
"v_mfma_f32_16x16x32_bf16 v[104:107], a[244:247], a[44:47], v[104:107]\n"
"v_mul_f32 v59, v43, v59\n"
"v_cvt_pk_bf16_f32 v10, v56, v57\n"
"v_mfma_f32_16x16x32_bf16 v[104:107], a[240:243], a[36:39], v[104:107]\n"
"s_waitcnt lgkmcnt(4)\n"
"buffer_load_dwordx4 v212, s[36:39], s71 offen offset:0 sc1 lds\n"
"v_mfma_f32_16x16x32_bf16 v[228:231], a[248:251], a[52:55], v[228:231]\n"
"ds_read_b128 a[244:247], v210 offset:22528\n"
"ds_read_b128 a[240:243], v210 offset:23552\n"
"v_mfma_f32_16x16x32_bf16 v[228:231], a[252:255], a[60:63], v[228:231]\n"
"Lpad8_%=:\n"
"s_cbranch_execz Lret_%=\n"
"v_cvt_pk_bf16_f32 v11, v58, v59\n"
"v_mfma_f32_16x16x32_bf16 v[224:227], a[252:255], a[56:59], v[224:227]\n"
"buffer_store_dwordx4 v[8:11], v212, s[36:39], s13 offen offset:0 sc1\n"
"v_mfma_f32_16x16x32_bf16 v[224:227], a[248:251], a[48:51], v[224:227]\n"
"s_waitcnt lgkmcnt(4)\n"
"v_max_f32 v12, 0, v12\n"
"v_max_f32 v13, 0, v13\n"
"v_mfma_f32_16x16x32_bf16 v[232:235], a[228:231], a[48:51], v[232:235]\n"
"ds_read_b128 a[252:255], v210 offset:24576\n"
"ds_read_b128 a[248:251], v210 offset:25600\n"
"v_mfma_f32_16x16x32_bf16 v[232:235], a[224:227], a[56:59], v[232:235]\n"
"v_max_f32 v14, 0, v14\n"
"v_max_f32 v15, 0, v15\n"
"v_mfma_f32_16x16x32_bf16 v[236:239], a[224:227], a[60:63], v[236:239]\n"
"v_mul_f32 v28, v12, v28\n"
"v_mul_f32 v29, v13, v29\n"
"v_mfma_f32_16x16x32_bf16 v[236:239], a[228:231], a[52:55], v[236:239]\n"
"s_waitcnt lgkmcnt(4)\n"
"ds_write_b32 v214, v219 offset:832\n"
"v_mfma_f32_16x16x32_bf16 v[244:247], a[236:239], a[52:55], v[244:247]\n"
"ds_read_b128 a[224:227], v211 offset:10240\n"
"ds_read_b128 a[228:231], v211 offset:11264\n"
"v_mfma_f32_16x16x32_bf16 v[244:247], a[232:235], a[60:63], v[244:247]\n"
"buffer_load_dwordx4 v212, s[36:39], s71 offen offset:1024 sc1 lds\n"
"v_mfma_f32_16x16x32_bf16 v[240:243], a[232:235], a[56:59], v[240:243]\n"
"v_mul_f32 v30, v14, v30\n"
"v_mul_f32 v31, v15, v31\n"
"v_mfma_f32_16x16x32_bf16 v[240:243], a[236:239], a[48:51], v[240:243]\n"
"s_waitcnt lgkmcnt(5)\n"
"v_cvt_pk_bf16_f32 v12, v28, v29\n"
"v_cvt_pk_bf16_f32 v13, v30, v31\n"
"v_mfma_f32_16x16x32_bf16 v[248:251], a[244:247], a[48:51], v[248:251]\n"
"ds_read_b128 a[232:235], v211 offset:12288\n"
"ds_read_b128 a[236:239], v211 offset:13312\n"
"v_mfma_f32_16x16x32_bf16 v[248:251], a[240:243], a[56:59], v[248:251]\n"
"v_max_f32 v44, 0, v44\n"
"v_max_f32 v45, 0, v45\n"
"v_mfma_f32_16x16x32_bf16 v[252:255], a[240:243], a[60:63], v[252:255]\n"
"ds_read_b32 v221, v220 offset:832\n"
"v_max_f32 v46, 0, v46\n"
"v_mfma_f32_16x16x32_bf16 v[252:255], a[244:247], a[52:55], v[252:255]\n"
"s_waitcnt lgkmcnt(5)\n"
"v_max_f32 v47, 0, v47\n"
"v_mul_f32 v60, v44, v60\n"
"v_mfma_f32_16x16x32_bf16 v[96:99], a[252:255], a[52:55], v[96:99]\n"
"ds_read_b128 a[240:243], v211 offset:14336\n"
"ds_read_b128 a[244:247], v211 offset:15360\n"
"v_mfma_f32_16x16x32_bf16 v[96:99], a[248:251], a[60:63], v[96:99]\n"
"v_mul_f32 v61, v45, v61\n"
"v_mul_f32 v62, v46, v62\n"
"v_mfma_f32_16x16x32_bf16 v[80:83], a[248:251], a[56:59], v[80:83]\n"
"v_mul_f32 v63, v47, v63\n"
"v_cvt_pk_bf16_f32 v14, v60, v61\n"
"v_mfma_f32_16x16x32_bf16 v[80:83], a[252:255], a[48:51], v[80:83]\n"
"s_waitcnt lgkmcnt(2)\n"
"v_cmp_ne_u32 vcc, s75, v221\n"
"v_cvt_pk_bf16_f32 v15, v62, v63\n"
"v_mfma_f32_16x16x32_bf16 v[112:115], a[224:227], a[48:51], v[112:115]\n"
"ds_read_b128 a[248:251], v211 offset:16384\n"
"ds_read_b128 a[252:255], v211 offset:17408\n"
"v_mfma_f32_16x16x32_bf16 v[112:115], a[228:231], a[56:59], v[112:115]\n"
"buffer_store_dwordx4 v[12:15], v212, s[36:39], s13 offen offset:1024 sc1\n"
"v_mfma_f32_16x16x32_bf16 v[64:67], a[228:231], a[60:63], v[64:67]\n"
"s_cbranch_vccnz Lsb26_%=\n"
"Lsr26_%=:\n"
"s_add_u32 m0, s70, 43008\n"
"v_mfma_f32_16x16x32_bf16 v[64:67], a[224:227], a[52:55], v[64:67]\n"
"s_add_u32 s71, s82, 172032\n"
"v_mfma_f32_16x16x32_bf16 v[100:103], a[232:235], a[52:55], v[100:103]\n"
"ds_read_b128 a[228:231], v211 offset:18432\n"
"ds_read_b128 a[224:227], v211 offset:19456\n"
"v_mfma_f32_16x16x32_bf16 v[100:103], a[236:239], a[60:63], v[100:103]\n"
"buffer_load_dwordx4 v212, s[36:39], s71 offen offset:0 sc1 lds\n"
"v_mfma_f32_16x16x32_bf16 v[84:87], a[236:239], a[56:59], v[84:87]\n"
"buffer_load_dwordx4 v[160:163], v134, s[36:39], s22 offen offset:0 sc1\n"
"v_mfma_f32_16x16x32_bf16 v[84:87], a[232:235], a[48:51], v[84:87]\n"
"s_waitcnt lgkmcnt(4)\n"
"s_add_u32 s22, s22, 4096\n"
"v_mfma_f32_16x16x32_bf16 v[116:119], a[240:243], a[48:51], v[116:119]\n"
"ds_read_b128 a[236:239], v211 offset:20480\n"
"ds_read_b128 a[232:235], v211 offset:21504\n"
"v_mfma_f32_16x16x32_bf16 v[116:119], a[244:247], a[56:59], v[116:119]\n"
"v_mfma_f32_16x16x32_bf16 v[68:71], a[244:247], a[60:63], v[68:71]\n"
"buffer_load_dwordx4 v[164:167], v134, s[36:39], s22 offen offset:0 sc1\n"
"v_mfma_f32_16x16x32_bf16 v[68:71], a[240:243], a[52:55], v[68:71]\n"
"s_waitcnt lgkmcnt(4)\n"
"s_add_u32 s22, s22, 4096\n"
"v_mfma_f32_16x16x32_bf16 v[104:107], a[248:251], a[52:55], v[104:107]\n"
"ds_read_b128 a[244:247], v211 offset:22528\n"
"ds_read_b128 a[240:243], v211 offset:23552\n"
"v_mfma_f32_16x16x32_bf16 v[104:107], a[252:255], a[60:63], v[104:107]\n"
"buffer_load_dwordx4 v[168:171], v134, s[36:39], s22 offen offset:0 sc1\n"
"v_mfma_f32_16x16x32_bf16 v[88:91], a[252:255], a[56:59], v[88:91]\n"
"s_add_u32 s22, s22, 4096\n"
"v_mfma_f32_16x16x32_bf16 v[88:91], a[248:251], a[48:51], v[88:91]\n"
"s_waitcnt lgkmcnt(4)\n"
"v_mfma_f32_16x16x32_bf16 v[224:227], a[228:231], a[64:67], v[224:227]\n"
"ds_read_b128 a[252:255], v211 offset:24576\n"
"ds_read_b128 a[248:251], v211 offset:25600\n"
"v_mfma_f32_16x16x32_bf16 v[224:227], a[224:227], a[72:75], v[224:227]\n"
"buffer_load_dwordx4 v[172:175], v134, s[36:39], s22 offen offset:0 sc1\n"
"v_mfma_f32_16x16x32_bf16 v[228:231], a[224:227], a[76:79], v[228:231]\n"
"s_add_u32 s22, s22, 4096\n"
"v_mfma_f32_16x16x32_bf16 v[228:231], a[228:231], a[68:71], v[228:231]\n"
"s_waitcnt lgkmcnt(4)\n"
"buffer_load_dwordx4 v[176:179], v134, s[36:39], s22 offen offset:0 sc1\n"
"v_mfma_f32_16x16x32_bf16 v[236:239], a[236:239], a[68:71], v[236:239]\n"
"s_add_u32 s22, s22, 4096\n"
"ds_read_b128 a[224:227], v211 offset:26624\n"
"ds_read_b128 a[228:231], v211 offset:27648\n"
"v_mfma_f32_16x16x32_bf16 v[236:239], a[232:235], a[76:79], v[236:239]\n"
"v_mfma_f32_16x16x32_bf16 v[232:235], a[232:235], a[72:75], v[232:235]\n"
"v_mfma_f32_16x16x32_bf16 v[232:235], a[236:239], a[64:67], v[232:235]\n"
"s_waitcnt lgkmcnt(4)\n"
"buffer_load_dwordx4 v[180:183], v134, s[36:39], s22 offen offset:0 sc1\n"
"v_mfma_f32_16x16x32_bf16 v[240:243], a[244:247], a[64:67], v[240:243]\n"
"s_add_u32 s22, s22, 4096\n"
"ds_read_b128 a[232:235], v211 offset:28672\n"
"ds_read_b128 a[236:239], v211 offset:29696\n"
"v_mfma_f32_16x16x32_bf16 v[240:243], a[240:243], a[72:75], v[240:243]\n"
"v_mfma_f32_16x16x32_bf16 v[244:247], a[240:243], a[76:79], v[244:247]\n"
"buffer_load_dwordx4 v[184:187], v134, s[36:39], s22 offen offset:0 sc1\n"
"v_mfma_f32_16x16x32_bf16 v[244:247], a[244:247], a[68:71], v[244:247]\n"
"s_waitcnt lgkmcnt(4)\n"
"s_add_u32 s22, s22, 4096\n"
"v_mfma_f32_16x16x32_bf16 v[252:255], a[252:255], a[68:71], v[252:255]\n"
"ds_read_b128 a[240:243], v211 offset:30720\n"
"ds_read_b128 a[244:247], v211 offset:31744\n"
"v_mfma_f32_16x16x32_bf16 v[252:255], a[248:251], a[76:79], v[252:255]\n"
"buffer_load_dwordx4 v[188:191], v134, s[36:39], s22 offen offset:0 sc1\n"
"v_mfma_f32_16x16x32_bf16 v[248:251], a[248:251], a[72:75], v[248:251]\n"
"ds_read_b128 v[32:35], v216 offset:0\n"
"v_mfma_f32_16x16x32_bf16 v[248:251], a[252:255], a[64:67], v[248:251]\n"
"s_waitcnt lgkmcnt(5)\n"
"v_mfma_f32_16x16x32_bf16 v[80:83], a[224:227], a[64:67], v[80:83]\n"
"ds_read_b128 a[248:251], v211 offset:32768\n"
"ds_read_b128 a[252:255], v211 offset:33792\n"
"v_mfma_f32_16x16x32_bf16 v[80:83], a[228:231], a[72:75], v[80:83]\n"
"v_and_b32 v129, 896, v212\n"
"v_lshlrev_b32 v129, 5, v129\n"
"v_mfma_f32_16x16x32_bf16 v[96:99], a[228:231], a[76:79], v[96:99]\n"
"buffer_load_dword v129, v129, s[36:39], s28 offen offset:256 sc1\n"
"v_mfma_f32_16x16x32_bf16 v[96:99], a[224:227], a[68:71], v[96:99]\n"
"s_waitcnt lgkmcnt(5)\n"
"ds_write_b32 v214, v219 offset:864\n"
"v_mfma_f32_16x16x32_bf16 v[64:67], a[232:235], a[68:71], v[64:67]\n"
"ds_read_b128 a[228:231], v211 offset:34816\n"
"ds_read_b128 a[224:227], v211 offset:35840\n"
"v_mfma_f32_16x16x32_bf16 v[64:67], a[236:239], a[76:79], v[64:67]\n"
"ds_read_b64 v[48:49], v217 offset:64\n"
"v_mfma_f32_16x16x32_bf16 v[112:115], a[236:239], a[72:75], v[112:115]\n"
"v_mfma_f32_16x16x32_bf16 v[112:115], a[232:235], a[64:67], v[112:115]\n"
"s_waitcnt lgkmcnt(6)\n"
"ds_read_b128 v[36:39], v216 offset:1280\n"
"v_mfma_f32_16x16x32_bf16 v[84:87], a[240:243], a[64:67], v[84:87]\n"
"ds_read_b128 a[236:239], v211 offset:36864\n"
"ds_read_b128 a[232:235], v211 offset:37888\n"
"v_mfma_f32_16x16x32_bf16 v[84:87], a[244:247], a[72:75], v[84:87]\n"
"buffer_load_dwordx4 v212, s[36:39], s71 offen offset:1024 sc1 lds\n"
"v_mfma_f32_16x16x32_bf16 v[100:103], a[244:247], a[76:79], v[100:103]\n"
"s_add_u32 m0, s70, 51200\n"
"ds_read_b32 v221, v220 offset:864\n"
"s_add_u32 s71, s82, 180224\n"
"v_mfma_f32_16x16x32_bf16 v[100:103], a[240:243], a[68:71], v[100:103]\n"
"s_waitcnt lgkmcnt(7)\n"
"ds_read_b64 v[50:51], v217 offset:1344\n"
"v_mfma_f32_16x16x32_bf16 v[68:71], a[248:251], a[68:71], v[68:71]\n"
"ds_read_b128 a[244:247], v211 offset:38912\n"
"ds_read_b128 a[240:243], v211 offset:39936\n"
"v_mfma_f32_16x16x32_bf16 v[68:71], a[252:255], a[76:79], v[68:71]\n"
"buffer_load_dwordx4 v212, s[36:39], s71 offen offset:0 sc1 lds\n"
"v_mfma_f32_16x16x32_bf16 v[116:119], a[252:255], a[72:75], v[116:119]\n"
"ds_read_b128 v[40:43], v216 offset:2560\n"
"v_mfma_f32_16x16x32_bf16 v[116:119], a[248:251], a[64:67], v[116:119]\n"
"s_waitcnt lgkmcnt(4)\n"
"v_cmp_ne_u32 vcc, s75, v221\n"
"v_mfma_f32_16x16x32_bf16 v[88:91], a[228:231], a[64:67], v[88:91]\n"
"ds_read_b128 a[252:255], v211 offset:40960\n"
"ds_read_b128 a[248:251], v211 offset:41984\n"
"v_mfma_f32_16x16x32_bf16 v[88:91], a[224:227], a[72:75], v[88:91]\n"
"buffer_load_dwordx4 v212, s[36:39], s71 offen offset:1024 sc1 lds\n"
"v_mfma_f32_16x16x32_bf16 v[104:107], a[224:227], a[76:79], v[104:107]\n"
"s_cbranch_vccnz Lsb27_%=\n"
"Lsr27_%=:\n"
"s_add_u32 m0, s70, 59392\n"
"v_mfma_f32_16x16x32_bf16 v[104:107], a[228:231], a[68:71], v[104:107]\n"
"s_add_u32 s71, s82, 188416\n"
"ds_read_b64 v[52:53], v217 offset:2624\n"
"v_mfma_f32_16x16x32_bf16 v[228:231], a[236:239], a[84:87], v[228:231]\n"
"ds_read_b128 a[224:227], v210 offset:26624\n"
"ds_read_b128 a[228:231], v210 offset:27648\n"
"v_mfma_f32_16x16x32_bf16 v[228:231], a[232:235], a[92:95], v[228:231]\n"
"ds_read_b128 v[44:47], v216 offset:3840\n"
"v_mfma_f32_16x16x32_bf16 v[224:227], a[232:235], a[88:91], v[224:227]\n"
"buffer_load_dwordx4 v212, s[36:39], s71 offen offset:0 sc1 lds\n"
"v_mfma_f32_16x16x32_bf16 v[224:227], a[236:239], a[80:83], v[224:227]\n"
"s_waitcnt lgkmcnt(6)\n"
"ds_read_b64 v[54:55], v217 offset:3904\n"
"v_mfma_f32_16x16x32_bf16 v[232:235], a[244:247], a[80:83], v[232:235]\n"
"ds_read_b128 a[232:235], v210 offset:28672\n"
"ds_read_b128 a[236:239], v210 offset:29696\n"
"v_mfma_f32_16x16x32_bf16 v[232:235], a[240:243], a[88:91], v[232:235]\n"
"s_waitcnt vmcnt(14)\n"
"buffer_store_dword v219, off, s[36:39], s23 offset:512 sc1\n"
"v_mfma_f32_16x16x32_bf16 v[236:239], a[240:243], a[92:95], v[236:239]\n"
"v_mfma_f32_16x16x32_bf16 v[236:239], a[244:247], a[84:87], v[236:239]\n"
"s_waitcnt lgkmcnt(6)\n"
"buffer_load_dwordx4 v212, s[36:39], s71 offen offset:1024 sc1 lds\n"
"v_mfma_f32_16x16x32_bf16 v[244:247], a[252:255], a[84:87], v[244:247]\n"
"s_add_u32 m0, s70, 67584\n"
"ds_read_b128 a[240:243], v210 offset:30720\n"
"ds_read_b128 a[244:247], v210 offset:31744\n"
"v_mfma_f32_16x16x32_bf16 v[244:247], a[248:251], a[92:95], v[244:247]\n"
"s_add_u32 s71, s82, 196608\n"
"v_mfma_f32_16x16x32_bf16 v[240:243], a[248:251], a[88:91], v[240:243]\n"
"v_mfma_f32_16x16x32_bf16 v[240:243], a[252:255], a[80:83], v[240:243]\n"
"s_waitcnt lgkmcnt(4)\n"
"v_mfma_f32_16x16x32_bf16 v[248:251], a[224:227], a[80:83], v[248:251]\n"
"ds_read_b128 a[248:251], v210 offset:32768\n"
"ds_read_b128 a[252:255], v210 offset:33792\n"
"v_mfma_f32_16x16x32_bf16 v[248:251], a[228:231], a[88:91], v[248:251]\n"
"buffer_load_dwordx4 v212, s[36:39], s71 offen offset:0 sc1 lds\n"
"v_mfma_f32_16x16x32_bf16 v[252:255], a[228:231], a[92:95], v[252:255]\n"
"s_waitcnt vmcnt(14)\n"
"v_add_f32 v160, v160, v164\n"
"v_add_f32 v161, v161, v165\n"
"v_mfma_f32_16x16x32_bf16 v[252:255], a[224:227], a[84:87], v[252:255]\n"
"s_waitcnt lgkmcnt(4)\n"
"v_add_f32 v162, v162, v166\n"
"v_add_f32 v163, v163, v167\n"
"v_mfma_f32_16x16x32_bf16 v[96:99], a[232:235], a[84:87], v[96:99]\n"
"ds_read_b128 a[228:231], v210 offset:34816\n"
"ds_read_b128 a[224:227], v210 offset:35840\n"
"v_mfma_f32_16x16x32_bf16 v[96:99], a[236:239], a[92:95], v[96:99]\n"
"buffer_load_dwordx4 v212, s[36:39], s71 offen offset:1024 sc1 lds\n"
"v_mfma_f32_16x16x32_bf16 v[80:83], a[236:239], a[88:91], v[80:83]\n"
"s_add_u32 m0, s70, 108544\n"
"s_add_u32 s71, s82, 204800\n"
"v_mfma_f32_16x16x32_bf16 v[80:83], a[232:235], a[80:83], v[80:83]\n"
"s_waitcnt vmcnt(14) lgkmcnt(4)\n"
"v_add_f32 v160, v160, v168\n"
"v_add_f32 v161, v161, v169\n"
"v_mfma_f32_16x16x32_bf16 v[112:115], a[240:243], a[80:83], v[112:115]\n"
"ds_read_b128 a[236:239], v210 offset:36864\n"
"ds_read_b128 a[232:235], v210 offset:37888\n"
"v_mfma_f32_16x16x32_bf16 v[112:115], a[244:247], a[88:91], v[112:115]\n"
"buffer_load_dwordx4 v212, s[36:39], s71 offen offset:0 sc1 lds\n"
"v_mfma_f32_16x16x32_bf16 v[64:67], a[244:247], a[92:95], v[64:67]\n"
"v_add_f32 v162, v162, v170\n"
"v_add_f32 v163, v163, v171\n"
"v_mfma_f32_16x16x32_bf16 v[64:67], a[240:243], a[84:87], v[64:67]\n"
"s_waitcnt vmcnt(14) lgkmcnt(4)\n"
"v_add_f32 v160, v160, v172\n"
"v_add_f32 v161, v161, v173\n"
"v_mfma_f32_16x16x32_bf16 v[100:103], a[248:251], a[84:87], v[100:103]\n"
"ds_read_b128 a[244:247], v210 offset:38912\n"
"ds_read_b128 a[240:243], v210 offset:39936\n"
"v_mfma_f32_16x16x32_bf16 v[100:103], a[252:255], a[92:95], v[100:103]\n"
"v_add_f32 v162, v162, v174\n"
"v_add_f32 v163, v163, v175\n"
"v_mfma_f32_16x16x32_bf16 v[84:87], a[252:255], a[88:91], v[84:87]\n"
"buffer_load_dwordx4 v212, s[36:39], s71 offen offset:1024 sc1 lds\n"
"v_mfma_f32_16x16x32_bf16 v[84:87], a[248:251], a[80:83], v[84:87]\n"
"s_waitcnt lgkmcnt(4)\n"
"s_add_u32 m0, s70, 116736\n"
"s_add_u32 s71, s82, 212992\n"
"v_mfma_f32_16x16x32_bf16 v[116:119], a[228:231], a[80:83], v[116:119]\n"
"ds_read_b128 a[252:255], v210 offset:40960\n"
"ds_read_b128 a[248:251], v210 offset:41984\n"
"v_mfma_f32_16x16x32_bf16 v[116:119], a[224:227], a[88:91], v[116:119]\n"
"s_waitcnt vmcnt(14)\n"
"v_add_f32 v160, v160, v176\n"
"v_add_f32 v161, v161, v177\n"
"v_mfma_f32_16x16x32_bf16 v[68:71], a[224:227], a[92:95], v[68:71]\n"
"buffer_load_dwordx4 v212, s[36:39], s71 offen offset:0 sc1 lds\n"
"v_mfma_f32_16x16x32_bf16 v[68:71], a[228:231], a[84:87], v[68:71]\n"
"s_waitcnt lgkmcnt(4)\n"
"ds_write_b32 v214, v219 offset:896\n"
"v_mfma_f32_16x16x32_bf16 v[104:107], a[236:239], a[84:87], v[104:107]\n"
"ds_read_b128 a[224:227], v210 offset:43008\n"
"ds_read_b128 a[228:231], v210 offset:44032\n"
"v_mfma_f32_16x16x32_bf16 v[104:107], a[232:235], a[92:95], v[104:107]\n"
"s_waitcnt vmcnt(11)\n"
"v_cmp_ne_u32 s[34:35], s75, v129\n"
"v_add_f32 v162, v162, v178\n"
"v_mfma_f32_16x16x32_bf16 v[88:91], a[232:235], a[88:91], v[88:91]\n"
"v_add_f32 v163, v163, v179\n"
"v_add_f32 v160, v160, v180\n"
"v_mfma_f32_16x16x32_bf16 v[88:91], a[236:239], a[80:83], v[88:91]\n"
"s_waitcnt lgkmcnt(5)\n"
"buffer_load_dwordx4 v212, s[36:39], s71 offen offset:1024 sc1 lds\n"
"v_mfma_f32_16x16x32_bf16 v[224:227], a[244:247], a[96:99], v[224:227]\n"
"ds_read_b128 a[232:235], v210 offset:45056\n"
"ds_read_b128 a[236:239], v210 offset:46080\n"
"v_mfma_f32_16x16x32_bf16 v[224:227], a[240:243], a[104:107], v[224:227]\n"
"s_cmp_lg_u64 s[34:35], 0\n"
"s_cbranch_scc1 Lslow5_%=\n"
"Lback5_%=:\n"
"v_mfma_f32_16x16x32_bf16 v[228:231], a[240:243], a[108:111], v[228:231]\n"
"ds_read_b32 v221, v220 offset:896\n"
"v_add_f32 v161, v161, v181\n"
"v_mfma_f32_16x16x32_bf16 v[228:231], a[244:247], a[100:103], v[228:231]\n"
"s_waitcnt lgkmcnt(5)\n"
"buffer_load_dwordx4 a[160:163], v212, s[36:39], s20 offen offset:0 sc1\n"
"v_mfma_f32_16x16x32_bf16 v[236:239], a[252:255], a[100:103], v[236:239]\n"
"ds_read_b128 a[240:243], v210 offset:47104\n"
"ds_read_b128 a[244:247], v210 offset:48128\n"
"v_mfma_f32_16x16x32_bf16 v[236:239], a[248:251], a[108:111], v[236:239]\n"
"v_add_f32 v162, v162, v182\n"
"v_add_f32 v163, v163, v183\n"
"v_mfma_f32_16x16x32_bf16 v[232:235], a[248:251], a[104:107], v[232:235]\n"
"v_add_f32 v160, v160, v184\n"
"v_add_f32 v161, v161, v185\n"
"v_mfma_f32_16x16x32_bf16 v[232:235], a[252:255], a[96:99], v[232:235]\n"
"s_waitcnt lgkmcnt(2)\n"
"v_cmp_ne_u32 vcc, s75, v221\n"
"v_add_f32 v162, v162, v186\n"
"v_mfma_f32_16x16x32_bf16 v[240:243], a[224:227], a[96:99], v[240:243]\n"
"ds_read_b128 a[248:251], v210 offset:49152\n"
"ds_read_b128 a[252:255], v210 offset:50176\n"
"v_mfma_f32_16x16x32_bf16 v[240:243], a[228:231], a[104:107], v[240:243]\n"
"buffer_load_dwordx4 a[164:167], v212, s[36:39], s20 offen offset:1024 sc1\n"
"v_mfma_f32_16x16x32_bf16 v[244:247], a[228:231], a[108:111], v[244:247]\n"
"s_cbranch_vccnz Lsb28_%=\n"
"Lsr28_%=:\n"
"s_add_u32 m0, s70, 124928\n"
"v_mfma_f32_16x16x32_bf16 v[244:247], a[224:227], a[100:103], v[244:247]\n"
"s_add_u32 s71, s82, 221184\n"
"v_add_f32 v163, v163, v187\n"
"v_add_f32 v160, v160, v188\n"
"v_mfma_f32_16x16x32_bf16 v[252:255], a[232:235], a[100:103], v[252:255]\n"
"ds_read_b128 a[228:231], v212 offset:2048\n"
"ds_read_b128 a[224:227], v212 offset:3072\n"
"v_mfma_f32_16x16x32_bf16 v[252:255], a[236:239], a[108:111], v[252:255]\n"
"buffer_load_dwordx4 a[168:171], v212, s[36:39], s20 offen offset:2048 sc1\n"
"v_mfma_f32_16x16x32_bf16 v[248:251], a[236:239], a[104:107], v[248:251]\n"
"v_add_f32 v161, v161, v189\n"
"v_add_f32 v162, v162, v190\n"
"v_mfma_f32_16x16x32_bf16 v[248:251], a[232:235], a[96:99], v[248:251]\n"
"s_waitcnt lgkmcnt(4)\n"
"v_add_f32 v163, v163, v191\n"
"v_cvt_pk_bf16_f32 v160, v160, v161\n"
"v_mfma_f32_16x16x32_bf16 v[80:83], a[240:243], a[96:99], v[80:83]\n"
"ds_read_b128 a[236:239], v212 offset:4096\n"
"ds_read_b128 a[232:235], v212 offset:5120\n"
"v_mfma_f32_16x16x32_bf16 v[80:83], a[244:247], a[104:107], v[80:83]\n"
"buffer_load_dwordx4 a[172:175], v212, s[36:39], s20 offen offset:3072 sc1\n"
"v_mfma_f32_16x16x32_bf16 v[96:99], a[244:247], a[108:111], v[96:99]\n"
"s_add_u32 s20, s20, 4096\n"
"v_cvt_pk_bf16_f32 v161, v162, v163\n"
"v_mfma_f32_16x16x32_bf16 v[96:99], a[240:243], a[100:103], v[96:99]\n"
"s_waitcnt lgkmcnt(4)\n"
"ds_write_b64 v215, v[160:161] offset:0\n"
"v_mfma_f32_16x16x32_bf16 v[64:67], a[248:251], a[100:103], v[64:67]\n"
"ds_read_b128 a[244:247], v212 offset:6144\n"
"ds_read_b128 a[240:243], v212 offset:7168\n"
"v_mfma_f32_16x16x32_bf16 v[64:67], a[252:255], a[108:111], v[64:67]\n"
"v_mfma_f32_16x16x32_bf16 v[112:115], a[252:255], a[104:107], v[112:115]\n"
"buffer_load_dwordx4 v212, s[36:39], s71 offen offset:0 sc1 lds\n"
"v_mfma_f32_16x16x32_bf16 v[112:115], a[248:251], a[96:99], v[112:115]\n"
"s_waitcnt lgkmcnt(2)\n"
"ds_write_b32 v214, v219 offset:2016\n"
"v_mfma_f32_16x16x32_bf16 v[84:87], a[228:231], a[96:99], v[84:87]\n"
"ds_read_b128 a[252:255], v212 offset:8192\n"
"ds_read_b128 a[248:251], v212 offset:9216\n"
"v_mfma_f32_16x16x32_bf16 v[84:87], a[224:227], a[104:107], v[84:87]\n"
"v_mfma_f32_16x16x32_bf16 v[100:103], a[224:227], a[108:111], v[100:103]\n"
"buffer_load_dwordx4 v212, s[36:39], s71 offen offset:1024 sc1 lds\n"
"v_mfma_f32_16x16x32_bf16 v[100:103], a[228:231], a[100:103], v[100:103]\n"
"s_add_u32 m0, s70, 133120\n"
"s_add_u32 s71, s82, 229376\n"
"v_mfma_f32_16x16x32_bf16 v[68:71], a[236:239], a[100:103], v[68:71]\n"
"ds_read_b128 a[224:227], v212 offset:10240\n"
"ds_read_b128 a[228:231], v212 offset:11264\n"
"v_mfma_f32_16x16x32_bf16 v[68:71], a[232:235], a[108:111], v[68:71]\n"
"ds_read_b32 v133, v220 offset:2016\n"
"v_mfma_f32_16x16x32_bf16 v[116:119], a[232:235], a[104:107], v[116:119]\n"
"v_mfma_f32_16x16x32_bf16 v[116:119], a[236:239], a[96:99], v[116:119]\n"
"s_waitcnt lgkmcnt(5)\n"
"buffer_load_dwordx4 v212, s[36:39], s71 offen offset:0 sc1 lds\n"
"v_mfma_f32_16x16x32_bf16 v[88:91], a[244:247], a[96:99], v[88:91]\n"
"ds_read_b128 a[232:235], v212 offset:12288\n"
"ds_read_b128 a[236:239], v212 offset:13312\n"
"v_mfma_f32_16x16x32_bf16 v[88:91], a[240:243], a[104:107], v[88:91]\n"
"v_mfma_f32_16x16x32_bf16 v[104:107], a[240:243], a[108:111], v[104:107]\n"
"s_waitcnt lgkmcnt(2)\n"
"v_cmp_ne_u32 s[92:93], s75, v133\n"
"v_mfma_f32_16x16x32_bf16 v[104:107], a[244:247], a[100:103], v[104:107]\n"
"buffer_load_dwordx4 a[176:179], v212, s[36:39], s20 offen offset:0 sc1\n"
"v_mfma_f32_16x16x32_bf16 v[228:231], a[252:255], a[116:119], v[228:231]\n"
"ds_read_b128 a[240:243], v212 offset:14336\n"
"ds_read_b128 a[244:247], v212 offset:15360\n"
"v_mfma_f32_16x16x32_bf16 v[228:231], a[248:251], a[124:127], v[228:231]\n"
"s_cmp_lg_u64 s[92:93], 0\n"
"s_cbranch_scc1 Lsb63_%=\n"
"Lsr63_%=:\n"
"v_mfma_f32_16x16x32_bf16 v[224:227], a[248:251], a[120:123], v[224:227]\n"
"ds_read2st64_b64 v[140:143], v135 offset0:0 offset1:1\n"
"ds_read2_b64 v[144:147], v136 offset0:16 offset1:32\n"
"v_mfma_f32_16x16x32_bf16 v[224:227], a[252:255], a[112:115], v[224:227]\n"
"buffer_load_dwordx4 a[180:183], v212, s[36:39], s20 offen offset:1024 sc1\n"
"v_mfma_f32_16x16x32_bf16 v[232:235], a[224:227], a[112:115], v[232:235]\n"
"ds_read_b128 a[248:251], v212 offset:16384\n"
"ds_read_b128 a[252:255], v212 offset:17408\n"
"v_mfma_f32_16x16x32_bf16 v[232:235], a[228:231], a[120:123], v[232:235]\n"
"ds_read_b64 v[148:149], v137 offset:0\n"
"ds_read_b64 v[150:151], v138 offset:0\n"
"v_mfma_f32_16x16x32_bf16 v[236:239], a[228:231], a[124:127], v[236:239]\n"
"v_mfma_f32_16x16x32_bf16 v[236:239], a[224:227], a[116:119], v[236:239]\n"
"s_waitcnt lgkmcnt(8)\n"
"ds_write_b32 v214, v219 offset:928\n"
"v_mfma_f32_16x16x32_bf16 v[244:247], a[232:235], a[116:119], v[244:247]\n"
"ds_read_b128 a[228:231], v211 offset:2048\n"
"ds_read_b128 a[224:227], v211 offset:3072\n"
"v_mfma_f32_16x16x32_bf16 v[244:247], a[236:239], a[124:127], v[244:247]\n"
"buffer_load_dwordx4 a[184:187], v212, s[36:39], s20 offen offset:2048 sc1\n"
"v_mfma_f32_16x16x32_bf16 v[240:243], a[236:239], a[120:123], v[240:243]\n"
"s_waitcnt lgkmcnt(7)\n"
"v_mfma_f32_16x16x32_bf16 v[4:7], v[144:147], v[32:35], 0\n"
"v_mfma_f32_16x16x32_bf16 v[0:3], v[140:143], v[32:35], 0\n"
"v_mfma_f32_16x16x32_bf16 v[12:15], v[144:147], v[36:39], 0\n"
"v_mfma_f32_16x16x32_bf16 v[8:11], v[140:143], v[36:39], 0\n"
"v_mfma_f32_16x16x32_bf16 v[240:243], a[232:235], a[112:115], v[240:243]\n"
"v_mfma_f32_16x16x32_bf16 v[248:251], a[240:243], a[112:115], v[248:251]\n"
"s_waitcnt lgkmcnt(0)\n"
"ds_read_b128 a[236:239], v211 offset:4096\n"
"ds_read_b128 a[232:235], v211 offset:5120\n"
"v_mfma_f32_16x16x16_bf16 v[4:7], v[150:151], v[48:49], v[4:7]\n"
"v_mfma_f32_16x16x16_bf16 v[0:3], v[148:149], v[48:49], v[0:3]\n"
"v_mfma_f32_16x16x32_bf16 v[248:251], a[244:247], a[120:123], v[248:251]\n"
"buffer_load_dwordx4 a[188:191], v212, s[36:39], s20 offen offset:3072 sc1\n"
"v_mfma_f32_16x16x32_bf16 v[252:255], a[244:247], a[124:127], v[252:255]\n"
"s_add_u32 s20, s20, 4096\n"
"ds_read_b32 v221, v220 offset:928\n"
"v_mfma_f32_16x16x32_bf16 v[20:23], v[144:147], v[40:43], 0\n"
"v_mfma_f32_16x16x32_bf16 v[16:19], v[140:143], v[40:43], 0\n"
"v_mfma_f32_16x16x32_bf16 v[252:255], a[240:243], a[116:119], v[252:255]\n"
"v_mfma_f32_16x16x32_bf16 v[96:99], a[248:251], a[116:119], v[96:99]\n"
"ds_read_b128 a[244:247], v211 offset:6144\n"
"ds_read_b128 a[240:243], v211 offset:7168\n"
"v_mfma_f32_16x16x16_bf16 v[12:15], v[150:151], v[50:51], v[12:15]\n"
"v_mfma_f32_16x16x16_bf16 v[8:11], v[148:149], v[50:51], v[8:11]\n"
"v_add_f32 v5, 0x358637bd, v5\n"
"v_mfma_f32_16x16x32_bf16 v[28:31], v[144:147], v[44:47], 0\n"
"v_rcp_f32 v5, v5\n"
"v_mfma_f32_16x16x32_bf16 v[24:27], v[140:143], v[44:47], 0\n"
"v_mul_f32 v0, v0, v5\n"
"v_mul_f32 v1, v1, v5\n"
"v_mfma_f32_16x16x16_bf16 v[20:23], v[150:151], v[52:53], v[20:23]\n"
"v_mul_f32 v2, v2, v5\n"
"v_mul_f32 v3, v3, v5\n"
"v_mfma_f32_16x16x16_bf16 v[16:19], v[148:149], v[52:53], v[16:19]\n"
"v_mul_f32 v4, v4, v5\n"
"v_mul_f32 v6, v6, v5\n"
"v_mfma_f32_16x16x16_bf16 v[28:31], v[150:151], v[54:55], v[28:31]\n"
"v_cvt_pk_bf16_f32 v0, v0, v1\n"
"v_cvt_pk_bf16_f32 v1, v2, v3\n"
"v_mfma_f32_16x16x16_bf16 v[24:27], v[148:149], v[54:55], v[24:27]\n"
"v_cvt_pk_bf16_f32 v4, v4, v6\n"
"v_add_f32 v13, 0x358637bd, v13\n"
"v_mfma_f32_16x16x32_bf16 v[96:99], a[252:255], a[124:127], v[96:99]\n"
"buffer_load_dwordx4 v212, s[36:39], s71 offen offset:1024 sc1 lds\n"
"v_mfma_f32_16x16x32_bf16 v[80:83], a[252:255], a[120:123], v[80:83]\n"
"s_add_u32 m0, s70, 75776\n"
"s_add_u32 s71, s82, 237568\n"
"v_add_f32 v21, 0x358637bd, v21\n"
"v_mfma_f32_16x16x32_bf16 v[80:83], a[248:251], a[112:115], v[80:83]\n"
"s_waitcnt lgkmcnt(0)\n"
"v_cmp_ne_u32 vcc, s75, v221\n"
"v_mfma_f32_16x16x32_bf16 v[112:115], a[228:231], a[112:115], v[112:115]\n"
"ds_read_b128 a[252:255], v211 offset:8192\n"
"ds_read_b128 a[248:251], v211 offset:9216\n"
"v_mfma_f32_16x16x32_bf16 v[112:115], a[224:227], a[120:123], v[112:115]\n"
"buffer_store_dwordx2 v[0:1], v128, s[36:39], s16 offen offset:0 sc1\n"
"v_mfma_f32_16x16x32_bf16 v[64:67], a[224:227], a[124:127], v[64:67]\n"
"s_cbranch_vccnz Lsb29_%=\n"
"Lsr29_%=:\n"
"v_mfma_f32_16x16x32_bf16 v[64:67], a[228:231], a[116:119], v[64:67]\n"
"v_rcp_f32 v13, v13\n"
"v_mfma_f32_16x16x32_bf16 v[100:103], a[236:239], a[116:119], v[100:103]\n"
"ds_read_b128 a[224:227], v212 offset:18432\n"
"ds_read_b128 a[228:231], v212 offset:19456\n"
"v_mfma_f32_16x16x32_bf16 v[100:103], a[232:235], a[124:127], v[100:103]\n"
"buffer_load_dwordx4 v212, s[36:39], s71 offen offset:0 sc1 lds\n"
"v_mfma_f32_16x16x32_bf16 v[84:87], a[232:235], a[120:123], v[84:87]\n"
"v_rcp_f32 v21, v21\n"
"v_mfma_f32_16x16x32_bf16 v[84:87], a[236:239], a[112:115], v[84:87]\n"
"v_mul_f32 v8, v8, v13\n"
"v_mul_f32 v9, v9, v13\n"
"v_mfma_f32_16x16x32_bf16 v[116:119], a[244:247], a[112:115], v[116:119]\n"
"ds_read_b128 a[232:235], v212 offset:20480\n"
"ds_read_b128 a[236:239], v212 offset:21504\n"
"v_mfma_f32_16x16x32_bf16 v[116:119], a[240:243], a[120:123], v[116:119]\n"
"buffer_load_dwordx4 v212, s[36:39], s71 offen offset:1024 sc1 lds\n"
"v_mfma_f32_16x16x32_bf16 v[68:71], a[240:243], a[124:127], v[68:71]\n"
"s_add_u32 m0, s70, 83968\n"
"s_add_u32 s71, s82, 245760\n"
"v_mul_f32 v10, v10, v13\n"
"v_mfma_f32_16x16x32_bf16 v[68:71], a[244:247], a[116:119], v[68:71]\n"
"s_waitcnt lgkmcnt(4)\n"
"v_mul_f32 v11, v11, v13\n"
"v_mul_f32 v12, v12, v13\n"
"v_mfma_f32_16x16x32_bf16 v[104:107], a[252:255], a[116:119], v[104:107]\n"
"ds_read_b128 a[240:243], v212 offset:22528\n"
"ds_read_b128 a[244:247], v212 offset:23552\n"
"v_mfma_f32_16x16x32_bf16 v[104:107], a[248:251], a[124:127], v[104:107]\n"
"buffer_load_dwordx4 v212, s[36:39], s71 offen offset:0 sc1 lds\n"
"v_mfma_f32_16x16x32_bf16 v[88:91], a[248:251], a[120:123], v[88:91]\n"
"v_mul_f32 v14, v14, v13\n"
"v_cvt_pk_bf16_f32 v8, v8, v9\n"
"v_mfma_f32_16x16x32_bf16 v[88:91], a[252:255], a[112:115], v[88:91]\n"
"s_waitcnt lgkmcnt(4)\n"
"v_cvt_pk_bf16_f32 v9, v10, v11\n"
"v_cvt_pk_bf16_f32 v12, v12, v14\n"
"v_mfma_f32_16x16x32_bf16 v[224:227], a[224:227], a[128:131], v[224:227]\n"
"ds_read_b128 a[248:251], v212 offset:24576\n"
"ds_read_b128 a[252:255], v212 offset:25600\n"
"v_mfma_f32_16x16x32_bf16 v[224:227], a[228:231], a[136:139], v[224:227]\n"
"buffer_store_dwordx2 v[8:9], v128, s[36:39], s16 offen offset:1024 sc1\n"
"v_mfma_f32_16x16x32_bf16 v[228:231], a[228:231], a[140:143], v[228:231]\n"
"v_permlane16_swap_b32 v4, v12\n"
"v_mfma_f32_16x16x32_bf16 v[228:231], a[224:227], a[132:135], v[228:231]\n"
"s_waitcnt lgkmcnt(4)\n"
"buffer_store_dword v4, v218, s[36:39], s18 offen offset:0 sc1\n"
"v_mfma_f32_16x16x32_bf16 v[236:239], a[232:235], a[132:135], v[236:239]\n"
"ds_read_b128 a[228:231], v212 offset:26624\n"
"ds_read_b128 a[224:227], v212 offset:27648\n"
"v_mfma_f32_16x16x32_bf16 v[236:239], a[236:239], a[140:143], v[236:239]\n"
"buffer_load_dwordx4 a[192:195], v212, s[36:39], s20 offen offset:0 sc1\n"
"v_mfma_f32_16x16x32_bf16 v[232:235], a[236:239], a[136:139], v[232:235]\n"
"v_add_f32 v29, 0x358637bd, v29\n"
"v_mfma_f32_16x16x32_bf16 v[232:235], a[232:235], a[128:131], v[232:235]\n"
"s_waitcnt lgkmcnt(4)\n"
"v_rcp_f32 v29, v29\n"
"v_mfma_f32_16x16x32_bf16 v[240:243], a[240:243], a[128:131], v[240:243]\n"
"ds_read_b128 a[236:239], v212 offset:28672\n"
"ds_read_b128 a[232:235], v212 offset:29696\n"
"v_mfma_f32_16x16x32_bf16 v[240:243], a[244:247], a[136:139], v[240:243]\n"
"buffer_load_dwordx4 a[196:199], v212, s[36:39], s20 offen offset:1024 sc1\n"
"v_mfma_f32_16x16x32_bf16 v[244:247], a[244:247], a[140:143], v[244:247]\n"
"v_mul_f32 v16, v16, v21\n"
"v_mul_f32 v17, v17, v21\n"
"v_mfma_f32_16x16x32_bf16 v[244:247], a[240:243], a[132:135], v[244:247]\n"
"s_waitcnt lgkmcnt(4)\n"
"v_mul_f32 v18, v18, v21\n"
"v_mul_f32 v19, v19, v21\n"
"v_mfma_f32_16x16x32_bf16 v[252:255], a[248:251], a[132:135], v[252:255]\n"
"ds_read_b128 a[244:247], v212 offset:30720\n"
"ds_read_b128 a[240:243], v212 offset:31744\n"
"v_mfma_f32_16x16x32_bf16 v[252:255], a[252:255], a[140:143], v[252:255]\n"
"v_mul_f32 v20, v20, v21\n"
"v_mul_f32 v22, v22, v21\n"
"v_mfma_f32_16x16x32_bf16 v[248:251], a[252:255], a[136:139], v[248:251]\n"
"buffer_load_dwordx4 a[200:203], v212, s[36:39], s20 offen offset:2048 sc1\n"
"v_mfma_f32_16x16x32_bf16 v[248:251], a[248:251], a[128:131], v[248:251]\n"
"s_waitcnt lgkmcnt(4)\n"
"v_cvt_pk_bf16_f32 v16, v16, v17\n"
"v_cvt_pk_bf16_f32 v17, v18, v19\n"
"v_mfma_f32_16x16x32_bf16 v[80:83], a[228:231], a[128:131], v[80:83]\n"
"ds_read_b128 a[252:255], v212 offset:32768\n"
"ds_read_b128 a[248:251], v212 offset:33792\n"
"v_mfma_f32_16x16x32_bf16 v[80:83], a[224:227], a[136:139], v[80:83]\n"
"v_and_b32 v129, 896, v212\n"
"v_lshlrev_b32 v129, 5, v129\n"
"v_mfma_f32_16x16x32_bf16 v[96:99], a[224:227], a[140:143], v[96:99]\n"
"buffer_load_dwordx4 a[204:207], v212, s[36:39], s20 offen offset:3072 sc1\n"
"v_mfma_f32_16x16x32_bf16 v[96:99], a[228:231], a[132:135], v[96:99]\n"
"s_waitcnt vmcnt(28) lgkmcnt(4)\n"
"ds_write_b32 v214, v219 offset:960\n"
"v_mfma_f32_16x16x32_bf16 v[64:67], a[236:239], a[132:135], v[64:67]\n"
"s_add_u32 s20, s20, 4096\n"
"ds_read_b128 a[224:227], v212 offset:34816\n"
"ds_read_b128 a[228:231], v212 offset:35840\n"
"v_mfma_f32_16x16x32_bf16 v[64:67], a[232:235], a[140:143], v[64:67]\n"
"buffer_load_dword v129, v129, s[36:39], s28 offen offset:512 sc1\n"
"v_mfma_f32_16x16x32_bf16 v[112:115], a[232:235], a[136:139], v[112:115]\n"
"v_cvt_pk_bf16_f32 v20, v20, v22\n"
"v_mul_f32 v24, v24, v29\n"
"v_mfma_f32_16x16x32_bf16 v[112:115], a[236:239], a[128:131], v[112:115]\n"
"s_waitcnt lgkmcnt(5)\n"
"v_mul_f32 v25, v25, v29\n"
"v_mul_f32 v26, v26, v29\n"
"v_mfma_f32_16x16x32_bf16 v[84:87], a[244:247], a[128:131], v[84:87]\n"
"ds_read_b128 a[232:235], v212 offset:36864\n"
"ds_read_b128 a[236:239], v212 offset:37888\n"
"v_mfma_f32_16x16x32_bf16 v[84:87], a[240:243], a[136:139], v[84:87]\n"
"Lpad9_%=:\n"
"s_cbranch_execz Lret_%=\n"
"buffer_store_dwordx2 v[16:17], v128, s[36:39], s17 offen offset:0 sc1\n"
"v_mfma_f32_16x16x32_bf16 v[100:103], a[240:243], a[140:143], v[100:103]\n"
"ds_read_b32 v221, v220 offset:960\n"
"v_mul_f32 v27, v27, v29\n"
"v_mfma_f32_16x16x32_bf16 v[100:103], a[244:247], a[132:135], v[100:103]\n"
"s_waitcnt lgkmcnt(5)\n"
"v_mul_f32 v28, v28, v29\n"
"v_mul_f32 v30, v30, v29\n"
"v_mfma_f32_16x16x32_bf16 v[68:71], a[252:255], a[132:135], v[68:71]\n"
"ds_read_b128 a[240:243], v212 offset:38912\n"
"ds_read_b128 a[244:247], v212 offset:39936\n"
"v_mfma_f32_16x16x32_bf16 v[68:71], a[248:251], a[140:143], v[68:71]\n"
"buffer_load_dwordx4 v212, s[36:39], s71 offen offset:1024 sc1 lds\n"
"v_mfma_f32_16x16x32_bf16 v[116:119], a[248:251], a[136:139], v[116:119]\n"
"s_add_u32 m0, s70, 92160\n"
"s_add_u32 s71, s82, 253952\n"
"v_cvt_pk_bf16_f32 v24, v24, v25\n"
"v_mfma_f32_16x16x32_bf16 v[116:119], a[252:255], a[128:131], v[116:119]\n"
"s_waitcnt lgkmcnt(2)\n"
"v_cmp_ne_u32 vcc, s75, v221\n"
"v_cvt_pk_bf16_f32 v25, v26, v27\n"
"v_mfma_f32_16x16x32_bf16 v[88:91], a[224:227], a[128:131], v[88:91]\n"
"ds_read_b128 a[248:251], v212 offset:40960\n"
"ds_read_b128 a[252:255], v212 offset:41984\n"
"v_mfma_f32_16x16x32_bf16 v[88:91], a[228:231], a[136:139], v[88:91]\n"
"buffer_store_dwordx2 v[24:25], v128, s[36:39], s17 offen offset:1024 sc1\n"
"v_mfma_f32_16x16x32_bf16 v[104:107], a[228:231], a[140:143], v[104:107]\n"
"s_cbranch_vccnz Lsb30_%=\n"
"Lsr30_%=:\n"
"v_cvt_pk_bf16_f32 v28, v28, v30\n"
"v_mfma_f32_16x16x32_bf16 v[104:107], a[224:227], a[132:135], v[104:107]\n"
"s_nop 0\n"
"v_permlane16_swap_b32 v20, v28\n"
"v_mfma_f32_16x16x32_bf16 v[228:231], a[232:235], a[148:151], v[228:231]\n"
"ds_read_b128 a[228:231], v212 offset:43008\n"
"ds_read_b128 a[224:227], v212 offset:44032\n"
"v_mfma_f32_16x16x32_bf16 v[228:231], a[236:239], a[156:159], v[228:231]\n"
"buffer_load_dwordx4 v212, s[36:39], s71 offen offset:0 sc1 lds\n"
"v_mfma_f32_16x16x32_bf16 v[224:227], a[236:239], a[152:155], v[224:227]\n"
"buffer_store_dword v20, v218, s[36:39], s19 offen offset:0 sc1\n"
"v_mfma_f32_16x16x32_bf16 v[224:227], a[232:235], a[144:147], v[224:227]\n"
"s_waitcnt lgkmcnt(4)\n"
"v_mfma_f32_16x16x32_bf16 v[232:235], a[240:243], a[144:147], v[232:235]\n"
"ds_read_b128 a[236:239], v212 offset:45056\n"
"ds_read_b128 a[232:235], v212 offset:46080\n"
"v_mfma_f32_16x16x32_bf16 v[232:235], a[244:247], a[152:155], v[232:235]\n"
"v_mfma_f32_16x16x32_bf16 v[236:239], a[244:247], a[156:159], v[236:239]\n"
"buffer_load_dwordx4 v212, s[36:39], s71 offen offset:1024 sc1 lds\n"
"v_mfma_f32_16x16x32_bf16 v[236:239], a[240:243], a[148:151], v[236:239]\n"
"s_waitcnt lgkmcnt(4)\n"
"s_add_u32 m0, s70, 2048\n"
"s_add_u32 s71, s82, 262144\n"
"v_mfma_f32_16x16x32_bf16 v[244:247], a[248:251], a[148:151], v[244:247]\n"
"ds_read_b128 a[244:247], v212 offset:47104\n"
"ds_read_b128 a[240:243], v212 offset:48128\n"
"v_mfma_f32_16x16x32_bf16 v[244:247], a[252:255], a[156:159], v[244:247]\n"
"v_mfma_f32_16x16x32_bf16 v[240:243], a[252:255], a[152:155], v[240:243]\n"
"buffer_load_dwordx4 v212, s[36:39], s71 offen offset:0 sc1 lds\n"
"v_mfma_f32_16x16x32_bf16 v[240:243], a[248:251], a[144:147], v[240:243]\n"
"s_waitcnt lgkmcnt(4)\n"
"v_mfma_f32_16x16x32_bf16 v[248:251], a[228:231], a[144:147], v[248:251]\n"
"ds_read_b128 a[252:255], v212 offset:49152\n"
"ds_read_b128 a[248:251], v212 offset:50176\n"
"v_mfma_f32_16x16x32_bf16 v[248:251], a[224:227], a[152:155], v[248:251]\n"
"v_mfma_f32_16x16x32_bf16 v[252:255], a[224:227], a[156:159], v[252:255]\n"
"v_mfma_f32_16x16x32_bf16 v[252:255], a[228:231], a[148:151], v[252:255]\n"
"s_waitcnt lgkmcnt(4)\n"
"buffer_load_dwordx4 v212, s[36:39], s71 offen offset:1024 sc1 lds\n"
"v_mfma_f32_16x16x32_bf16 v[96:99], a[236:239], a[148:151], v[96:99]\n"
"s_add_u32 m0, s70, 10240\n"
"ds_read_b128 a[224:227], v210 offset:2048\n"
"ds_read_b128 a[228:231], v210 offset:3072\n"
"v_mfma_f32_16x16x32_bf16 v[96:99], a[232:235], a[156:159], v[96:99]\n"
"s_add_u32 s71, s82, 270336\n"
"v_mfma_f32_16x16x32_bf16 v[80:83], a[232:235], a[152:155], v[80:83]\n"
"v_mfma_f32_16x16x32_bf16 v[80:83], a[236:239], a[144:147], v[80:83]\n"
"s_waitcnt lgkmcnt(4)\n"
"buffer_load_dwordx4 a[208:211], v212, s[36:39], s20 offen offset:0 sc1\n"
"v_mfma_f32_16x16x32_bf16 v[112:115], a[244:247], a[144:147], v[112:115]\n"
"ds_read_b128 a[232:235], v210 offset:4096\n"
"ds_read_b128 a[236:239], v210 offset:5120\n"
"v_mfma_f32_16x16x32_bf16 v[112:115], a[240:243], a[152:155], v[112:115]\n"
"v_mfma_f32_16x16x32_bf16 v[64:67], a[240:243], a[156:159], v[64:67]\n"
"v_mfma_f32_16x16x32_bf16 v[64:67], a[244:247], a[148:151], v[64:67]\n"
"s_waitcnt lgkmcnt(4)\n"
"v_mfma_f32_16x16x32_bf16 v[100:103], a[252:255], a[148:151], v[100:103]\n"
"ds_read_b128 a[240:243], v210 offset:6144\n"
"ds_read_b128 a[244:247], v210 offset:7168\n"
"v_mfma_f32_16x16x32_bf16 v[100:103], a[248:251], a[156:159], v[100:103]\n"
"buffer_load_dwordx4 a[212:215], v212, s[36:39], s20 offen offset:1024 sc1\n"
"v_mfma_f32_16x16x32_bf16 v[84:87], a[248:251], a[152:155], v[84:87]\n"
"v_mfma_f32_16x16x32_bf16 v[84:87], a[252:255], a[144:147], v[84:87]\n"
"s_waitcnt lgkmcnt(4)\n"
"v_mfma_f32_16x16x32_bf16 v[116:119], a[224:227], a[144:147], v[116:119]\n"
"ds_read_b128 a[248:251], v210 offset:8192\n"
"ds_read_b128 a[252:255], v210 offset:9216\n"
"v_mfma_f32_16x16x32_bf16 v[116:119], a[228:231], a[152:155], v[116:119]\n"
"buffer_load_dwordx4 a[216:219], v212, s[36:39], s20 offen offset:2048 sc1\n"
"v_mfma_f32_16x16x32_bf16 v[68:71], a[228:231], a[156:159], v[68:71]\n"
"v_mfma_f32_16x16x32_bf16 v[68:71], a[224:227], a[148:151], v[68:71]\n"
"s_waitcnt vmcnt(34) lgkmcnt(4)\n"
"ds_write_b32 v214, v219 offset:992\n"
"v_mfma_f32_16x16x32_bf16 v[104:107], a[232:235], a[148:151], v[104:107]\n"
"ds_read_b128 a[228:231], v210 offset:10240\n"
"ds_read_b128 a[224:227], v210 offset:11264\n"
"v_mfma_f32_16x16x32_bf16 v[104:107], a[236:239], a[156:159], v[104:107]\n"
"buffer_load_dwordx4 a[220:223], v212, s[36:39], s20 offen offset:3072 sc1\n"
"v_mfma_f32_16x16x32_bf16 v[88:91], a[236:239], a[152:155], v[88:91]\n"
"s_add_u32 s20, s20, 4096\n"
"v_mfma_f32_16x16x32_bf16 v[88:91], a[232:235], a[144:147], v[88:91]\n"
"s_waitcnt vmcnt(12) lgkmcnt(5)\n"
"v_cmp_ne_u32 s[34:35], s75, v129\n"
"v_mfma_f32_16x16x32_bf16 v[224:227], a[240:243], a[160:163], v[224:227]\n"
"ds_read_b128 a[236:239], v210 offset:12288\n"
"ds_read_b128 a[232:235], v210 offset:13312\n"
"v_mfma_f32_16x16x32_bf16 v[224:227], a[244:247], a[168:171], v[224:227]\n"
"s_cmp_lg_u64 s[34:35], 0\n"
"s_cbranch_scc1 Lslow6_%=\n"
"Lback6_%=:\n"
"v_mfma_f32_16x16x32_bf16 v[228:231], a[244:247], a[172:175], v[228:231]\n"
"ds_read_b32 v221, v220 offset:992\n"
"v_mfma_f32_16x16x32_bf16 v[228:231], a[240:243], a[164:167], v[228:231]\n"
"s_waitcnt lgkmcnt(5)\n"
"buffer_load_dwordx4 v212, s[36:39], s71 offen offset:0 sc1 lds\n"
"v_mfma_f32_16x16x32_bf16 v[236:239], a[248:251], a[164:167], v[236:239]\n"
"ds_read_b128 a[244:247], v210 offset:14336\n"
"ds_read_b128 a[240:243], v210 offset:15360\n"
"v_mfma_f32_16x16x32_bf16 v[236:239], a[252:255], a[172:175], v[236:239]\n"
"v_mfma_f32_16x16x32_bf16 v[232:235], a[252:255], a[168:171], v[232:235]\n"
"v_mfma_f32_16x16x32_bf16 v[232:235], a[248:251], a[160:163], v[232:235]\n"
"s_waitcnt lgkmcnt(2)\n"
"v_cmp_ne_u32 vcc, s75, v221\n"
"v_mfma_f32_16x16x32_bf16 v[240:243], a[228:231], a[160:163], v[240:243]\n"
"ds_read_b128 a[252:255], v210 offset:16384\n"
"ds_read_b128 a[248:251], v210 offset:17408\n"
"v_mfma_f32_16x16x32_bf16 v[240:243], a[224:227], a[168:171], v[240:243]\n"
"buffer_load_dwordx4 v212, s[36:39], s71 offen offset:1024 sc1 lds\n"
"v_mfma_f32_16x16x32_bf16 v[244:247], a[224:227], a[172:175], v[244:247]\n"
"s_cbranch_vccnz Lsb31_%=\n"
"Lsr31_%=:\n"
"s_add_u32 m0, s70, 100352\n"
"v_mfma_f32_16x16x32_bf16 v[244:247], a[228:231], a[164:167], v[244:247]\n"
"s_add_u32 s71, s82, 278528\n"
"v_mfma_f32_16x16x32_bf16 v[252:255], a[236:239], a[164:167], v[252:255]\n"
"ds_read_b128 a[224:227], v210 offset:18432\n"
"ds_read_b128 a[228:231], v210 offset:19456\n"
"v_mfma_f32_16x16x32_bf16 v[252:255], a[232:235], a[172:175], v[252:255]\n"
"buffer_load_dwordx4 v212, s[36:39], s71 offen offset:0 sc1 lds\n"
"v_mfma_f32_16x16x32_bf16 v[248:251], a[232:235], a[168:171], v[248:251]\n"
"s_waitcnt vmcnt(10)\n"
"buffer_store_dword v219, off, s[36:39], s23 offset:384 sc1\n"
"v_mfma_f32_16x16x32_bf16 v[248:251], a[236:239], a[160:163], v[248:251]\n"
"s_waitcnt lgkmcnt(4)\n"
"v_mfma_f32_16x16x32_bf16 v[80:83], a[244:247], a[160:163], v[80:83]\n"
"ds_read_b128 a[232:235], v210 offset:20480\n"
"ds_read_b128 a[236:239], v210 offset:21504\n"
"v_mfma_f32_16x16x32_bf16 v[80:83], a[240:243], a[168:171], v[80:83]\n"
"v_mfma_f32_16x16x32_bf16 v[96:99], a[240:243], a[172:175], v[96:99]\n"
"buffer_load_dwordx4 v212, s[36:39], s71 offen offset:1024 sc1 lds\n"
"v_mfma_f32_16x16x32_bf16 v[96:99], a[244:247], a[164:167], v[96:99]\n"
"s_waitcnt lgkmcnt(4)\n"
"s_add_u32 m0, s70, 18432\n"
"s_add_u32 s71, s82, 286720\n"
"v_mfma_f32_16x16x32_bf16 v[64:67], a[252:255], a[164:167], v[64:67]\n"
"ds_read_b128 a[240:243], v210 offset:22528\n"
"ds_read_b128 a[244:247], v210 offset:23552\n"
"v_mfma_f32_16x16x32_bf16 v[64:67], a[248:251], a[172:175], v[64:67]\n"
"v_mfma_f32_16x16x32_bf16 v[112:115], a[248:251], a[168:171], v[112:115]\n"
"v_mfma_f32_16x16x32_bf16 v[112:115], a[252:255], a[160:163], v[112:115]\n"
"s_waitcnt lgkmcnt(4)\n"
"buffer_load_dwordx4 v212, s[36:39], s71 offen offset:0 sc1 lds\n"
"v_mfma_f32_16x16x32_bf16 v[84:87], a[224:227], a[160:163], v[84:87]\n"
"ds_read_b128 a[248:251], v210 offset:24576\n"
"ds_read_b128 a[252:255], v210 offset:25600\n"
"v_mfma_f32_16x16x32_bf16 v[84:87], a[228:231], a[168:171], v[84:87]\n"
"v_mfma_f32_16x16x32_bf16 v[100:103], a[228:231], a[172:175], v[100:103]\n"
"v_mfma_f32_16x16x32_bf16 v[100:103], a[224:227], a[164:167], v[100:103]\n"
"s_waitcnt lgkmcnt(4)\n"
"buffer_load_dwordx4 v212, s[36:39], s71 offen offset:1024 sc1 lds\n"
"v_mfma_f32_16x16x32_bf16 v[68:71], a[232:235], a[164:167], v[68:71]\n"
"s_add_u32 m0, s70, 26624\n"
"ds_read_b128 a[228:231], v211 offset:10240\n"
"ds_read_b128 a[224:227], v211 offset:11264\n"
"v_mfma_f32_16x16x32_bf16 v[68:71], a[236:239], a[172:175], v[68:71]\n"
"s_add_u32 s71, s82, 294912\n"
"v_mfma_f32_16x16x32_bf16 v[116:119], a[236:239], a[168:171], v[116:119]\n"
"v_mfma_f32_16x16x32_bf16 v[116:119], a[232:235], a[160:163], v[116:119]\n"
"s_waitcnt lgkmcnt(4)\n"
"v_mfma_f32_16x16x32_bf16 v[88:91], a[240:243], a[160:163], v[88:91]\n"
"ds_read_b128 a[236:239], v211 offset:12288\n"
"ds_read_b128 a[232:235], v211 offset:13312\n"
"v_mfma_f32_16x16x32_bf16 v[88:91], a[244:247], a[168:171], v[88:91]\n"
"buffer_load_dwordx4 a[0:3], v212, s[36:39], s20 offen offset:0 sc1\n"
"v_mfma_f32_16x16x32_bf16 v[104:107], a[244:247], a[172:175], v[104:107]\n"
"v_mfma_f32_16x16x32_bf16 v[104:107], a[240:243], a[164:167], v[104:107]\n"
"s_waitcnt lgkmcnt(4)\n"
"v_mfma_f32_16x16x32_bf16 v[228:231], a[248:251], a[180:183], v[228:231]\n"
"ds_read_b128 a[244:247], v211 offset:14336\n"
"ds_read_b128 a[240:243], v211 offset:15360\n"
"v_mfma_f32_16x16x32_bf16 v[228:231], a[252:255], a[188:191], v[228:231]\n"
"buffer_load_dwordx4 a[4:7], v212, s[36:39], s20 offen offset:1024 sc1\n"
"v_mfma_f32_16x16x32_bf16 v[224:227], a[252:255], a[184:187], v[224:227]\n"
"v_mfma_f32_16x16x32_bf16 v[224:227], a[248:251], a[176:179], v[224:227]\n"
"s_waitcnt lgkmcnt(4)\n"
"v_mfma_f32_16x16x32_bf16 v[232:235], a[228:231], a[176:179], v[232:235]\n"
"ds_read_b128 a[252:255], v211 offset:16384\n"
"ds_read_b128 a[248:251], v211 offset:17408\n"
"v_mfma_f32_16x16x32_bf16 v[232:235], a[224:227], a[184:187], v[232:235]\n"
"buffer_load_dwordx4 a[8:11], v212, s[36:39], s20 offen offset:2048 sc1\n"
"v_mfma_f32_16x16x32_bf16 v[236:239], a[224:227], a[188:191], v[236:239]\n"
"v_mfma_f32_16x16x32_bf16 v[236:239], a[228:231], a[180:183], v[236:239]\n"
"s_waitcnt lgkmcnt(4)\n"
"ds_write_b32 v214, v219 offset:1024\n"
"v_mfma_f32_16x16x32_bf16 v[244:247], a[236:239], a[180:183], v[244:247]\n"
"ds_read_b128 a[224:227], v211 offset:18432\n"
"ds_read_b128 a[228:231], v211 offset:19456\n"
"v_mfma_f32_16x16x32_bf16 v[244:247], a[232:235], a[188:191], v[244:247]\n"
"v_mfma_f32_16x16x32_bf16 v[240:243], a[232:235], a[184:187], v[240:243]\n"
"buffer_load_dwordx4 a[12:15], v212, s[36:39], s20 offen offset:3072 sc1\n"
"v_mfma_f32_16x16x32_bf16 v[240:243], a[236:239], a[176:179], v[240:243]\n"
"s_waitcnt lgkmcnt(5)\n"
"s_add_u32 s20, s20, 4096\n"
"v_mfma_f32_16x16x32_bf16 v[248:251], a[244:247], a[176:179], v[248:251]\n"
"ds_read_b128 a[232:235], v211 offset:20480\n"
"ds_read_b128 a[236:239], v211 offset:21504\n"
"v_mfma_f32_16x16x32_bf16 v[248:251], a[240:243], a[184:187], v[248:251]\n"
"v_mfma_f32_16x16x32_bf16 v[252:255], a[240:243], a[188:191], v[252:255]\n"
"ds_read_b32 v221, v220 offset:1024\n"
"v_mfma_f32_16x16x32_bf16 v[252:255], a[244:247], a[180:183], v[252:255]\n"
"s_waitcnt lgkmcnt(5)\n"
"buffer_load_dwordx4 a[16:19], v212, s[36:39], s20 offen offset:0 sc1\n"
"v_mfma_f32_16x16x32_bf16 v[96:99], a[252:255], a[180:183], v[96:99]\n"
"ds_read_b128 a[240:243], v211 offset:22528\n"
"ds_read_b128 a[244:247], v211 offset:23552\n"
"v_mfma_f32_16x16x32_bf16 v[96:99], a[248:251], a[188:191], v[96:99]\n"
"v_mfma_f32_16x16x32_bf16 v[80:83], a[248:251], a[184:187], v[80:83]\n"
"v_mfma_f32_16x16x32_bf16 v[80:83], a[252:255], a[176:179], v[80:83]\n"
"s_waitcnt lgkmcnt(2)\n"
"v_cmp_ne_u32 vcc, s75, v221\n"
"v_mfma_f32_16x16x32_bf16 v[112:115], a[224:227], a[176:179], v[112:115]\n"
"ds_read_b128 a[248:251], v211 offset:24576\n"
"ds_read_b128 a[252:255], v211 offset:25600\n"
"v_mfma_f32_16x16x32_bf16 v[112:115], a[228:231], a[184:187], v[112:115]\n"
"buffer_load_dwordx4 a[20:23], v212, s[36:39], s20 offen offset:1024 sc1\n"
"v_mfma_f32_16x16x32_bf16 v[64:67], a[228:231], a[188:191], v[64:67]\n"
"s_cbranch_vccnz Lsb32_%=\n"
"Lsr32_%=:\n"
"v_mfma_f32_16x16x32_bf16 v[64:67], a[224:227], a[180:183], v[64:67]\n"
"v_mfma_f32_16x16x32_bf16 v[100:103], a[232:235], a[180:183], v[100:103]\n"
"ds_read_b128 v[32:35], v211 offset:26624\n"
"ds_read_b128 v[36:39], v211 offset:27648\n"
"v_mfma_f32_16x16x32_bf16 v[100:103], a[236:239], a[188:191], v[100:103]\n"
"buffer_load_dwordx4 a[24:27], v212, s[36:39], s20 offen offset:2048 sc1\n"
"v_mfma_f32_16x16x32_bf16 v[84:87], a[236:239], a[184:187], v[84:87]\n"
"v_mfma_f32_16x16x32_bf16 v[84:87], a[232:235], a[176:179], v[84:87]\n"
"s_waitcnt lgkmcnt(4)\n"
"v_mfma_f32_16x16x32_bf16 v[116:119], a[240:243], a[176:179], v[116:119]\n"
"ds_read_b128 v[40:43], v211 offset:28672\n"
"ds_read_b128 v[44:47], v211 offset:29696\n"
"v_mfma_f32_16x16x32_bf16 v[116:119], a[244:247], a[184:187], v[116:119]\n"
"buffer_load_dwordx4 a[28:31], v212, s[36:39], s20 offen offset:3072 sc1\n"
"v_mfma_f32_16x16x32_bf16 v[68:71], a[244:247], a[188:191], v[68:71]\n"
"s_add_u32 s20, s20, 4096\n"
"v_mfma_f32_16x16x32_bf16 v[68:71], a[240:243], a[180:183], v[68:71]\n"
"s_waitcnt lgkmcnt(4)\n"
"v_mfma_f32_16x16x32_bf16 v[104:107], a[248:251], a[180:183], v[104:107]\n"
"ds_read_b128 v[48:51], v211 offset:30720\n"
"ds_read_b128 v[52:55], v211 offset:31744\n"
"v_mfma_f32_16x16x32_bf16 v[104:107], a[252:255], a[188:191], v[104:107]\n"
"v_mfma_f32_16x16x32_bf16 v[88:91], a[252:255], a[184:187], v[88:91]\n"
"buffer_load_dwordx4 v212, s[36:39], s71 offen offset:0 sc1 lds\n"
"v_mfma_f32_16x16x32_bf16 v[88:91], a[248:251], a[176:179], v[88:91]\n"
"s_waitcnt lgkmcnt(4)\n"
"v_mfma_f32_16x16x32_bf16 v[224:227], v[32:35], a[192:195], v[224:227]\n"
"ds_read_b128 v[56:59], v211 offset:32768\n"
"ds_read_b128 v[60:63], v211 offset:33792\n"
"v_mfma_f32_16x16x32_bf16 v[224:227], v[36:39], a[200:203], v[224:227]\n"
"v_mfma_f32_16x16x32_bf16 v[228:231], v[36:39], a[204:207], v[228:231]\n"
"buffer_load_dwordx4 v212, s[36:39], s71 offen offset:1024 sc1 lds\n"
"v_mfma_f32_16x16x32_bf16 v[228:231], v[32:35], a[196:199], v[228:231]\n"
"s_waitcnt lgkmcnt(4)\n"
"s_add_u32 m0, s70, 34816\n"
"s_add_u32 s71, s82, 303104\n"
"v_mfma_f32_16x16x32_bf16 v[236:239], v[40:43], a[196:199], v[236:239]\n"
"ds_read_b128 a[228:231], v211 offset:34816\n"
"ds_read_b128 a[224:227], v211 offset:35840\n"
"v_mfma_f32_16x16x32_bf16 v[236:239], v[44:47], a[204:207], v[236:239]\n"
"v_mfma_f32_16x16x32_bf16 v[232:235], v[44:47], a[200:203], v[232:235]\n"
"v_mfma_f32_16x16x32_bf16 v[232:235], v[40:43], a[192:195], v[232:235]\n"
"s_waitcnt lgkmcnt(4)\n"
"buffer_load_dwordx4 v212, s[36:39], s71 offen offset:0 sc1 lds\n"
"v_mfma_f32_16x16x32_bf16 v[240:243], v[48:51], a[192:195], v[240:243]\n"
"ds_read_b128 a[236:239], v211 offset:36864\n"
"ds_read_b128 a[232:235], v211 offset:37888\n"
"v_mfma_f32_16x16x32_bf16 v[240:243], v[52:55], a[200:203], v[240:243]\n"
"v_mfma_f32_16x16x32_bf16 v[244:247], v[52:55], a[204:207], v[244:247]\n"
"v_mfma_f32_16x16x32_bf16 v[244:247], v[48:51], a[196:199], v[244:247]\n"
"s_waitcnt lgkmcnt(4)\n"
"buffer_load_dwordx4 v212, s[36:39], s71 offen offset:1024 sc1 lds\n"
"v_mfma_f32_16x16x32_bf16 v[252:255], v[56:59], a[196:199], v[252:255]\n"
"s_add_u32 m0, s70, 43008\n"
"ds_read_b128 a[244:247], v211 offset:38912\n"
"ds_read_b128 a[240:243], v211 offset:39936\n"
"v_mfma_f32_16x16x32_bf16 v[252:255], v[60:63], a[204:207], v[252:255]\n"
"s_add_u32 s71, s82, 311296\n"
"v_mfma_f32_16x16x32_bf16 v[248:251], v[60:63], a[200:203], v[248:251]\n"
"v_mfma_f32_16x16x32_bf16 v[248:251], v[56:59], a[192:195], v[248:251]\n"
"s_waitcnt lgkmcnt(4)\n"
"buffer_load_dwordx4 v212, s[36:39], s71 offen offset:0 sc1 lds\n"
"v_mfma_f32_16x16x32_bf16 v[80:83], a[228:231], a[192:195], v[80:83]\n"
"ds_read_b128 a[252:255], v211 offset:40960\n"
"ds_read_b128 a[248:251], v211 offset:41984\n"
"v_mfma_f32_16x16x32_bf16 v[80:83], a[224:227], a[200:203], v[80:83]\n"
"v_mfma_f32_16x16x32_bf16 v[96:99], a[224:227], a[204:207], v[96:99]\n"
"v_mfma_f32_16x16x32_bf16 v[96:99], a[228:231], a[196:199], v[96:99]\n"
"s_waitcnt vmcnt(24) lgkmcnt(4)\n"
"ds_write_b32 v214, v219 offset:1056\n"
"v_mfma_f32_16x16x32_bf16 v[64:67], a[236:239], a[196:199], v[64:67]\n"
"ds_read_b128 v[36:39], v210 offset:26624\n"
"ds_read_b128 v[32:35], v210 offset:27648\n"
"v_mfma_f32_16x16x32_bf16 v[64:67], a[232:235], a[204:207], v[64:67]\n"
"buffer_load_dwordx4 v212, s[36:39], s71 offen offset:1024 sc1 lds\n"
"v_mfma_f32_16x16x32_bf16 v[112:115], a[232:235], a[200:203], v[112:115]\n"
"s_add_u32 m0, s70, 51200\n"
"s_add_u32 s71, s82, 319488\n"
"v_lshrrev_b32 v135, 2, v212\n"
"v_mfma_f32_16x16x32_bf16 v[112:115], a[236:239], a[192:195], v[112:115]\n"
"s_waitcnt lgkmcnt(5)\n"
"s_lshl3_add_u32 s66, s63, s64\n"
"s_lshl4_add_u32 s66, s66, s61\n"
"v_mfma_f32_16x16x32_bf16 v[84:87], a[244:247], a[192:195], v[84:87]\n"
"s_xor_b32 s67, s62, 0\n"
"ds_read_b128 v[44:47], v210 offset:28672\n"
"ds_read_b128 v[40:43], v210 offset:29696\n"
"v_mfma_f32_16x16x32_bf16 v[84:87], a[240:243], a[200:203], v[84:87]\n"
"buffer_load_dwordx4 v212, s[36:39], s71 offen offset:0 sc1 lds\n"
"v_mfma_f32_16x16x32_bf16 v[100:103], a[240:243], a[204:207], v[100:103]\n"
"s_lshl2_add_u32 s67, s67, s66\n"
"ds_read_b32 v221, v220 offset:1056\n"
"s_mul_i32 s68, s67, 7168\n"
"v_mfma_f32_16x16x32_bf16 v[100:103], a[244:247], a[196:199], v[100:103]\n"
"s_waitcnt lgkmcnt(5)\n"
"s_add_u32 s83, s68, 35782656\n"
"s_xor_b32 s67, s62, 1\n"
"v_mfma_f32_16x16x32_bf16 v[68:71], a[252:255], a[196:199], v[68:71]\n"
"s_lshl2_add_u32 s67, s67, s66\n"
"ds_read_b128 v[52:55], v210 offset:30720\n"
"ds_read_b128 v[48:51], v210 offset:31744\n"
"v_mfma_f32_16x16x32_bf16 v[68:71], a[248:251], a[204:207], v[68:71]\n"
"buffer_load_dwordx4 v212, s[36:39], s71 offen offset:1024 sc1 lds\n"
"v_mfma_f32_16x16x32_bf16 v[116:119], a[248:251], a[200:203], v[116:119]\n"
"s_add_u32 m0, s70, 59392\n"
"s_add_u32 s71, s82, 327680\n"
"s_mul_i32 s68, s67, 7168\n"
"v_mfma_f32_16x16x32_bf16 v[116:119], a[252:255], a[192:195], v[116:119]\n"
"s_waitcnt lgkmcnt(2)\n"
"v_cmp_ne_u32 vcc, s75, v221\n"
"s_add_u32 s8, s68, 35782656\n"
"v_mfma_f32_16x16x32_bf16 v[88:91], v[36:39], a[192:195], v[88:91]\n"
"s_xor_b32 s67, s62, 2\n"
"ds_read_b128 v[60:63], v210 offset:32768\n"
"ds_read_b128 v[56:59], v210 offset:33792\n"
"v_mfma_f32_16x16x32_bf16 v[88:91], v[32:35], a[200:203], v[88:91]\n"
"s_lshl2_add_u32 s67, s67, s66\n"
"s_mul_i32 s68, s67, 7168\n"
"s_add_u32 s9, s68, 35784960\n"
"v_mfma_f32_16x16x32_bf16 v[104:107], v[32:35], a[204:207], v[104:107]\n"
"s_cbranch_vccnz Lsb33_%=\n"
"Lsr33_%=:\n"
"s_xor_b32 s67, s62, 3\n"
"v_mfma_f32_16x16x32_bf16 v[104:107], v[36:39], a[196:199], v[104:107]\n"
"s_waitcnt vmcnt(25)\n"
"buffer_load_dwordx4 a[32:35], v212, s[36:39], s20 offen offset:0 sc1\n"
"v_mfma_f32_16x16x32_bf16 v[228:231], v[44:47], a[212:215], v[228:231]\n"
"s_waitcnt vmcnt(24)\n"
"ds_read_b128 a[224:227], v210 offset:34816\n"
"ds_read_b128 a[228:231], v210 offset:35840\n"
"v_mfma_f32_16x16x32_bf16 v[228:231], v[40:43], a[220:223], v[228:231]\n"
"s_lshl2_add_u32 s67, s67, s66\n"
"s_mul_i32 s68, s67, 7168\n"
"s_add_u32 s78, s68, 35787264\n"
"v_mfma_f32_16x16x32_bf16 v[224:227], v[40:43], a[216:219], v[224:227]\n"
"s_lshr_b32 s67, s64, 2\n"
"s_lshl1_add_u32 s66, s63, s67\n"
"s_lshl2_add_u32 s66, s66, s61\n"
"v_mfma_f32_16x16x32_bf16 v[224:227], v[44:47], a[208:211], v[224:227]\n"
"s_waitcnt lgkmcnt(4)\n"
"buffer_load_dwordx4 a[36:39], v212, s[36:39], s20 offen offset:1024 sc1\n"
"v_mfma_f32_16x16x32_bf16 v[232:235], v[52:55], a[208:211], v[232:235]\n"
"s_lshr_b32 s67, s62, 1\n"
"ds_read_b128 a[232:235], v210 offset:36864\n"
"ds_read_b128 a[236:239], v210 offset:37888\n"
"v_mfma_f32_16x16x32_bf16 v[232:235], v[48:51], a[216:219], v[232:235]\n"
"s_lshl1_add_u32 s66, s66, s67\n"
"s_mul_i32 s84, s66, 18432\n"
"s_and_b32 s66, s64, 3\n"
"v_mfma_f32_16x16x32_bf16 v[236:239], v[48:51], a[220:223], v[236:239]\n"
"v_lshrrev_b32 v150, 4, v212\n"
"v_med3_u32 v150, v150, 1, 3\n"
"v_mfma_f32_16x16x32_bf16 v[236:239], v[52:55], a[212:215], v[236:239]\n"
"s_waitcnt lgkmcnt(4)\n"
"buffer_load_dwordx4 a[40:43], v212, s[36:39], s20 offen offset:2048 sc1\n"
"v_mfma_f32_16x16x32_bf16 v[244:247], v[60:63], a[212:215], v[244:247]\n"
"ds_read_b128 a[240:243], v210 offset:38912\n"
"ds_read_b128 a[244:247], v210 offset:39936\n"
"v_mfma_f32_16x16x32_bf16 v[244:247], v[56:59], a[220:223], v[244:247]\n"
"v_and_b32 v134, 1, v150\n"
"v_xor_b32 v150, s62, v150\n"
"v_mfma_f32_16x16x32_bf16 v[240:243], v[56:59], a[216:219], v[240:243]\n"
"v_lshlrev_b32 v150, 18, v150\n"
"v_lshl_add_u32 v150, v134, 7, v150\n"
"v_mfma_f32_16x16x32_bf16 v[240:243], v[60:63], a[208:211], v[240:243]\n"
"s_waitcnt lgkmcnt(4)\n"
"v_lshrrev_b32 v134, 8, v212\n"
"s_and_b32 s67, s62, 1\n"
"v_mfma_f32_16x16x32_bf16 v[248:251], a[224:227], a[208:211], v[248:251]\n"
"ds_read_b128 a[248:251], v210 offset:40960\n"
"ds_read_b128 a[252:255], v210 offset:41984\n"
"v_mfma_f32_16x16x32_bf16 v[248:251], a[228:231], a[216:219], v[248:251]\n"
"buffer_load_dwordx4 a[44:47], v212, s[36:39], s20 offen offset:3072 sc1\n"
"v_mfma_f32_16x16x32_bf16 v[252:255], a[228:231], a[220:223], v[252:255]\n"
"s_add_u32 s20, s20, 4096\n"
"v_bfe_u32 v152, v212, 7, 1\n"
"v_xor_b32 v152, s67, v152\n"
"v_mfma_f32_16x16x32_bf16 v[252:255], a[224:227], a[212:215], v[252:255]\n"
"s_waitcnt lgkmcnt(4)\n"
"v_lshl_add_u32 v151, v134, 5, v220\n"
"v_lshlrev_b32 v151, 1, v151\n"
"v_mfma_f32_16x16x32_bf16 v[96:99], a[232:235], a[212:215], v[96:99]\n"
"s_mul_i32 s87, s61, 5120\n"
"ds_read_b128 v[32:35], v210 offset:43008\n"
"ds_read_b128 v[36:39], v210 offset:44032\n"
"v_mfma_f32_16x16x32_bf16 v[96:99], a[236:239], a[220:223], v[96:99]\n"
"buffer_load_dwordx4 v212, s[36:39], s71 offen offset:0 sc1 lds\n"
"v_mfma_f32_16x16x32_bf16 v[80:83], a[236:239], a[216:219], v[80:83]\n"
"s_add_u32 s87, s87, 141312\n"
"s_mul_i32 s86, s67, 9216\n"
"s_lshl_b32 s68, s66, 5\n"
"v_mfma_f32_16x16x32_bf16 v[80:83], a[232:235], a[208:211], v[80:83]\n"
"s_waitcnt lgkmcnt(4)\n"
"s_add_u32 s86, s86, s68\n"
"s_add_u32 s86, s86, s84\n"
"v_mfma_f32_16x16x32_bf16 v[112:115], a[240:243], a[208:211], v[112:115]\n"
"s_add_u32 s86, s86, 1024\n"
"ds_read_b128 v[40:43], v210 offset:45056\n"
"ds_read_b128 v[44:47], v210 offset:46080\n"
"v_mfma_f32_16x16x32_bf16 v[112:115], a[244:247], a[216:219], v[112:115]\n"
"buffer_load_dwordx4 v212, s[36:39], s71 offen offset:1024 sc1 lds\n"
"v_mfma_f32_16x16x32_bf16 v[64:67], a[244:247], a[220:223], v[64:67]\n"
"s_add_u32 m0, s70, 67584\n"
"s_add_u32 s71, s82, 335872\n"
"v_and_b32 v129, 896, v212\n"
"v_mfma_f32_16x16x32_bf16 v[64:67], a[240:243], a[212:215], v[64:67]\n"
"s_waitcnt lgkmcnt(4)\n"
"v_lshlrev_b32 v129, 5, v129\n"
"s_lshl1_add_u32 s67, s66, s67\n"
"v_mfma_f32_16x16x32_bf16 v[100:103], a[248:251], a[212:215], v[100:103]\n"
"s_lshl_b32 s67, s67, 7\n"
"ds_read_b128 v[48:51], v210 offset:47104\n"
"ds_read_b128 v[52:55], v210 offset:48128\n"
"v_mfma_f32_16x16x32_bf16 v[100:103], a[252:255], a[220:223], v[100:103]\n"
"buffer_load_dword v129, v129, s[36:39], s28 offen offset:384 sc1\n"
"v_mfma_f32_16x16x32_bf16 v[84:87], a[252:255], a[216:219], v[84:87]\n"
"s_add_u32 s84, s84, s67\n"
"s_add_u32 s85, s84, 9216\n"
"v_lshrrev_b32 v132, 3, v212\n"
"v_mfma_f32_16x16x32_bf16 v[84:87], a[248:251], a[208:211], v[84:87]\n"
"s_waitcnt lgkmcnt(4)\n"
"v_xor_b32 v132, v132, v212\n"
"v_and_b32 v132, 112, v132\n"
"v_mfma_f32_16x16x32_bf16 v[116:119], v[32:35], a[208:211], v[116:119]\n"
"ds_read_b128 v[56:59], v210 offset:49152\n"
"ds_read_b128 v[60:63], v210 offset:50176\n"
"v_mfma_f32_16x16x32_bf16 v[116:119], v[36:39], a[216:219], v[116:119]\n"
"v_lshrrev_b32 v133, 7, v212\n"
"v_mul_u32_u24 v133, 1152, v133\n"
"v_mfma_f32_16x16x32_bf16 v[68:71], v[36:39], a[220:223], v[68:71]\n"
"buffer_load_dwordx4 v212, s[36:39], s71 offen offset:0 sc1 lds\n"
"v_mfma_f32_16x16x32_bf16 v[68:71], v[32:35], a[212:215], v[68:71]\n"
"s_waitcnt vmcnt(24) lgkmcnt(4)\n"
"ds_write_b32 v214, v219 offset:1088\n"
"v_mfma_f32_16x16x32_bf16 v[104:107], v[40:43], a[212:215], v[104:107]\n"
"ds_read_b128 a[228:231], v212 offset:2048\n"
"ds_read_b128 a[224:227], v212 offset:3072\n"
"v_mfma_f32_16x16x32_bf16 v[104:107], v[44:47], a[220:223], v[104:107]\n"
"v_add_u32 v132, v132, v133\n"
"v_lshrrev_b32 v133, 5, v212\n"
"v_mfma_f32_16x16x32_bf16 v[88:91], v[44:47], a[216:219], v[88:91]\n"
"v_mul_u32_u24 v133, 1152, v133\n"
"v_and_b32 v156, 16, v212\n"
"v_mfma_f32_16x16x32_bf16 v[88:91], v[40:43], a[208:211], v[88:91]\n"
"s_waitcnt vmcnt(23) lgkmcnt(5)\n"
"buffer_load_dwordx4 v212, s[36:39], s71 offen offset:1024 sc1 lds\n"
"v_mfma_f32_16x16x32_bf16 v[224:227], v[48:51], a[0:3], v[224:227]\n"
"s_waitcnt vmcnt(22)\n"
"ds_read_b128 a[236:239], v212 offset:4096\n"
"ds_read_b128 a[232:235], v212 offset:5120\n"
"v_mfma_f32_16x16x32_bf16 v[224:227], v[52:55], a[8:11], v[224:227]\n"
"s_waitcnt vmcnt(21)\n"
"s_add_u32 m0, s70, 108544\n"
"s_add_u32 s71, s82, 344064\n"
"v_mfma_f32_16x16x32_bf16 v[228:231], v[52:55], a[12:15], v[228:231]\n"
"ds_read_b32 v221, v220 offset:1088\n"
"v_add_u32 v133, v133, v156\n"
"v_mfma_f32_16x16x32_bf16 v[228:231], v[48:51], a[4:7], v[228:231]\n"
"s_waitcnt lgkmcnt(5)\n"
"buffer_load_dwordx4 v212, s[36:39], s71 offen offset:0 sc1 lds\n"
"v_mfma_f32_16x16x32_bf16 v[236:239], v[56:59], a[4:7], v[236:239]\n"
"ds_read_b128 a[244:247], v212 offset:6144\n"
"ds_read_b128 a[240:243], v212 offset:7168\n"
"v_mfma_f32_16x16x32_bf16 v[236:239], v[60:63], a[12:15], v[236:239]\n"
"v_lshl_add_u32 v133, v134, 28, v133\n"
"v_lshlrev_b32 v156, 4, v134\n"
"v_mfma_f32_16x16x32_bf16 v[232:235], v[60:63], a[8:11], v[232:235]\n"
"v_xor_b32 v156, v156, v212\n"
"v_and_b32 v156, 112, v156\n"
"v_mfma_f32_16x16x32_bf16 v[232:235], v[56:59], a[0:3], v[232:235]\n"
"s_waitcnt lgkmcnt(2)\n"
"v_cmp_ne_u32 vcc, s75, v221\n"
"v_bfe_u32 v157, v212, 4, 4\n"
"v_mfma_f32_16x16x32_bf16 v[240:243], a[228:231], a[0:3], v[240:243]\n"
"ds_read_b128 a[252:255], v212 offset:8192\n"
"ds_read_b128 a[248:251], v212 offset:9216\n"
"v_mfma_f32_16x16x32_bf16 v[240:243], a[224:227], a[8:11], v[240:243]\n"
"buffer_load_dwordx4 v212, s[36:39], s71 offen offset:1024 sc1 lds\n"
"v_mfma_f32_16x16x32_bf16 v[244:247], a[224:227], a[12:15], v[244:247]\n"
"s_cbranch_vccnz Lsb34_%=\n"
"Lsr34_%=:\n"
"s_add_u32 m0, s70, 116736\n"
"v_mfma_f32_16x16x32_bf16 v[244:247], a[228:231], a[4:7], v[244:247]\n"
"s_add_u32 s71, s82, 352256\n"
"v_lshl_add_u32 v156, v157, 7, v156\n"
"v_add_u32 v156, s87, v156\n"
"v_mfma_f32_16x16x32_bf16 v[252:255], a[236:239], a[4:7], v[252:255]\n"
"ds_read_b128 v[36:39], v212 offset:10240\n"
"ds_read_b128 v[32:35], v212 offset:11264\n"
"v_mfma_f32_16x16x32_bf16 v[252:255], a[232:235], a[12:15], v[252:255]\n"
"buffer_load_dwordx4 v212, s[36:39], s71 offen offset:0 sc1 lds\n"
"v_mfma_f32_16x16x32_bf16 v[248:251], a[232:235], a[8:11], v[248:251]\n"
"v_xor_b32 v157, 64, v156\n"
"v_add_u32 v158, v220, v134\n"
"v_mfma_f32_16x16x32_bf16 v[248:251], a[236:239], a[0:3], v[248:251]\n"
"s_waitcnt lgkmcnt(4)\n"
"v_lshl_add_u32 v158, v152, 7, v158\n"
"v_lshl_add_u32 v158, v158, 3, s87\n"
"v_mfma_f32_16x16x32_bf16 v[80:83], a[244:247], a[0:3], v[80:83]\n"
"ds_read_b128 v[44:47], v212 offset:12288\n"
"ds_read_b128 v[40:43], v212 offset:13312\n"
"v_mfma_f32_16x16x32_bf16 v[80:83], a[240:243], a[8:11], v[80:83]\n"
"buffer_load_dwordx4 v212, s[36:39], s71 offen offset:1024 sc1 lds\n"
"v_mfma_f32_16x16x32_bf16 v[96:99], a[240:243], a[12:15], v[96:99]\n"
"s_add_u32 m0, s70, 124928\n"
"s_add_u32 s71, s82, 360448\n"
"v_add_u32 v159, s87, v212\n"
"v_mfma_f32_16x16x32_bf16 v[96:99], a[244:247], a[4:7], v[96:99]\n"
"s_waitcnt lgkmcnt(4)\n"
"v_xor_b32 v153, 1, v152\n"
"v_lshl_add_u32 v153, v153, 30, v151\n"
"v_mfma_f32_16x16x32_bf16 v[64:67], a[252:255], a[4:7], v[64:67]\n"
"s_sub_u32 s68, s78, s9\n"
"ds_read_b128 v[52:55], v212 offset:14336\n"
"ds_read_b128 v[48:51], v212 offset:15360\n"
"v_mfma_f32_16x16x32_bf16 v[64:67], a[248:251], a[12:15], v[64:67]\n"
"v_mul_lo_u32 v134, s68, v152\n"
"v_add3_u32 v134, v134, v151, s9\n"
"v_mfma_f32_16x16x32_bf16 v[112:115], a[248:251], a[8:11], v[112:115]\n"
"buffer_load_dwordx4 v212, s[36:39], s71 offen offset:0 sc1 lds\n"
"v_mfma_f32_16x16x32_bf16 v[112:115], a[252:255], a[0:3], v[112:115]\n"
"s_waitcnt lgkmcnt(4)\n"
"v_lshl_add_u32 v151, v152, 30, v151\n"
"v_mov_b32 v152, v134\n"
"v_mfma_f32_16x16x32_bf16 v[84:87], v[36:39], a[0:3], v[84:87]\n"
"ds_read_b128 v[60:63], v212 offset:16384\n"
"ds_read_b128 v[56:59], v212 offset:17408\n"
"v_mfma_f32_16x16x32_bf16 v[84:87], v[32:35], a[8:11], v[84:87]\n"
"Lpad10_%=:\n"
"s_cbranch_execz Lret_%=\n"
"v_mfma_f32_16x16x32_bf16 v[100:103], v[32:35], a[12:15], v[100:103]\n"
"buffer_load_dwordx4 v212, s[36:39], s71 offen offset:1024 sc1 lds\n"
"v_mfma_f32_16x16x32_bf16 v[100:103], v[36:39], a[4:7], v[100:103]\n"
"s_waitcnt lgkmcnt(4)\n"
"s_add_u32 m0, s70, 133120\n"
"s_add_u32 s71, s82, 368640\n"
"v_mfma_f32_16x16x32_bf16 v[68:71], v[44:47], a[4:7], v[68:71]\n"
"ds_read_b128 a[224:227], v211 offset:2048\n"
"ds_read_b128 a[228:231], v211 offset:3072\n"
"v_mfma_f32_16x16x32_bf16 v[68:71], v[40:43], a[12:15], v[68:71]\n"
"v_mfma_f32_16x16x32_bf16 v[116:119], v[40:43], a[8:11], v[116:119]\n"
"v_mfma_f32_16x16x32_bf16 v[116:119], v[44:47], a[0:3], v[116:119]\n"
"s_waitcnt lgkmcnt(4)\n"
"buffer_load_dwordx4 a[48:51], v212, s[36:39], s20 offen offset:0 sc1\n"
"v_mfma_f32_16x16x32_bf16 v[88:91], v[52:55], a[0:3], v[88:91]\n"
"ds_read_b128 a[232:235], v211 offset:4096\n"
"ds_read_b128 a[236:239], v211 offset:5120\n"
"v_mfma_f32_16x16x32_bf16 v[88:91], v[48:51], a[8:11], v[88:91]\n"
"v_mfma_f32_16x16x32_bf16 v[104:107], v[48:51], a[12:15], v[104:107]\n"
"v_mfma_f32_16x16x32_bf16 v[104:107], v[52:55], a[4:7], v[104:107]\n"
"s_waitcnt vmcnt(26) lgkmcnt(4)\n"
"buffer_load_dwordx4 a[52:55], v212, s[36:39], s20 offen offset:1024 sc1\n"
"v_mfma_f32_16x16x32_bf16 v[228:231], v[60:63], a[20:23], v[228:231]\n"
"s_waitcnt vmcnt(25)\n"
"ds_read_b128 a[240:243], v211 offset:6144\n"
"ds_read_b128 a[244:247], v211 offset:7168\n"
"v_mfma_f32_16x16x32_bf16 v[228:231], v[56:59], a[28:31], v[228:231]\n"
"v_mfma_f32_16x16x32_bf16 v[224:227], v[56:59], a[24:27], v[224:227]\n"
"v_mfma_f32_16x16x32_bf16 v[224:227], v[60:63], a[16:19], v[224:227]\n"
"s_waitcnt lgkmcnt(4)\n"
"buffer_load_dwordx4 a[56:59], v212, s[36:39], s20 offen offset:2048 sc1\n"
"v_mfma_f32_16x16x32_bf16 v[232:235], a[224:227], a[16:19], v[232:235]\n"
"ds_read_b128 a[248:251], v211 offset:8192\n"
"ds_read_b128 a[252:255], v211 offset:9216\n"
"v_mfma_f32_16x16x32_bf16 v[232:235], a[228:231], a[24:27], v[232:235]\n"
"s_waitcnt vmcnt(11)\n"
"v_cmp_ne_u32 s[34:35], s75, v129\n"
"v_mfma_f32_16x16x32_bf16 v[236:239], a[228:231], a[28:31], v[236:239]\n"
"v_mfma_f32_16x16x32_bf16 v[236:239], a[224:227], a[20:23], v[236:239]\n"
"s_waitcnt lgkmcnt(4)\n"
"ds_write_b32 v214, v219 offset:1120\n"
"v_mfma_f32_16x16x32_bf16 v[244:247], a[232:235], a[20:23], v[244:247]\n"
"ds_read_b128 v[32:35], v212 offset:18432\n"
"ds_read_b128 v[36:39], v212 offset:19456\n"
"v_mfma_f32_16x16x32_bf16 v[244:247], a[236:239], a[28:31], v[244:247]\n"
"buffer_load_dwordx4 a[60:63], v212, s[36:39], s20 offen offset:3072 sc1\n"
"v_mfma_f32_16x16x32_bf16 v[240:243], a[236:239], a[24:27], v[240:243]\n"
"s_add_u32 s20, s20, 4096\n"
"v_mfma_f32_16x16x32_bf16 v[240:243], a[232:235], a[16:19], v[240:243]\n"
"s_waitcnt lgkmcnt(5)\n"
"v_mfma_f32_16x16x32_bf16 v[248:251], a[240:243], a[16:19], v[248:251]\n"
"ds_read_b128 v[40:43], v212 offset:20480\n"
"ds_read_b128 v[44:47], v212 offset:21504\n"
"v_mfma_f32_16x16x32_bf16 v[248:251], a[244:247], a[24:27], v[248:251]\n"
"buffer_load_dwordx4 v212, s[36:39], s71 offen offset:0 sc1 lds\n"
"v_mfma_f32_16x16x32_bf16 v[252:255], a[244:247], a[28:31], v[252:255]\n"
"ds_read_b32 v221, v220 offset:1120\n"
"v_mfma_f32_16x16x32_bf16 v[252:255], a[240:243], a[20:23], v[252:255]\n"
"s_waitcnt lgkmcnt(5)\n"
"v_mfma_f32_16x16x32_bf16 v[96:99], a[248:251], a[20:23], v[96:99]\n"
"ds_read_b128 v[48:51], v212 offset:22528\n"
"ds_read_b128 v[52:55], v212 offset:23552\n"
"v_mfma_f32_16x16x32_bf16 v[96:99], a[252:255], a[28:31], v[96:99]\n"
"buffer_load_dwordx4 v212, s[36:39], s71 offen offset:1024 sc1 lds\n"
"v_mfma_f32_16x16x32_bf16 v[80:83], a[252:255], a[24:27], v[80:83]\n"
"s_add_u32 m0, s70, 75776\n"
"s_add_u32 s71, s82, 376832\n"
"v_mfma_f32_16x16x32_bf16 v[80:83], a[248:251], a[16:19], v[80:83]\n"
"s_waitcnt lgkmcnt(2)\n"
"v_cmp_ne_u32 vcc, s75, v221\n"
"v_mfma_f32_16x16x32_bf16 v[112:115], v[32:35], a[16:19], v[112:115]\n"
"ds_read_b128 v[56:59], v212 offset:24576\n"
"ds_read_b128 v[60:63], v212 offset:25600\n"
"v_mfma_f32_16x16x32_bf16 v[112:115], v[36:39], a[24:27], v[112:115]\n"
"s_cmp_lg_u64 s[34:35], 0\n"
"s_cbranch_scc1 Lslow7_%=\n"
"Lback7_%=:\n"
"v_mfma_f32_16x16x32_bf16 v[64:67], v[36:39], a[28:31], v[64:67]\n"
"s_cbranch_vccnz Lsb35_%=\n"
"Lsr35_%=:\n"
"v_mfma_f32_16x16x32_bf16 v[64:67], v[32:35], a[20:23], v[64:67]\n"
"buffer_load_dwordx2 a[64:65], v128, s[36:39], s20 offen offset:0 sc1\n"
"v_mfma_f32_16x16x32_bf16 v[100:103], v[40:43], a[20:23], v[100:103]\n"
"ds_read_b128 a[228:231], v212 offset:26624\n"
"ds_read_b128 a[224:227], v212 offset:27648\n"
"v_mfma_f32_16x16x32_bf16 v[100:103], v[44:47], a[28:31], v[100:103]\n"
"buffer_load_dwordx2 a[66:67], v128, s[36:39], s20 offen offset:512 sc1\n"
"v_mfma_f32_16x16x32_bf16 v[84:87], v[44:47], a[24:27], v[84:87]\n"
"v_mfma_f32_16x16x32_bf16 v[84:87], v[40:43], a[16:19], v[84:87]\n"
"s_waitcnt lgkmcnt(4)\n"
"buffer_load_dwordx2 a[68:69], v128, s[36:39], s20 offen offset:1024 sc1\n"
"v_mfma_f32_16x16x32_bf16 v[116:119], v[48:51], a[16:19], v[116:119]\n"
"ds_read_b128 a[236:239], v212 offset:28672\n"
"ds_read_b128 a[232:235], v212 offset:29696\n"
"v_mfma_f32_16x16x32_bf16 v[116:119], v[52:55], a[24:27], v[116:119]\n"
"buffer_load_dwordx2 a[70:71], v128, s[36:39], s20 offen offset:1536 sc1\n"
"v_mfma_f32_16x16x32_bf16 v[68:71], v[52:55], a[28:31], v[68:71]\n"
"v_mfma_f32_16x16x32_bf16 v[68:71], v[48:51], a[20:23], v[68:71]\n"
"s_waitcnt lgkmcnt(4)\n"
"buffer_load_dwordx2 a[72:73], v128, s[36:39], s20 offen offset:2048 sc1\n"
"v_mfma_f32_16x16x32_bf16 v[104:107], v[56:59], a[20:23], v[104:107]\n"
"ds_read_b128 a[244:247], v212 offset:30720\n"
"ds_read_b128 a[240:243], v212 offset:31744\n"
"v_mfma_f32_16x16x32_bf16 v[104:107], v[60:63], a[28:31], v[104:107]\n"
"buffer_load_dwordx2 a[74:75], v128, s[36:39], s20 offen offset:2560 sc1\n"
"v_mfma_f32_16x16x32_bf16 v[88:91], v[60:63], a[24:27], v[88:91]\n"
"v_mfma_f32_16x16x32_bf16 v[88:91], v[56:59], a[16:19], v[88:91]\n"
"s_waitcnt lgkmcnt(4)\n"
"v_mfma_f32_16x16x32_bf16 v[224:227], a[228:231], a[32:35], v[224:227]\n"
"ds_read_b128 a[252:255], v212 offset:32768\n"
"ds_read_b128 a[248:251], v212 offset:33792\n"
"v_mfma_f32_16x16x32_bf16 v[224:227], a[224:227], a[40:43], v[224:227]\n"
"buffer_load_dwordx2 a[76:77], v128, s[36:39], s20 offen offset:3072 sc1\n"
"v_mfma_f32_16x16x32_bf16 v[228:231], a[224:227], a[44:47], v[228:231]\n"
"v_mfma_f32_16x16x32_bf16 v[228:231], a[228:231], a[36:39], v[228:231]\n"
"s_waitcnt lgkmcnt(4)\n"
"buffer_load_dwordx2 a[78:79], v128, s[36:39], s20 offen offset:3584 sc1\n"
"v_mfma_f32_16x16x32_bf16 v[236:239], a[236:239], a[36:39], v[236:239]\n"
"s_add_u32 s20, s20, 4096\n"
"ds_read_b128 v[36:39], v212 offset:34816\n"
"ds_read_b128 v[32:35], v212 offset:35840\n"
"v_mfma_f32_16x16x32_bf16 v[236:239], a[232:235], a[44:47], v[236:239]\n"
"buffer_load_dwordx4 v212, s[36:39], s71 offen offset:0 sc1 lds\n"
"v_mfma_f32_16x16x32_bf16 v[232:235], a[232:235], a[40:43], v[232:235]\n"
"s_add_u32 m0, s87, 0\n"
"v_mfma_f32_16x16x32_bf16 v[232:235], a[236:239], a[32:35], v[232:235]\n"
"s_waitcnt lgkmcnt(4)\n"
"v_mfma_f32_16x16x32_bf16 v[240:243], a[244:247], a[32:35], v[240:243]\n"
"ds_read_b128 v[44:47], v212 offset:36864\n"
"ds_read_b128 v[40:43], v212 offset:37888\n"
"v_mfma_f32_16x16x32_bf16 v[240:243], a[240:243], a[40:43], v[240:243]\n"
"buffer_load_dwordx2 a[80:81], v128, s[36:39], s20 offen offset:0 sc1\n"
"v_mfma_f32_16x16x32_bf16 v[244:247], a[240:243], a[44:47], v[244:247]\n"
"v_mfma_f32_16x16x32_bf16 v[244:247], a[244:247], a[36:39], v[244:247]\n"
"s_waitcnt lgkmcnt(4)\n"
"buffer_load_dwordx2 a[82:83], v128, s[36:39], s20 offen offset:512 sc1\n"
"v_mfma_f32_16x16x32_bf16 v[252:255], a[252:255], a[36:39], v[252:255]\n"
"ds_read_b128 v[52:55], v212 offset:38912\n"
"ds_read_b128 v[48:51], v212 offset:39936\n"
"v_mfma_f32_16x16x32_bf16 v[252:255], a[248:251], a[44:47], v[252:255]\n"
"v_mfma_f32_16x16x32_bf16 v[248:251], a[248:251], a[40:43], v[248:251]\n"
"buffer_load_dwordx2 a[84:85], v128, s[36:39], s20 offen offset:1024 sc1\n"
"v_mfma_f32_16x16x32_bf16 v[248:251], a[252:255], a[32:35], v[248:251]\n"
"s_waitcnt lgkmcnt(4)\n"
"v_mfma_f32_16x16x32_bf16 v[80:83], v[36:39], a[32:35], v[80:83]\n"
"ds_read_b128 v[60:63], v212 offset:40960\n"
"ds_read_b128 v[56:59], v212 offset:41984\n"
"v_mfma_f32_16x16x32_bf16 v[80:83], v[32:35], a[40:43], v[80:83]\n"
"buffer_load_dwordx2 a[86:87], v128, s[36:39], s20 offen offset:1536 sc1\n"
"v_mfma_f32_16x16x32_bf16 v[96:99], v[32:35], a[44:47], v[96:99]\n"
"v_mfma_f32_16x16x32_bf16 v[96:99], v[36:39], a[36:39], v[96:99]\n"
"s_waitcnt vmcnt(19) lgkmcnt(4)\n"
"ds_write_b32 v214, v219 offset:1152\n"
"v_mfma_f32_16x16x32_bf16 v[64:67], v[44:47], a[36:39], v[64:67]\n"
"ds_read_b128 a[224:227], v212 offset:43008\n"
"ds_read_b128 a[228:231], v212 offset:44032\n"
"v_mfma_f32_16x16x32_bf16 v[64:67], v[40:43], a[44:47], v[64:67]\n"
"buffer_load_dwordx2 a[88:89], v128, s[36:39], s20 offen offset:2048 sc1\n"
"v_mfma_f32_16x16x32_bf16 v[112:115], v[40:43], a[40:43], v[112:115]\n"
"v_mfma_f32_16x16x32_bf16 v[112:115], v[44:47], a[32:35], v[112:115]\n"
"s_waitcnt lgkmcnt(5)\n"
"buffer_load_dwordx2 a[90:91], v128, s[36:39], s20 offen offset:2560 sc1\n"
"v_mfma_f32_16x16x32_bf16 v[84:87], v[52:55], a[32:35], v[84:87]\n"
"ds_read_b128 a[232:235], v210 offset:4096\n"
"ds_read_b128 a[236:239], v210 offset:5120\n"
"v_mfma_f32_16x16x32_bf16 v[84:87], v[48:51], a[40:43], v[84:87]\n"
"buffer_load_dwordx2 a[92:93], v128, s[36:39], s20 offen offset:3072 sc1\n"
"v_mfma_f32_16x16x32_bf16 v[100:103], v[48:51], a[44:47], v[100:103]\n"
"ds_read_b32 v221, v220 offset:1152\n"
"v_mfma_f32_16x16x32_bf16 v[100:103], v[52:55], a[36:39], v[100:103]\n"
"s_waitcnt lgkmcnt(5)\n"
"buffer_load_dwordx2 a[94:95], v128, s[36:39], s20 offen offset:3584 sc1\n"
"v_mfma_f32_16x16x32_bf16 v[68:71], v[60:63], a[36:39], v[68:71]\n"
"s_add_u32 s20, s20, 4096\n"
"ds_read_b128 a[240:243], v210 offset:6144\n"
"ds_read_b128 a[244:247], v210 offset:7168\n"
"v_mfma_f32_16x16x32_bf16 v[68:71], v[56:59], a[44:47], v[68:71]\n"
"v_lshrrev_b32 v130, 8, v212\n"
"v_mfma_f32_16x16x32_bf16 v[116:119], v[56:59], a[40:43], v[116:119]\n"
"v_lshlrev_b32 v130, 8, v130\n"
"v_mfma_f32_16x16x32_bf16 v[116:119], v[60:63], a[32:35], v[116:119]\n"
"s_waitcnt lgkmcnt(2)\n"
"v_cmp_ne_u32 vcc, s75, v221\n"
"v_bfe_u32 v131, v212, 4, 4\n"
"v_mfma_f32_16x16x32_bf16 v[88:91], a[224:227], a[32:35], v[88:91]\n"
"ds_read_b128 a[248:251], v210 offset:8192\n"
"ds_read_b128 a[252:255], v210 offset:9216\n"
"v_mfma_f32_16x16x32_bf16 v[88:91], a[228:231], a[40:43], v[88:91]\n"
"v_lshl_add_u32 v130, v131, 2, v130\n"
"v_mfma_f32_16x16x32_bf16 v[104:107], a[228:231], a[44:47], v[104:107]\n"
"s_cbranch_vccnz Lsb36_%=\n"
"Lsr36_%=:\n"
"v_mfma_f32_16x16x32_bf16 v[104:107], a[224:227], a[36:39], v[104:107]\n"
"s_waitcnt vmcnt(21)\n"
"buffer_load_dword a96, v130, s[36:39], s20 offen offset:0 sc1\n"
"v_mfma_f32_16x16x32_bf16 v[96:99], a[232:235], a[52:55], v[96:99]\n"
"s_waitcnt vmcnt(20)\n"
"ds_read_b128 v[32:35], v210 offset:10240\n"
"ds_read_b128 v[36:39], v210 offset:11264\n"
"v_mfma_f32_16x16x32_bf16 v[96:99], a[236:239], a[60:63], v[96:99]\n"
"buffer_load_dword a97, v130, s[36:39], s20 offen offset:128 sc1\n"
"v_mfma_f32_16x16x32_bf16 v[80:83], a[236:239], a[56:59], v[80:83]\n"
"buffer_load_dword a100, v130, s[36:39], s20 offen offset:64 sc1\n"
"v_mfma_f32_16x16x32_bf16 v[80:83], a[232:235], a[48:51], v[80:83]\n"
"s_waitcnt lgkmcnt(4)\n"
"buffer_load_dword a101, v130, s[36:39], s20 offen offset:192 sc1\n"
"v_mfma_f32_16x16x32_bf16 v[112:115], a[240:243], a[48:51], v[112:115]\n"
"ds_read_b128 v[40:43], v210 offset:12288\n"
"ds_read_b128 v[44:47], v210 offset:13312\n"
"v_mfma_f32_16x16x32_bf16 v[112:115], a[244:247], a[56:59], v[112:115]\n"
"buffer_load_dwordx4 v132, s[48:51], s84 offen offset:0 lds\n"
"v_mfma_f32_16x16x32_bf16 v[64:67], a[244:247], a[60:63], v[64:67]\n"
"s_add_u32 m0, s87, 1024\n"
"v_mfma_f32_16x16x32_bf16 v[64:67], a[240:243], a[52:55], v[64:67]\n"
"s_waitcnt lgkmcnt(4)\n"
"v_mfma_f32_16x16x32_bf16 v[100:103], a[248:251], a[52:55], v[100:103]\n"
"ds_read_b128 v[48:51], v210 offset:22528\n"
"ds_read_b128 v[52:55], v210 offset:23552\n"
"v_mfma_f32_16x16x32_bf16 v[100:103], a[252:255], a[60:63], v[100:103]\n"
"buffer_load_dwordx4 v132, s[48:51], s85 offen offset:0 lds\n"
"v_mfma_f32_16x16x32_bf16 v[84:87], a[252:255], a[56:59], v[84:87]\n"
"s_add_u32 m0, s87, 2048\n"
"v_mfma_f32_16x16x32_bf16 v[84:87], a[248:251], a[48:51], v[84:87]\n"
"s_waitcnt lgkmcnt(4)\n"
"v_mfma_f32_16x16x32_bf16 v[116:119], v[32:35], a[48:51], v[116:119]\n"
"ds_read_b128 v[56:59], v210 offset:24576\n"
"ds_read_b128 v[60:63], v210 offset:25600\n"
"v_mfma_f32_16x16x32_bf16 v[116:119], v[36:39], a[56:59], v[116:119]\n"
"buffer_load_dwordx4 v133, s[48:51], s86 offen offset:0 lds\n"
"v_mfma_f32_16x16x32_bf16 v[68:71], v[36:39], a[60:63], v[68:71]\n"
"v_mfma_f32_16x16x32_bf16 v[68:71], v[32:35], a[52:55], v[68:71]\n"
"s_waitcnt lgkmcnt(4)\n"
"v_mfma_f32_16x16x32_bf16 v[104:107], v[40:43], a[52:55], v[104:107]\n"
"ds_read_b128 a[228:231], v211 offset:10240\n"
"ds_read_b128 a[224:227], v211 offset:11264\n"
"v_mfma_f32_16x16x32_bf16 v[104:107], v[44:47], a[60:63], v[104:107]\n"
"v_mfma_f32_16x16x32_bf16 v[88:91], v[44:47], a[56:59], v[88:91]\n"
"v_mfma_f32_16x16x32_bf16 v[88:91], v[40:43], a[48:51], v[88:91]\n"
"s_waitcnt vmcnt(22) lgkmcnt(4)\n"
"v_mfma_f32_16x16x32_bf16 v[80:83], v[48:51], a[64:67], v[80:83]\n"
"s_waitcnt vmcnt(18)\n"
"ds_read_b128 a[236:239], v211 offset:12288\n"
"ds_read_b128 a[232:235], v211 offset:13312\n"
"v_mfma_f32_16x16x32_bf16 v[80:83], v[52:55], a[72:75], v[80:83]\n"
"s_waitcnt vmcnt(16)\n"
"v_mfma_f32_16x16x32_bf16 v[96:99], v[52:55], a[76:79], v[96:99]\n"
"v_mfma_f32_16x16x32_bf16 v[96:99], v[48:51], a[68:71], v[96:99]\n"
"s_waitcnt lgkmcnt(4)\n"
"v_mfma_f32_16x16x32_bf16 v[64:67], v[56:59], a[68:71], v[64:67]\n"
"ds_read_b128 a[244:247], v211 offset:14336\n"
"ds_read_b128 a[240:243], v211 offset:15360\n"
"v_mfma_f32_16x16x32_bf16 v[64:67], v[60:63], a[76:79], v[64:67]\n"
"v_mfma_f32_16x16x32_bf16 v[112:115], v[60:63], a[72:75], v[112:115]\n"
"v_mfma_f32_16x16x32_bf16 v[112:115], v[56:59], a[64:67], v[112:115]\n"
"s_waitcnt lgkmcnt(4)\n"
"v_mfma_f32_16x16x32_bf16 v[84:87], a[228:231], a[64:67], v[84:87]\n"
"ds_read_b128 a[252:255], v211 offset:24576\n"
"ds_read_b128 a[248:251], v211 offset:25600\n"
"v_mfma_f32_16x16x32_bf16 v[84:87], a[224:227], a[72:75], v[84:87]\n"
"v_mfma_f32_16x16x32_bf16 v[100:103], a[224:227], a[76:79], v[100:103]\n"
"v_mfma_f32_16x16x32_bf16 v[100:103], a[228:231], a[68:71], v[100:103]\n"
"s_waitcnt vmcnt(15) lgkmcnt(4)\n"
"ds_write_b32 v214, v219 offset:1184\n"
"v_mfma_f32_16x16x32_bf16 v[68:71], a[236:239], a[68:71], v[68:71]\n"
"ds_read_b128 v[36:39], v211 offset:26624\n"
"ds_read_b128 v[32:35], v211 offset:27648\n"
"v_mfma_f32_16x16x32_bf16 v[68:71], a[232:235], a[76:79], v[68:71]\n"
"v_mfma_f32_16x16x32_bf16 v[116:119], a[232:235], a[72:75], v[116:119]\n"
"v_mfma_f32_16x16x32_bf16 v[116:119], a[236:239], a[64:67], v[116:119]\n"
"s_waitcnt lgkmcnt(5)\n"
"v_mfma_f32_16x16x32_bf16 v[88:91], a[244:247], a[64:67], v[88:91]\n"
"ds_read_b128 v[44:47], v211 offset:28672\n"
"ds_read_b128 v[40:43], v211 offset:29696\n"
"v_mfma_f32_16x16x32_bf16 v[88:91], a[240:243], a[72:75], v[88:91]\n"
"v_mfma_f32_16x16x32_bf16 v[104:107], a[240:243], a[76:79], v[104:107]\n"
"ds_read_b32 v221, v220 offset:1184\n"
"v_mfma_f32_16x16x32_bf16 v[104:107], a[244:247], a[68:71], v[104:107]\n"
"s_waitcnt vmcnt(11) lgkmcnt(5)\n"
"v_mfma_f32_16x16x32_bf16 v[96:99], a[252:255], a[84:87], v[96:99]\n"
"s_waitcnt vmcnt(7)\n"
"ds_read_b128 v[52:55], v211 offset:30720\n"
"ds_read_b128 v[48:51], v211 offset:31744\n"
"v_mfma_f32_16x16x32_bf16 v[96:99], a[248:251], a[92:95], v[96:99]\n"
"v_mfma_f32_16x16x32_bf16 v[80:83], a[248:251], a[88:91], v[80:83]\n"
"v_mfma_f32_16x16x32_bf16 v[80:83], a[252:255], a[80:83], v[80:83]\n"
"s_waitcnt lgkmcnt(2)\n"
"v_cmp_ne_u32 vcc, s75, v221\n"
"v_mfma_f32_16x16x32_bf16 v[112:115], v[36:39], a[80:83], v[112:115]\n"
"ds_read_b128 v[60:63], v211 offset:32768\n"
"ds_read_b128 v[56:59], v211 offset:33792\n"
"v_mfma_f32_16x16x32_bf16 v[112:115], v[32:35], a[88:91], v[112:115]\n"
"v_mfma_f32_16x16x32_bf16 v[64:67], v[32:35], a[92:95], v[64:67]\n"
"s_cbranch_vccnz Lsb37_%=\n"
"Lsr37_%=:\n"
"v_mfma_f32_16x16x32_bf16 v[64:67], v[36:39], a[84:87], v[64:67]\n"
"v_mfma_f32_16x16x32_bf16 v[100:103], v[44:47], a[84:87], v[100:103]\n"
"ds_read_b64 a[224:225], v211 offset:38912\n"
"ds_read_b64 a[228:229], v211 offset:39936\n"
"v_mfma_f32_16x16x32_bf16 v[100:103], v[40:43], a[92:95], v[100:103]\n"
"v_mfma_f32_16x16x32_bf16 v[84:87], v[40:43], a[88:91], v[84:87]\n"
"v_mfma_f32_16x16x32_bf16 v[84:87], v[44:47], a[80:83], v[84:87]\n"
"s_waitcnt lgkmcnt(4)\n"
"v_mfma_f32_16x16x32_bf16 v[116:119], v[52:55], a[80:83], v[116:119]\n"
"ds_read_b64 a[232:233], v211 offset:40960\n"
"ds_read_b64 a[236:237], v211 offset:41984\n"
"v_mfma_f32_16x16x32_bf16 v[116:119], v[48:51], a[88:91], v[116:119]\n"
"v_mfma_f32_16x16x32_bf16 v[68:71], v[48:51], a[92:95], v[68:71]\n"
"v_mfma_f32_16x16x32_bf16 v[68:71], v[52:55], a[84:87], v[68:71]\n"
"s_waitcnt lgkmcnt(4)\n"
"v_mfma_f32_16x16x32_bf16 v[104:107], v[60:63], a[84:87], v[104:107]\n"
"ds_read_b64 a[240:241], v210 offset:26624\n"
"ds_read_b128 a[244:247], v212 offset:45056\n"
"v_mfma_f32_16x16x32_bf16 v[104:107], v[56:59], a[92:95], v[104:107]\n"
"v_mfma_f32_16x16x32_bf16 v[88:91], v[56:59], a[88:91], v[88:91]\n"
"v_mfma_f32_16x16x32_bf16 v[88:91], v[60:63], a[80:83], v[88:91]\n"
"s_waitcnt vmcnt(5) lgkmcnt(4)\n"
"ds_read_b128 a[248:251], v212 offset:46080\n"
"ds_read_b128 a[252:255], v212 offset:48128\n"
"v_mfma_f32_16x16x16_bf16 v[80:83], a[224:225], a[96:97], v[80:83]\n"
"s_waitcnt vmcnt(3)\n"
"v_mfma_f32_16x16x16_bf16 v[96:99], a[224:225], a[100:101], v[96:99]\n"
"ds_read_b128 v[32:35], v212 offset:47104\n"
"ds_read_b128 v[36:39], v212 offset:49152\n"
"v_mfma_f32_16x16x16_bf16 v[64:67], a[228:229], a[100:101], v[64:67]\n"
"v_mfma_f32_16x16x16_bf16 v[112:115], a[228:229], a[96:97], v[112:115]\n"
"s_waitcnt lgkmcnt(6)\n"
"ds_read_b128 v[40:43], v212 offset:50176\n"
"ds_read_b128 v[44:47], v210 offset:3072\n"
"v_mfma_f32_16x16x16_bf16 v[84:87], a[232:233], a[96:97], v[84:87]\n"
"s_waitcnt vmcnt(0)\n"
"ds_read_b128 v[136:139], v156 offset:0\n"
"ds_read_b128 v[140:143], v157 offset:0\n"
"v_mfma_f32_16x16x16_bf16 v[100:103], a[232:233], a[100:101], v[100:103]\n"
"ds_read_b128 v[48:51], v210 offset:2048\n"
"ds_read_b128 v[52:55], v210 offset:14336\n"
"v_mfma_f32_16x16x16_bf16 v[68:71], a[236:237], a[100:101], v[68:71]\n"
"ds_read_b64 v[144:145], v158 offset:2048\n"
"v_mfma_f32_16x16x16_bf16 v[116:119], a[236:237], a[96:97], v[116:119]\n"
"s_waitcnt lgkmcnt(9)\n"
"ds_read_b128 v[56:59], v210 offset:15360\n"
"ds_read_b128 v[60:63], v210 offset:17408\n"
"v_mfma_f32_16x16x16_bf16 v[88:91], a[240:241], a[96:97], v[88:91]\n"
"v_mfma_f32_16x16x16_bf16 v[104:107], a[240:241], a[100:101], v[104:107]\n"
"ds_read_b128 a[224:227], v210 offset:16384\n"
"ds_read_b128 a[228:231], v210 offset:18432\n"
"v_mfma_f32_16x16x32_bf16 v[228:231], a[244:247], a[52:55], v[228:231]\n"
"v_mfma_f32_16x16x32_bf16 v[228:231], a[248:251], a[60:63], v[228:231]\n"
"ds_read_b128 a[232:235], v210 offset:19456\n"
"ds_read_b128 a[236:239], v210 offset:21504\n"
"v_mfma_f32_16x16x32_bf16 v[236:239], a[252:255], a[60:63], v[236:239]\n"
"v_cvt_pk_bf16_f32 v80, v80, v81\n"
"v_cvt_pk_bf16_f32 v81, v82, v83\n"
"v_cvt_pk_bf16_f32 v82, v112, v113\n"
"v_cvt_pk_bf16_f32 v83, v114, v115\n"
"buffer_store_dwordx4 v[80:83], v212, s[36:39], s8 offen offset:0 sc1\n"
"v_cvt_pk_bf16_f32 v96, v96, v97\n"
"v_cvt_pk_bf16_f32 v97, v98, v99\n"
"v_cvt_pk_bf16_f32 v98, v64, v65\n"
"v_cvt_pk_bf16_f32 v99, v66, v67\n"
"buffer_store_dwordx4 v[96:99], v212, s[36:39], s78 offen offset:0 sc1\n"
"v_cvt_pk_bf16_f32 v84, v84, v85\n"
"v_cvt_pk_bf16_f32 v85, v86, v87\n"
"v_cvt_pk_bf16_f32 v86, v116, v117\n"
"v_cvt_pk_bf16_f32 v87, v118, v119\n"
"buffer_store_dwordx4 v[84:87], v212, s[36:39], s8 offen offset:1024 sc1\n"
"v_cvt_pk_bf16_f32 v100, v100, v101\n"
"v_cvt_pk_bf16_f32 v101, v102, v103\n"
"v_cvt_pk_bf16_f32 v102, v68, v69\n"
"v_cvt_pk_bf16_f32 v103, v70, v71\n"
"buffer_store_dwordx4 v[100:103], v212, s[36:39], s78 offen offset:1024 sc1\n"
"v_cvt_pk_bf16_f32 v104, v104, v105\n"
"v_cvt_pk_bf16_f32 v105, v106, v107\n"
"buffer_store_dwordx2 v[104:105], v152, s[36:39], 0 offen offset:2048 sc1\n"
"v_cvt_pk_bf16_f32 v154, v88, v89\n"
"v_cvt_pk_bf16_f32 v155, v90, v91\n"
"buffer_store_dwordx2 v[154:155], v153, s[36:39], s8 offen offset:2048 sc1\n"
"s_waitcnt lgkmcnt(9)\n"
"v_mfma_f32_16x16x32_bf16 v[236:239], v[32:35], a[52:55], v[236:239]\n"
"ds_read_b128 a[240:243], v210 offset:20480\n"
"ds_read_b128 a[244:247], v211 offset:16384\n"
"v_mfma_f32_16x16x32_bf16 v[244:247], v[36:39], a[52:55], v[244:247]\n"
"v_mfma_f32_16x16x32_bf16 v[244:247], v[40:43], a[60:63], v[244:247]\n"
"ds_read_b128 a[248:251], v211 offset:17408\n"
"ds_read_b128 a[252:255], v211 offset:19456\n"
"v_mfma_f32_16x16x32_bf16 v[252:255], v[44:47], a[60:63], v[252:255]\n"
"s_waitcnt lgkmcnt(8)\n"
"v_mfma_f32_16x16x32_bf16 v[252:255], v[48:51], a[52:55], v[252:255]\n"
"ds_read_b128 v[32:35], v211 offset:18432\n"
"ds_read_b128 v[36:39], v211 offset:20480\n"
"v_mfma_f32_16x16x32_bf16 v[228:231], v[52:55], a[68:71], v[228:231]\n"
"v_mfma_f32_16x16x32_bf16 v[228:231], v[56:59], a[76:79], v[228:231]\n"
"ds_read_b128 v[40:43], v211 offset:21504\n"
"ds_read_b128 v[44:47], v211 offset:23552\n"
"v_mfma_f32_16x16x32_bf16 v[236:239], v[60:63], a[76:79], v[236:239]\n"
"s_waitcnt lgkmcnt(8)\n"
"v_mfma_f32_16x16x32_bf16 v[236:239], a[224:227], a[68:71], v[236:239]\n"
"ds_read_b128 v[48:51], v211 offset:22528\n"
"ds_read_b64 v[52:53], v211 offset:34816\n"
"v_mfma_f32_16x16x32_bf16 v[244:247], a[228:231], a[68:71], v[244:247]\n"
"v_mfma_f32_16x16x32_bf16 v[244:247], a[232:235], a[76:79], v[244:247]\n"
"ds_read_b64 v[56:57], v211 offset:35840\n"
"ds_read_b64 v[60:61], v211 offset:36864\n"
"v_mfma_f32_16x16x32_bf16 v[252:255], a[236:239], a[76:79], v[252:255]\n"
"s_waitcnt lgkmcnt(8)\n"
"v_mfma_f32_16x16x32_bf16 v[252:255], a[240:243], a[68:71], v[252:255]\n"
"ds_read_b64 a[224:225], v211 offset:37888\n"
"ds_read_b128 a[228:231], v212 offset:45056\n"
"v_mfma_f32_16x16x32_bf16 v[228:231], a[244:247], a[84:87], v[228:231]\n"
"v_mfma_f32_16x16x32_bf16 v[228:231], a[248:251], a[92:95], v[228:231]\n"
"ds_read_b128 a[232:235], v212 offset:46080\n"
"ds_read_b128 a[236:239], v212 offset:48128\n"
"v_mfma_f32_16x16x32_bf16 v[236:239], a[252:255], a[92:95], v[236:239]\n"
"s_waitcnt lgkmcnt(8)\n"
"v_mfma_f32_16x16x32_bf16 v[236:239], v[32:35], a[84:87], v[236:239]\n"
"ds_read_b128 a[240:243], v212 offset:47104\n"
"ds_read_b128 a[244:247], v212 offset:49152\n"
"v_mfma_f32_16x16x32_bf16 v[244:247], v[36:39], a[84:87], v[244:247]\n"
"v_mfma_f32_16x16x32_bf16 v[244:247], v[40:43], a[92:95], v[244:247]\n"
"ds_read_b128 a[248:251], v212 offset:50176\n"
"ds_read_b128 a[252:255], v210 offset:3072\n"
"v_mfma_f32_16x16x32_bf16 v[252:255], v[44:47], a[92:95], v[252:255]\n"
"s_waitcnt lgkmcnt(8)\n"
"v_mfma_f32_16x16x32_bf16 v[252:255], v[48:51], a[84:87], v[252:255]\n"
"ds_read_b128 v[32:35], v210 offset:2048\n"
"ds_read_b128 v[36:39], v210 offset:14336\n"
"v_mfma_f32_16x16x16_bf16 v[228:231], v[52:53], a[100:101], v[228:231]\n"
"v_mfma_f32_16x16x16_bf16 v[236:239], v[56:57], a[100:101], v[236:239]\n"
"ds_read_b128 v[40:43], v210 offset:15360\n"
"ds_read_b128 v[44:47], v210 offset:17408\n"
"v_mfma_f32_16x16x16_bf16 v[244:247], v[60:61], a[100:101], v[244:247]\n"
"s_waitcnt lgkmcnt(8)\n"
"v_mfma_f32_16x16x16_bf16 v[252:255], a[224:225], a[100:101], v[252:255]\n"
"ds_read_b128 v[48:51], v210 offset:16384\n"
"ds_read_b128 v[52:55], v210 offset:18432\n"
"v_mfma_f32_16x16x32_bf16 v[224:227], a[228:231], a[48:51], v[224:227]\n"
"v_mfma_f32_16x16x32_bf16 v[224:227], a[232:235], a[56:59], v[224:227]\n"
"ds_read_b128 v[56:59], v210 offset:19456\n"
"ds_read_b128 v[60:63], v210 offset:21504\n"
"v_mfma_f32_16x16x32_bf16 v[232:235], a[236:239], a[56:59], v[232:235]\n"
"s_waitcnt lgkmcnt(8)\n"
"v_cvt_pk_bf16_f32 v228, v228, v229\n"
"v_cvt_pk_bf16_f32 v229, v230, v231\n"
"v_mfma_f32_16x16x32_bf16 v[232:235], a[240:243], a[48:51], v[232:235]\n"
"ds_read_b128 a[224:227], v210 offset:20480\n"
"ds_read_b128 a[228:231], v211 offset:16384\n"
"v_mfma_f32_16x16x32_bf16 v[240:243], a[244:247], a[48:51], v[240:243]\n"
"v_cvt_pk_bf16_f32 v230, v236, v237\n"
"v_cvt_pk_bf16_f32 v231, v238, v239\n"
"buffer_store_dwordx4 v[228:231], v212, s[36:39], s9 offen offset:0 sc1\n"
"v_cvt_pk_bf16_f32 v244, v244, v245\n"
"v_cvt_pk_bf16_f32 v245, v246, v247\n"
"v_cvt_pk_bf16_f32 v246, v252, v253\n"
"v_cvt_pk_bf16_f32 v247, v254, v255\n"
"buffer_store_dwordx4 v[244:247], v212, s[36:39], s9 offen offset:1024 sc1\n"
"v_mfma_f32_16x16x32_bf16 v[240:243], a[248:251], a[56:59], v[240:243]\n"
"ds_read_b128 a[232:235], v211 offset:17408\n"
"ds_read_b128 a[236:239], v211 offset:19456\n"
"v_mfma_f32_16x16x32_bf16 v[248:251], a[252:255], a[56:59], v[248:251]\n"
"s_waitcnt lgkmcnt(8)\n"
"v_mfma_f32_16x16x32_bf16 v[248:251], v[32:35], a[48:51], v[248:251]\n"
"ds_read_b128 a[240:243], v211 offset:18432\n"
"ds_read_b128 a[244:247], v211 offset:20480\n"
"v_mfma_f32_16x16x32_bf16 v[224:227], v[36:39], a[64:67], v[224:227]\n"
"v_mfma_f32_16x16x32_bf16 v[224:227], v[40:43], a[72:75], v[224:227]\n"
"ds_read_b128 a[248:251], v211 offset:21504\n"
"ds_read_b128 a[252:255], v211 offset:23552\n"
"v_mfma_f32_16x16x32_bf16 v[232:235], v[44:47], a[72:75], v[232:235]\n"
"s_waitcnt lgkmcnt(8)\n"
"v_mfma_f32_16x16x32_bf16 v[232:235], v[48:51], a[64:67], v[232:235]\n"
"ds_read_b128 v[32:35], v211 offset:22528\n"
"ds_read_b64 v[36:37], v211 offset:34816\n"
"v_mfma_f32_16x16x32_bf16 v[240:243], v[52:55], a[64:67], v[240:243]\n"
"v_mfma_f32_16x16x32_bf16 v[240:243], v[56:59], a[72:75], v[240:243]\n"
"ds_read_b64 v[40:41], v211 offset:35840\n"
"ds_read_b64 v[44:45], v211 offset:36864\n"
"v_mfma_f32_16x16x32_bf16 v[248:251], v[60:63], a[72:75], v[248:251]\n"
"s_waitcnt lgkmcnt(8)\n"
"v_mfma_f32_16x16x32_bf16 v[248:251], a[224:227], a[64:67], v[248:251]\n"
"ds_read_b64 v[48:49], v211 offset:37888\n"
"v_mfma_f32_16x16x32_bf16 v[224:227], a[228:231], a[80:83], v[224:227]\n"
"v_mfma_f32_16x16x32_bf16 v[224:227], a[232:235], a[88:91], v[224:227]\n"
"v_mfma_f32_16x16x32_bf16 v[232:235], a[236:239], a[88:91], v[232:235]\n"
"s_waitcnt lgkmcnt(5)\n"
"v_mfma_f32_16x16x32_bf16 v[232:235], a[240:243], a[80:83], v[232:235]\n"
"v_mfma_f32_16x16x32_bf16 v[240:243], a[244:247], a[80:83], v[240:243]\n"
"s_waitcnt vmcnt(2)\n"
"buffer_store_dword v219, off, s[36:39], s23 offset:896 sc1\n"
"v_mfma_f32_16x16x32_bf16 v[240:243], a[248:251], a[88:91], v[240:243]\n"
"v_mfma_f32_16x16x32_bf16 v[248:251], a[252:255], a[88:91], v[248:251]\n"
"s_waitcnt lgkmcnt(1)\n"
"v_mfma_f32_16x16x32_bf16 v[248:251], v[32:35], a[80:83], v[248:251]\n"
"v_mfma_f32_16x16x16_bf16 v[224:227], v[36:37], a[96:97], v[224:227]\n"
"v_mfma_f32_16x16x16_bf16 v[232:235], v[40:41], a[96:97], v[232:235]\n"
"s_nop 0\n"
"v_mfma_f32_16x16x16_bf16 v[240:243], v[44:45], a[96:97], v[240:243]\n"
"s_waitcnt lgkmcnt(0)\n"
"s_nop 0\n"
"v_mfma_f32_16x16x16_bf16 v[248:251], v[48:49], a[96:97], v[248:251]\n"
"s_nop 7\n"
"v_mov_b32 v148, 0x00003f80\n"
"v_mov_b32 v149, 0x3f800000\n"
"s_add_u32 s67, s83, 2304\n"
"s_add_u32 s68, s83, 4608\n"
"v_dot2c_f32_bf16 v224, v136, v148\n"
"v_dot2c_f32_bf16 v226, v137, v148\n"
"v_dot2c_f32_bf16 v232, v138, v148\n"
"v_dot2c_f32_bf16 v234, v139, v148\n"
"v_dot2c_f32_bf16 v240, v140, v148\n"
"v_dot2c_f32_bf16 v242, v141, v148\n"
"v_dot2c_f32_bf16 v248, v142, v148\n"
"v_dot2c_f32_bf16 v250, v143, v148\n"
"v_dot2c_f32_bf16 v88, v144, v148\n"
"v_dot2c_f32_bf16 v90, v145, v148\n"
"v_dot2c_f32_bf16 v225, v136, v149\n"
"v_dot2c_f32_bf16 v227, v137, v149\n"
"v_dot2c_f32_bf16 v233, v138, v149\n"
"v_dot2c_f32_bf16 v235, v139, v149\n"
"v_dot2c_f32_bf16 v241, v140, v149\n"
"v_dot2c_f32_bf16 v243, v141, v149\n"
"v_dot2c_f32_bf16 v249, v142, v149\n"
"v_dot2c_f32_bf16 v251, v143, v149\n"
"v_dot2c_f32_bf16 v89, v144, v149\n"
"v_dot2c_f32_bf16 v91, v145, v149\n"
"buffer_load_dword v129, v150, s[36:39], s29 offen offset:768 sc1\n"
"s_mov_b32 s66, 14\n"
"s_waitcnt vmcnt(2)\n"
"buffer_store_dword v219, off, s[36:39], s23 offset:768 sc1\n"
"s_waitcnt vmcnt(1)\n"
"Lpp_%=:\n"
"v_cmp_eq_u32 vcc, s75, v129\n"
"s_nop 1\n"
"s_and_b32 vcc_lo, vcc_lo, s66\n"
"s_andn2_b32 s66, s66, vcc_lo\n"
"s_bitcmp1_b32 vcc_lo, 1\n"
"s_cbranch_scc0 Lpn0_%=\n"
"buffer_load_dwordx4 v[0:3], v212, s[36:39], s83 offen offset:0 sc1\n"
"buffer_load_dwordx4 v[4:7], v212, s[36:39], s83 offen offset:1024 sc1\n"
"buffer_load_dwordx2 v[8:9], v151, s[36:39], s83 offen offset:2048 sc1\n"
"Lpn0_%=:\n"
"s_bitcmp1_b32 vcc_lo, 3\n"
"s_cbranch_scc0 Lpn2_%=\n"
"buffer_load_dwordx4 v[24:27], v212, s[36:39], s68 offen offset:0 sc1\n"
"buffer_load_dwordx4 v[28:31], v212, s[36:39], s68 offen offset:1024 sc1\n"
"buffer_load_dwordx2 v[32:33], v151, s[36:39], s68 offen offset:2048 sc1\n"
"Lpn2_%=:\n"
"s_bitcmp1_b32 vcc_lo, 2\n"
"s_cbranch_scc0 Lpn1_%=\n"
"buffer_load_dwordx4 v[12:15], v212, s[36:39], s67 offen offset:0 sc1\n"
"buffer_load_dwordx4 v[16:19], v212, s[36:39], s67 offen offset:1024 sc1\n"
"buffer_load_dwordx2 v[20:21], v151, s[36:39], s67 offen offset:2048 sc1\n"
"Lpn1_%=:\n"
"s_cmp_eq_u32 s66, 0\n"
"s_cbranch_scc1 Lpe_%=\n"
"buffer_load_dword v129, v150, s[36:39], s29 offen offset:768 sc1\n"
"s_waitcnt vmcnt(0)\n"
"s_sub_u32 s76, s76, 1\n"
"s_cbranch_scc0 Lpp_%=\n"
"Lpe_%=:\n"
"s_waitcnt vmcnt(3)\n"
"s_bitcmp1_b32 vcc_lo, 2\n"
"s_cbranch_scc1 Lpw_%=\n"
"s_waitcnt vmcnt(0)\n"
"Lpw_%=:\n"
"v_dot2c_f32_bf16 v224, v0, v148\n"
"v_dot2c_f32_bf16 v226, v1, v148\n"
"v_dot2c_f32_bf16 v232, v2, v148\n"
"v_dot2c_f32_bf16 v234, v3, v148\n"
"v_dot2c_f32_bf16 v240, v4, v148\n"
"v_dot2c_f32_bf16 v242, v5, v148\n"
"v_dot2c_f32_bf16 v248, v6, v148\n"
"v_dot2c_f32_bf16 v250, v7, v148\n"
"v_dot2c_f32_bf16 v88, v8, v148\n"
"v_dot2c_f32_bf16 v90, v9, v148\n"
"v_dot2c_f32_bf16 v225, v0, v149\n"
"v_dot2c_f32_bf16 v227, v1, v149\n"
"v_dot2c_f32_bf16 v233, v2, v149\n"
"v_dot2c_f32_bf16 v235, v3, v149\n"
"v_dot2c_f32_bf16 v241, v4, v149\n"
"v_dot2c_f32_bf16 v243, v5, v149\n"
"v_dot2c_f32_bf16 v249, v6, v149\n"
"v_dot2c_f32_bf16 v251, v7, v149\n"
"v_dot2c_f32_bf16 v89, v8, v149\n"
"v_dot2c_f32_bf16 v91, v9, v149\n"
"v_dot2c_f32_bf16 v224, v24, v148\n"
"v_dot2c_f32_bf16 v226, v25, v148\n"
"v_dot2c_f32_bf16 v232, v26, v148\n"
"v_dot2c_f32_bf16 v234, v27, v148\n"
"v_dot2c_f32_bf16 v240, v28, v148\n"
"v_dot2c_f32_bf16 v242, v29, v148\n"
"v_dot2c_f32_bf16 v248, v30, v148\n"
"v_dot2c_f32_bf16 v250, v31, v148\n"
"v_dot2c_f32_bf16 v88, v32, v148\n"
"v_dot2c_f32_bf16 v90, v33, v148\n"
"v_dot2c_f32_bf16 v225, v24, v149\n"
"v_dot2c_f32_bf16 v227, v25, v149\n"
"v_dot2c_f32_bf16 v233, v26, v149\n"
"v_dot2c_f32_bf16 v235, v27, v149\n"
"v_dot2c_f32_bf16 v241, v28, v149\n"
"v_dot2c_f32_bf16 v243, v29, v149\n"
"v_dot2c_f32_bf16 v249, v30, v149\n"
"v_dot2c_f32_bf16 v251, v31, v149\n"
"v_dot2c_f32_bf16 v89, v32, v149\n"
"v_dot2c_f32_bf16 v91, v33, v149\n"
"Lpad11_%=:\n"
"s_cbranch_execz Lret_%=\n"
"s_waitcnt vmcnt(0)\n"
"v_dot2c_f32_bf16 v224, v12, v148\n"
"v_dot2c_f32_bf16 v226, v13, v148\n"
"v_dot2c_f32_bf16 v232, v14, v148\n"
"v_dot2c_f32_bf16 v234, v15, v148\n"
"v_dot2c_f32_bf16 v240, v16, v148\n"
"v_dot2c_f32_bf16 v242, v17, v148\n"
"v_dot2c_f32_bf16 v248, v18, v148\n"
"v_dot2c_f32_bf16 v250, v19, v148\n"
"v_dot2c_f32_bf16 v88, v20, v148\n"
"v_dot2c_f32_bf16 v90, v21, v148\n"
"v_dot2c_f32_bf16 v225, v12, v149\n"
"v_dot2c_f32_bf16 v227, v13, v149\n"
"v_dot2c_f32_bf16 v233, v14, v149\n"
"v_dot2c_f32_bf16 v235, v15, v149\n"
"v_dot2c_f32_bf16 v241, v16, v149\n"
"v_dot2c_f32_bf16 v243, v17, v149\n"
"v_dot2c_f32_bf16 v249, v18, v149\n"
"v_dot2c_f32_bf16 v251, v19, v149\n"
"v_dot2c_f32_bf16 v89, v20, v149\n"
"v_dot2c_f32_bf16 v91, v21, v149\n"
"v_cvt_pk_bf16_f32 v224, v224, v225\n"
"v_cvt_pk_bf16_f32 v225, v226, v227\n"
"v_cvt_pk_bf16_f32 v226, v232, v233\n"
"v_cvt_pk_bf16_f32 v227, v234, v235\n"
"ds_write_b128 v156, v[224:227] offset:0\n"
"v_cvt_pk_bf16_f32 v240, v240, v241\n"
"v_cvt_pk_bf16_f32 v241, v242, v243\n"
"v_cvt_pk_bf16_f32 v242, v248, v249\n"
"v_cvt_pk_bf16_f32 v243, v250, v251\n"
"ds_write_b128 v157, v[240:243] offset:0\n"
"v_cvt_pk_bf16_f32 v88, v88, v89\n"
"v_cvt_pk_bf16_f32 v89, v90, v91\n"
"ds_write_b64 v158, v[88:89] offset:2048\n"
"ds_read_b128 v[40:43], v159 offset:0\n"
"ds_read_b128 v[44:47], v159 offset:1024\n"
"ds_read_b128 v[48:51], v159 offset:2048\n"
"s_waitcnt lgkmcnt(2)\n"
"buffer_store_dwordx4 v[40:43], v132, s[52:55], s84 offen offset:0\n"
"s_waitcnt lgkmcnt(1)\n"
"buffer_store_dwordx4 v[44:47], v132, s[52:55], s85 offen offset:0\n"
"s_waitcnt lgkmcnt(0)\n"
"buffer_store_dwordx4 v[48:51], v133, s[52:55], s86 offen offset:0\n"
"s_or_b32 s66, s60, s61\n"
"s_cbranch_scc1 Lnoep_%=\n"
"s_mov_b32 s66, 44564480\n"
"buffer_store_dword v219, off, s[36:39], s66 offset:0 sc1\n"
"Lnoep_%=:\n"
"s_branch Lend_%=\n"
"Lsb61_%=:\n"
"ds_read_b32 v191, v220 offset:1952\n"
"s_waitcnt lgkmcnt(0)\n"
"v_cmp_ne_u32 s[92:93], s75, v191\n"
"s_nop 1\n"
"s_cmp_lg_u64 s[92:93], 0\n"
"s_cbranch_scc0 Lsr61_%=\n"
"s_sub_u32 s77, s77, 1\n"
"s_cbranch_scc0 Lsb61_%=\n"
"s_mov_b32 s77, 0\n"
"s_branch Lsr61_%=\n"
"Lslow0_%=:\n"
"s_sleep 6\n"
"buffer_load_dword v184, v183, s[36:39], s78 offen offset:0 sc1\n"
"s_waitcnt vmcnt(0)\n"
"v_cmp_ne_u32 s[92:93], s75, v184\n"
"s_nop 1\n"
"s_cmp_lg_u64 s[92:93], 0\n"
"s_cbranch_scc0 Lback0_%=\n"
"s_sub_u32 s76, s76, 1\n"
"s_cbranch_scc0 Lslow0_%=\n"
"s_mov_b32 s76, 0\n"
"s_branch Lback0_%=\n"
"Lsb62_%=:\n"
"ds_read_b32 v171, v220 offset:1984\n"
"s_waitcnt lgkmcnt(0)\n"
"v_cmp_ne_u32 s[92:93], s75, v171\n"
"s_nop 1\n"
"s_cmp_lg_u64 s[92:93], 0\n"
"s_cbranch_scc0 Lsr62_%=\n"
"s_sub_u32 s77, s77, 1\n"
"s_cbranch_scc0 Lsb62_%=\n"
"s_mov_b32 s77, 0\n"
"s_branch Lsr62_%=\n"
"Lslow1_%=:\n"
"s_sleep 6\n"
"buffer_load_dword v131, v130, s[36:39], s99 offen offset:0 sc1\n"
"s_waitcnt vmcnt(0)\n"
"v_cmp_ne_u32 s[92:93], s75, v131\n"
"s_nop 1\n"
"s_cmp_lg_u64 s[92:93], 0\n"
"s_cbranch_scc0 Lback1_%=\n"
"s_sub_u32 s76, s76, 1\n"
"s_cbranch_scc0 Lslow1_%=\n"
"s_mov_b32 s76, 0\n"
"s_branch Lback1_%=\n"
"Lslow2_%=:\n"
"s_sleep 6\n"
"buffer_load_dword v132, v139, s[36:39], s30 offen offset:640 sc1\n"
"s_waitcnt vmcnt(0)\n"
"v_cmp_ne_u32 s[92:93], s75, v132\n"
"s_nop 1\n"
"s_cmp_lg_u64 s[92:93], 0\n"
"s_cbranch_scc0 Lback2_%=\n"
"s_sub_u32 s76, s76, 1\n"
"s_cbranch_scc0 Lslow2_%=\n"
"s_mov_b32 s76, 0\n"
"s_branch Lback2_%=\n"
"Lsb63_%=:\n"
"ds_read_b32 v133, v220 offset:2016\n"
"s_waitcnt lgkmcnt(0)\n"
"v_cmp_ne_u32 s[92:93], s75, v133\n"
"s_nop 1\n"
"s_cmp_lg_u64 s[92:93], 0\n"
"s_cbranch_scc0 Lsr63_%=\n"
"s_sub_u32 s77, s77, 1\n"
"s_cbranch_scc0 Lsb63_%=\n"
"s_mov_b32 s77, 0\n"
"s_branch Lsr63_%=\n"
"Lslow3_%=:\n"
"s_sleep 6\n"
"v_and_b32 v129, 896, v212\n"
"v_lshlrev_b32 v129, 5, v129\n"
"buffer_load_dword v129, v129, s[36:39], s28 offen offset:0 sc1\n"
"s_waitcnt vmcnt(0)\n"
"v_cmp_ne_u32 s[34:35], s75, v129\n"
"s_nop 1\n"
"s_cmp_lg_u64 s[34:35], 0\n"
"s_cbranch_scc0 Lback3_%=\n"
"s_sub_u32 s76, s76, 1\n"
"s_cbranch_scc0 Lslow3_%=\n"
"s_mov_b32 s76, 0\n"
"s_branch Lback3_%=\n"
"Lslow4_%=:\n"
"s_sleep 6\n"
"v_and_b32 v129, 896, v212\n"
"v_lshlrev_b32 v129, 5, v129\n"
"buffer_load_dword v129, v129, s[36:39], s28 offen offset:128 sc1\n"
"s_waitcnt vmcnt(0)\n"
"v_cmp_ne_u32 s[34:35], s75, v129\n"
"s_nop 1\n"
"s_cmp_lg_u64 s[34:35], 0\n"
"s_cbranch_scc0 Lback4_%=\n"
"s_sub_u32 s76, s76, 1\n"
"s_cbranch_scc0 Lslow4_%=\n"
"s_mov_b32 s76, 0\n"
"s_branch Lback4_%=\n"
"Lslow5_%=:\n"
"s_sleep 6\n"
"v_and_b32 v129, 896, v212\n"
"v_lshlrev_b32 v129, 5, v129\n"
"buffer_load_dword v129, v129, s[36:39], s28 offen offset:256 sc1\n"
"s_waitcnt vmcnt(0)\n"
"v_cmp_ne_u32 s[34:35], s75, v129\n"
"s_nop 1\n"
"s_cmp_lg_u64 s[34:35], 0\n"
"s_cbranch_scc0 Lback5_%=\n"
"s_sub_u32 s76, s76, 1\n"
"s_cbranch_scc0 Lslow5_%=\n"
"s_mov_b32 s76, 0\n"
"s_branch Lback5_%=\n"
"Lslow6_%=:\n"
"s_sleep 6\n"
"v_and_b32 v129, 896, v212\n"
"v_lshlrev_b32 v129, 5, v129\n"
"buffer_load_dword v129, v129, s[36:39], s28 offen offset:512 sc1\n"
"s_waitcnt vmcnt(0)\n"
"v_cmp_ne_u32 s[34:35], s75, v129\n"
"s_nop 1\n"
"s_cmp_lg_u64 s[34:35], 0\n"
"s_cbranch_scc0 Lback6_%=\n"
"s_sub_u32 s76, s76, 1\n"
"s_cbranch_scc0 Lslow6_%=\n"
"s_mov_b32 s76, 0\n"
"s_branch Lback6_%=\n"
"Lslow7_%=:\n"
"s_sleep 6\n"
"v_and_b32 v129, 896, v212\n"
"v_lshlrev_b32 v129, 5, v129\n"
"buffer_load_dword v129, v129, s[36:39], s28 offen offset:384 sc1\n"
"s_waitcnt vmcnt(0)\n"
"v_cmp_ne_u32 s[34:35], s75, v129\n"
"s_nop 1\n"
"s_cmp_lg_u64 s[34:35], 0\n"
"s_cbranch_scc0 Lback7_%=\n"
"s_sub_u32 s76, s76, 1\n"
"s_cbranch_scc0 Lslow7_%=\n"
"s_mov_b32 s76, 0\n"
"s_branch Lback7_%=\n"
"Lsb0_%=:\n"
"ds_read_b32 v221, v220 offset:0\n"
"s_waitcnt lgkmcnt(0)\n"
"v_cmp_ne_u32 vcc, s75, v221\n"
"s_nop 1\n"
"s_cbranch_vccz Lsr0_%=\n"
"s_sub_u32 s77, s77, 1\n"
"s_cbranch_scc0 Lsb0_%=\n"
"s_mov_b32 s77, 0\n"
"s_branch Lsr0_%=\n"
"Lsb1_%=:\n"
"ds_read_b32 v221, v220 offset:32\n"
"s_waitcnt lgkmcnt(0)\n"
"v_cmp_ne_u32 vcc, s75, v221\n"
"s_nop 1\n"
"s_cbranch_vccz Lsr1_%=\n"
"s_sub_u32 s77, s77, 1\n"
"s_cbranch_scc0 Lsb1_%=\n"
"s_mov_b32 s77, 0\n"
"s_branch Lsr1_%=\n"
"Lsb2_%=:\n"
"ds_read_b32 v221, v220 offset:64\n"
"s_waitcnt lgkmcnt(0)\n"
"v_cmp_ne_u32 vcc, s75, v221\n"
"s_nop 1\n"
"s_cbranch_vccz Lsr2_%=\n"
"s_sub_u32 s77, s77, 1\n"
"s_cbranch_scc0 Lsb2_%=\n"
"s_mov_b32 s77, 0\n"
"s_branch Lsr2_%=\n"
"Lsb3_%=:\n"
"ds_read_b32 v221, v220 offset:96\n"
"s_waitcnt lgkmcnt(0)\n"
"v_cmp_ne_u32 vcc, s75, v221\n"
"s_nop 1\n"
"s_cbranch_vccz Lsr3_%=\n"
"s_sub_u32 s77, s77, 1\n"
"s_cbranch_scc0 Lsb3_%=\n"
"s_mov_b32 s77, 0\n"
"s_branch Lsr3_%=\n"
"Lsb4_%=:\n"
"ds_read_b32 v221, v220 offset:128\n"
"s_waitcnt lgkmcnt(0)\n"
"v_cmp_ne_u32 vcc, s75, v221\n"
"s_nop 1\n"
"s_cbranch_vccz Lsr4_%=\n"
"s_sub_u32 s77, s77, 1\n"
"s_cbranch_scc0 Lsb4_%=\n"
"s_mov_b32 s77, 0\n"
"s_branch Lsr4_%=\n"
"Lsb5_%=:\n"
"ds_read_b32 v221, v220 offset:160\n"
"s_waitcnt lgkmcnt(0)\n"
"v_cmp_ne_u32 vcc, s75, v221\n"
"s_nop 1\n"
"s_cbranch_vccz Lsr5_%=\n"
"s_sub_u32 s77, s77, 1\n"
"s_cbranch_scc0 Lsb5_%=\n"
"s_mov_b32 s77, 0\n"
"s_branch Lsr5_%=\n"
"Lsb6_%=:\n"
"ds_read_b32 v221, v220 offset:192\n"
"s_waitcnt lgkmcnt(0)\n"
"v_cmp_ne_u32 vcc, s75, v221\n"
"s_nop 1\n"
"s_cbranch_vccz Lsr6_%=\n"
"s_sub_u32 s77, s77, 1\n"
"s_cbranch_scc0 Lsb6_%=\n"
"s_mov_b32 s77, 0\n"
"s_branch Lsr6_%=\n"
"Lsb7_%=:\n"
"ds_read_b32 v221, v220 offset:224\n"
"s_waitcnt lgkmcnt(0)\n"
"v_cmp_ne_u32 vcc, s75, v221\n"
"s_nop 1\n"
"s_cbranch_vccz Lsr7_%=\n"
"s_sub_u32 s77, s77, 1\n"
"s_cbranch_scc0 Lsb7_%=\n"
"s_mov_b32 s77, 0\n"
"s_branch Lsr7_%=\n"
"Lsb8_%=:\n"
"ds_read_b32 v221, v220 offset:256\n"
"s_waitcnt lgkmcnt(0)\n"
"v_cmp_ne_u32 vcc, s75, v221\n"
"s_nop 1\n"
"s_cbranch_vccz Lsr8_%=\n"
"s_sub_u32 s77, s77, 1\n"
"s_cbranch_scc0 Lsb8_%=\n"
"s_mov_b32 s77, 0\n"
"s_branch Lsr8_%=\n"
"Lsb9_%=:\n"
"ds_read_b32 v221, v220 offset:288\n"
"s_waitcnt lgkmcnt(0)\n"
"v_cmp_ne_u32 vcc, s75, v221\n"
"s_nop 1\n"
"s_cbranch_vccz Lsr9_%=\n"
"s_sub_u32 s77, s77, 1\n"
"s_cbranch_scc0 Lsb9_%=\n"
"s_mov_b32 s77, 0\n"
"s_branch Lsr9_%=\n"
"Lsb10_%=:\n"
"ds_read_b32 v221, v220 offset:320\n"
"s_waitcnt lgkmcnt(0)\n"
"v_cmp_ne_u32 vcc, s75, v221\n"
"s_nop 1\n"
"s_cbranch_vccz Lsr10_%=\n"
"s_sub_u32 s77, s77, 1\n"
"s_cbranch_scc0 Lsb10_%=\n"
"s_mov_b32 s77, 0\n"
"s_branch Lsr10_%=\n"
"Lsb11_%=:\n"
"ds_read_b32 v221, v220 offset:352\n"
"s_waitcnt lgkmcnt(0)\n"
"v_cmp_ne_u32 vcc, s75, v221\n"
"s_nop 1\n"
"s_cbranch_vccz Lsr11_%=\n"
"s_sub_u32 s77, s77, 1\n"
"s_cbranch_scc0 Lsb11_%=\n"
"s_mov_b32 s77, 0\n"
"s_branch Lsr11_%=\n"
"Lsb12_%=:\n"
"ds_read_b32 v221, v220 offset:384\n"
"s_waitcnt lgkmcnt(0)\n"
"v_cmp_ne_u32 vcc, s75, v221\n"
"s_nop 1\n"
"s_cbranch_vccz Lsr12_%=\n"
"s_sub_u32 s77, s77, 1\n"
"s_cbranch_scc0 Lsb12_%=\n"
"s_mov_b32 s77, 0\n"
"s_branch Lsr12_%=\n"
"Lsb13_%=:\n"
"ds_read_b32 v221, v220 offset:416\n"
"s_waitcnt lgkmcnt(0)\n"
"v_cmp_ne_u32 vcc, s75, v221\n"
"s_nop 1\n"
"s_cbranch_vccz Lsr13_%=\n"
"s_sub_u32 s77, s77, 1\n"
"s_cbranch_scc0 Lsb13_%=\n"
"s_mov_b32 s77, 0\n"
"s_branch Lsr13_%=\n"
"Lsb14_%=:\n"
"ds_read_b32 v221, v220 offset:448\n"
"s_waitcnt lgkmcnt(0)\n"
"v_cmp_ne_u32 vcc, s75, v221\n"
"s_nop 1\n"
"s_cbranch_vccz Lsr14_%=\n"
"s_sub_u32 s77, s77, 1\n"
"s_cbranch_scc0 Lsb14_%=\n"
"s_mov_b32 s77, 0\n"
"s_branch Lsr14_%=\n"
"Lsb15_%=:\n"
"ds_read_b32 v221, v220 offset:480\n"
"s_waitcnt lgkmcnt(0)\n"
"v_cmp_ne_u32 vcc, s75, v221\n"
"s_nop 1\n"
"s_cbranch_vccz Lsr15_%=\n"
"s_sub_u32 s77, s77, 1\n"
"s_cbranch_scc0 Lsb15_%=\n"
"s_mov_b32 s77, 0\n"
"s_branch Lsr15_%=\n"
"Lsb16_%=:\n"
"ds_read_b32 v221, v220 offset:512\n"
"s_waitcnt lgkmcnt(0)\n"
"v_cmp_ne_u32 vcc, s75, v221\n"
"s_nop 1\n"
"s_cbranch_vccz Lsr16_%=\n"
"s_sub_u32 s77, s77, 1\n"
"s_cbranch_scc0 Lsb16_%=\n"
"s_mov_b32 s77, 0\n"
"s_branch Lsr16_%=\n"
"Lsb17_%=:\n"
"ds_read_b32 v221, v220 offset:544\n"
"s_waitcnt lgkmcnt(0)\n"
"v_cmp_ne_u32 vcc, s75, v221\n"
"s_nop 1\n"
"s_cbranch_vccz Lsr17_%=\n"
"s_sub_u32 s77, s77, 1\n"
"s_cbranch_scc0 Lsb17_%=\n"
"s_mov_b32 s77, 0\n"
"s_branch Lsr17_%=\n"
"Lsb18_%=:\n"
"ds_read_b32 v221, v220 offset:576\n"
"s_waitcnt lgkmcnt(0)\n"
"v_cmp_ne_u32 vcc, s75, v221\n"
"s_nop 1\n"
"s_cbranch_vccz Lsr18_%=\n"
"s_sub_u32 s77, s77, 1\n"
"s_cbranch_scc0 Lsb18_%=\n"
"s_mov_b32 s77, 0\n"
"s_branch Lsr18_%=\n"
"Lsb19_%=:\n"
"ds_read_b32 v221, v220 offset:608\n"
"s_waitcnt lgkmcnt(0)\n"
"v_cmp_ne_u32 vcc, s75, v221\n"
"s_nop 1\n"
"s_cbranch_vccz Lsr19_%=\n"
"s_sub_u32 s77, s77, 1\n"
"s_cbranch_scc0 Lsb19_%=\n"
"s_mov_b32 s77, 0\n"
"s_branch Lsr19_%=\n"
"Lsb20_%=:\n"
"ds_read_b32 v221, v220 offset:640\n"
"s_waitcnt lgkmcnt(0)\n"
"v_cmp_ne_u32 vcc, s75, v221\n"
"s_nop 1\n"
"s_cbranch_vccz Lsr20_%=\n"
"s_sub_u32 s77, s77, 1\n"
"s_cbranch_scc0 Lsb20_%=\n"
"s_mov_b32 s77, 0\n"
"s_branch Lsr20_%=\n"
"Lsb21_%=:\n"
"ds_read_b32 v221, v220 offset:672\n"
"s_waitcnt lgkmcnt(0)\n"
"v_cmp_ne_u32 vcc, s75, v221\n"
"s_nop 1\n"
"s_cbranch_vccz Lsr21_%=\n"
"s_sub_u32 s77, s77, 1\n"
"s_cbranch_scc0 Lsb21_%=\n"
"s_mov_b32 s77, 0\n"
"s_branch Lsr21_%=\n"
"Lsb22_%=:\n"
"ds_read_b32 v221, v220 offset:704\n"
"s_waitcnt lgkmcnt(0)\n"
"v_cmp_ne_u32 vcc, s75, v221\n"
"s_nop 1\n"
"s_cbranch_vccz Lsr22_%=\n"
"s_sub_u32 s77, s77, 1\n"
"s_cbranch_scc0 Lsb22_%=\n"
"s_mov_b32 s77, 0\n"
"s_branch Lsr22_%=\n"
"Lsb23_%=:\n"
"ds_read_b32 v221, v220 offset:736\n"
"s_waitcnt lgkmcnt(0)\n"
"v_cmp_ne_u32 vcc, s75, v221\n"
"s_nop 1\n"
"s_cbranch_vccz Lsr23_%=\n"
"s_sub_u32 s77, s77, 1\n"
"s_cbranch_scc0 Lsb23_%=\n"
"s_mov_b32 s77, 0\n"
"s_branch Lsr23_%=\n"
"Lsb24_%=:\n"
"ds_read_b32 v221, v220 offset:768\n"
"s_waitcnt lgkmcnt(0)\n"
"v_cmp_ne_u32 vcc, s75, v221\n"
"s_nop 1\n"
"s_cbranch_vccz Lsr24_%=\n"
"s_sub_u32 s77, s77, 1\n"
"s_cbranch_scc0 Lsb24_%=\n"
"s_mov_b32 s77, 0\n"
"s_branch Lsr24_%=\n"
"Lsb25_%=:\n"
"ds_read_b32 v221, v220 offset:800\n"
"s_waitcnt lgkmcnt(0)\n"
"v_cmp_ne_u32 vcc, s75, v221\n"
"s_nop 1\n"
"s_cbranch_vccz Lsr25_%=\n"
"s_sub_u32 s77, s77, 1\n"
"s_cbranch_scc0 Lsb25_%=\n"
"s_mov_b32 s77, 0\n"
"s_branch Lsr25_%=\n"
"Lsb26_%=:\n"
"ds_read_b32 v221, v220 offset:832\n"
"s_waitcnt lgkmcnt(0)\n"
"v_cmp_ne_u32 vcc, s75, v221\n"
"s_nop 1\n"
"s_cbranch_vccz Lsr26_%=\n"
"s_sub_u32 s77, s77, 1\n"
"s_cbranch_scc0 Lsb26_%=\n"
"s_mov_b32 s77, 0\n"
"s_branch Lsr26_%=\n"
"Lsb27_%=:\n"
"ds_read_b32 v221, v220 offset:864\n"
"s_waitcnt lgkmcnt(0)\n"
"v_cmp_ne_u32 vcc, s75, v221\n"
"s_nop 1\n"
"s_cbranch_vccz Lsr27_%=\n"
"s_sub_u32 s77, s77, 1\n"
"s_cbranch_scc0 Lsb27_%=\n"
"s_mov_b32 s77, 0\n"
"s_branch Lsr27_%=\n"
"Lsb28_%=:\n"
"ds_read_b32 v221, v220 offset:896\n"
"s_waitcnt lgkmcnt(0)\n"
"v_cmp_ne_u32 vcc, s75, v221\n"
"s_nop 1\n"
"s_cbranch_vccz Lsr28_%=\n"
"s_sub_u32 s77, s77, 1\n"
"s_cbranch_scc0 Lsb28_%=\n"
"s_mov_b32 s77, 0\n"
"s_branch Lsr28_%=\n"
"Lsb29_%=:\n"
"ds_read_b32 v221, v220 offset:928\n"
"s_waitcnt lgkmcnt(0)\n"
"v_cmp_ne_u32 vcc, s75, v221\n"
"s_nop 1\n"
"s_cbranch_vccz Lsr29_%=\n"
"s_sub_u32 s77, s77, 1\n"
"s_cbranch_scc0 Lsb29_%=\n"
"s_mov_b32 s77, 0\n"
"s_branch Lsr29_%=\n"
"Lsb30_%=:\n"
"ds_read_b32 v221, v220 offset:960\n"
"s_waitcnt lgkmcnt(0)\n"
"v_cmp_ne_u32 vcc, s75, v221\n"
"s_nop 1\n"
"s_cbranch_vccz Lsr30_%=\n"
"s_sub_u32 s77, s77, 1\n"
"s_cbranch_scc0 Lsb30_%=\n"
"s_mov_b32 s77, 0\n"
"s_branch Lsr30_%=\n"
"Lsb31_%=:\n"
"ds_read_b32 v221, v220 offset:992\n"
"s_waitcnt lgkmcnt(0)\n"
"v_cmp_ne_u32 vcc, s75, v221\n"
"s_nop 1\n"
"s_cbranch_vccz Lsr31_%=\n"
"s_sub_u32 s77, s77, 1\n"
"s_cbranch_scc0 Lsb31_%=\n"
"s_mov_b32 s77, 0\n"
"s_branch Lsr31_%=\n"
"Lsb32_%=:\n"
"ds_read_b32 v221, v220 offset:1024\n"
"s_waitcnt lgkmcnt(0)\n"
"v_cmp_ne_u32 vcc, s75, v221\n"
"s_nop 1\n"
"s_cbranch_vccz Lsr32_%=\n"
"s_sub_u32 s77, s77, 1\n"
"s_cbranch_scc0 Lsb32_%=\n"
"s_mov_b32 s77, 0\n"
"s_branch Lsr32_%=\n"
"Lsb33_%=:\n"
"ds_read_b32 v221, v220 offset:1056\n"
"s_waitcnt lgkmcnt(0)\n"
"v_cmp_ne_u32 vcc, s75, v221\n"
"s_nop 1\n"
"s_cbranch_vccz Lsr33_%=\n"
"s_sub_u32 s77, s77, 1\n"
"s_cbranch_scc0 Lsb33_%=\n"
"s_mov_b32 s77, 0\n"
"s_branch Lsr33_%=\n"
"Lsb34_%=:\n"
"ds_read_b32 v221, v220 offset:1088\n"
"s_waitcnt lgkmcnt(0)\n"
"v_cmp_ne_u32 vcc, s75, v221\n"
"s_nop 1\n"
"s_cbranch_vccz Lsr34_%=\n"
"s_sub_u32 s77, s77, 1\n"
"s_cbranch_scc0 Lsb34_%=\n"
"s_mov_b32 s77, 0\n"
"s_branch Lsr34_%=\n"
"Lsb35_%=:\n"
"ds_read_b32 v221, v220 offset:1120\n"
"s_waitcnt lgkmcnt(0)\n"
"v_cmp_ne_u32 vcc, s75, v221\n"
"s_nop 1\n"
"s_cbranch_vccz Lsr35_%=\n"
"s_sub_u32 s77, s77, 1\n"
"s_cbranch_scc0 Lsb35_%=\n"
"s_mov_b32 s77, 0\n"
"s_branch Lsr35_%=\n"
"Lsb36_%=:\n"
"ds_read_b32 v221, v220 offset:1152\n"
"s_waitcnt lgkmcnt(0)\n"
"v_cmp_ne_u32 vcc, s75, v221\n"
"s_nop 1\n"
"s_cbranch_vccz Lsr36_%=\n"
"s_sub_u32 s77, s77, 1\n"
"s_cbranch_scc0 Lsb36_%=\n"
"s_mov_b32 s77, 0\n"
"s_branch Lsr36_%=\n"
"Lsb37_%=:\n"
"ds_read_b32 v221, v220 offset:1184\n"
"s_waitcnt lgkmcnt(0)\n"
"v_cmp_ne_u32 vcc, s75, v221\n"
"s_nop 1\n"
"s_cbranch_vccz Lsr37_%=\n"
"s_sub_u32 s77, s77, 1\n"
"s_cbranch_scc0 Lsb37_%=\n"
"s_mov_b32 s77, 0\n"
"s_branch Lsr37_%=\n"
"Lend_%=:\n"
    :: [ka]"s"(__builtin_amdgcn_kernarg_segment_ptr()),[bid]"s"(blockIdx.x),[wid]"s"(wid),[lds]"s"((unsigned)(size_t)smem)
    : "memory","vcc","scc","s5","s6","s7","s8","s9","s10","s11","s12","s13","s14","s15","s16","s17","s18","s19","s20","s21","s22","s23","s24","s25","s26","s27","s28","s29","s30","s31","s34","s35","s36","s37","s38","s39","s40","s41","s42","s43","s44","s45","s46","s47","s48","s49","s50","s51","s52","s53","s54","s55","s56","s57","s58","s59","s60","s61","s62","s63","s64","s65","s66","s67","s68","s69","s70","s71","s72","s73","s74","s75","s76","s77","s78","s79","s80","s81","s82","s83","s84","s85","s86","s87","s88","s89","s90","s91","s92","s93","s94","s95","s96","s97","s98","s99","v0","v1","v2","v3","v4","v5","v6","v7","v8","v9","v10","v11","v12","v13","v14","v15","v16","v17","v18","v19","v20","v21","v22","v23","v24","v25","v26","v27","v28","v29","v30","v31","v32","v33","v34","v35","v36","v37","v38","v39","v40","v41","v42","v43","v44","v45","v46","v47","v48","v49","v50","v51","v52","v53","v54","v55","v56","v57","v58","v59","v60","v61","v62","v63","v64","v65","v66","v67","v68","v69","v70","v71","v72","v73","v74","v75","v76","v77","v78","v79","v80","v81","v82","v83","v84","v85","v86","v87","v88","v89","v90","v91","v92","v93","v94","v95","v96","v97","v98","v99","v100","v101","v102","v103","v104","v105","v106","v107","v108","v109","v110","v111","v112","v113","v114","v115","v116","v117","v118","v119","v120","v121","v122","v123","v124","v125","v126","v127","v128","v129","v130","v131","v132","v133","v134","v135","v136","v137","v138","v139","v140","v141","v142","v143","v144","v145","v146","v147","v148","v149","v150","v151","v152","v153","v154","v155","v156","v157","v158","v159","v160","v161","v162","v163","v164","v165","v166","v167","v168","v169","v170","v171","v172","v173","v174","v175","v176","v177","v178","v179","v180","v181","v182","v183","v184","v185","v186","v187","v188","v189","v190","v191","v192","v193","v194","v195","v196","v197","v198","v199","v200","v201","v202","v203","v204","v205","v206","v207","v208","v209","v210","v211","v212","v213","v214","v215","v216","v217","v218","v219","v220","v221","v222","v223","v224","v225","v226","v227","v228","v229","v230","v231","v232","v233","v234","v235","v236","v237","v238","v239","v240","v241","v242","v243","v244","v245","v246","v247","v248","v249","v250","v251","v252","v253","v254","v255","a0","a1","a2","a3","a4","a5","a6","a7","a8","a9","a10","a11","a12","a13","a14","a15","a16","a17","a18","a19","a20","a21","a22","a23","a24","a25","a26","a27","a28","a29","a30","a31","a32","a33","a34","a35","a36","a37","a38","a39","a40","a41","a42","a43","a44","a45","a46","a47","a48","a49","a50","a51","a52","a53","a54","a55","a56","a57","a58","a59","a60","a61","a62","a63","a64","a65","a66","a67","a68","a69","a70","a71","a72","a73","a74","a75","a76","a77","a78","a79","a80","a81","a82","a83","a84","a85","a86","a87","a88","a89","a90","a91","a92","a93","a94","a95","a96","a97","a98","a99","a100","a101","a102","a103","a104","a105","a106","a107","a108","a109","a110","a111","a112","a113","a114","a115","a116","a117","a118","a119","a120","a121","a122","a123","a124","a125","a126","a127","a128","a129","a130","a131","a132","a133","a134","a135","a136","a137","a138","a139","a140","a141","a142","a143","a144","a145","a146","a147","a148","a149","a150","a151","a152","a153","a154","a155","a156","a157","a158","a159","a160","a161","a162","a163","a164","a165","a166","a167","a168","a169","a170","a171","a172","a173","a174","a175","a176","a177","a178","a179","a180","a181","a182","a183","a184","a185","a186","a187","a188","a189","a190","a191","a192","a193","a194","a195","a196","a197","a198","a199","a200","a201","a202","a203","a204","a205","a206","a207","a208","a209","a210","a211","a212","a213","a214","a215","a216","a217","a218","a219","a220","a221","a222","a223","a224","a225","a226","a227","a228","a229","a230","a231","a232","a233","a234","a235","a236","a237","a238","a239","a240","a241","a242","a243","a244","a245","a246","a247","a248","a249","a250","a251","a252","a253","a254","a255");
}


extern "C" void kernel_launch(void* const* d_in, const int* in_sizes, int n_in, void* d_out, int out_size, void* d_ws, size_t ws_size, hipStream_t stream){

  k_main<<<256,256,0,stream>>>(d_in[0],d_in[1],d_in[2],d_in[3],d_in[5],d_in[6],d_in[7],d_ws,d_out);

}
```
